```python
import torch

def _softmax(x, axis=-1):
    return torch.softmax(x, dim=axis)
def _gelu(x):
    return torch.nn.functional.gelu(x, approximate="tanh")
def _silu(x):
    return torch.nn.functional.silu(x)
def _sigmoid(x):
    return torch.sigmoid(x)
def _layernorm(x, axis=-1, eps=1e-5):
    m = x.mean(dim=axis, keepdim=True)
    v = x.var(dim=axis, keepdim=True, unbiased=False)
    return (x - m) / torch.sqrt(v + eps)
def _scatter_add(tgt, idx, src, axis=0):
    return tgt.clone().index_add_(axis, idx, src)
def _segment_sum(src, seg, n):
    out = torch.zeros((n,) + src.shape[1:], dtype=src.dtype)
    return out.index_add_(0, seg, src)
def _pad(x, pw):
    flat = tuple(v for pair in reversed(pw) for v in pair)
    return torch.nn.functional.pad(x, flat)

kernel_name = 'interleave_layers__gqa_block_cached__glu_gated_swiglu__decode'

def setup_inputs():

    _w = (lambda a: (lambda t: t.to(torch.bfloat16) if t.is_floating_point() else t)(torch.as_tensor(a)))
    def _A_setup():
        B, Sq, D = 1, 1, 512
        Hq, Hkv, Sk, Dh = 32, 8, 65536, 128
        assert Hq % Hkv == 0, (Hq, Hkv)
        return {
            "x": _w((lambda *s: torch.randn(*s, dtype=torch.bfloat16) * 0.05)(B, Sq, D)),
            "k_cache": _w((lambda *s: torch.randn(*s, dtype=torch.bfloat16) * 0.8)(B, Hkv, Sk, Dh)),
            "v_cache": _w((lambda *s: torch.randn(*s, dtype=torch.bfloat16) * 1.6)(B, Hkv, Sk, Dh)),
            "wq": _w((lambda *s: torch.randn(*s, dtype=torch.bfloat16) * 0.8)(D, Hq * Dh) * (D ** -0.5)),
            "wo": _w((lambda *s: torch.randn(*s, dtype=torch.bfloat16) * 1.6)(Hq * Dh, D) * ((Hq * Dh) ** -0.5)),
        }
    _A_in = _A_setup()

    def _B_setup():
        B, S, D, F = 1, 1, 512, 1792
        return {
            "x": _w((lambda *s: torch.randn(*s, dtype=torch.bfloat16) * 0.05)(B, S, D)),
            "w1": _w((lambda *s: torch.randn(*s, dtype=torch.bfloat16) * 0.4)(D, F) * (2.0 / D) ** 0.5),
            "w3": _w((lambda *s: torch.randn(*s, dtype=torch.bfloat16) * 0.4)(D, F) * (2.0 / D) ** 0.5),
            "w2": _w((lambda *s: torch.randn(*s, dtype=torch.bfloat16) * 0.4)(F, D) * (2.0 / F) ** 0.5),
        }
    _B_in = _B_setup()

    return {
        "A_x": _A_in["x"],
        "A_k_cache": _A_in["k_cache"],
        "A_v_cache": _A_in["v_cache"],
        "A_wq": _A_in["wq"],
        "A_wo": _A_in["wo"],
        "B_x": _B_in["x"],
        "B_w1": _B_in["w1"],
        "B_w3": _B_in["w3"],
        "B_w2": _B_in["w2"],
    }

def reference(A_x, A_k_cache, A_v_cache, A_wq, A_wo, B_x, B_w1, B_w3, B_w2):
    def _A_ref(x, k_cache, v_cache, wq, wo):
        B, Sq, D = x.shape
        Hq, Dh = 32, 128
        Hkv = k_cache.shape[1]
        rep = Hq // Hkv
        q = (lambda x, *d: x.permute(*d))((x @ wq).reshape(B, Sq, Hq, Dh), 0, 2, 1, 3)
        k = (lambda x, n, axis: torch.repeat_interleave(x, n, dim=axis))(k_cache, rep, axis=1)
        v = (lambda x, n, axis: torch.repeat_interleave(x, n, dim=axis))(v_cache, rep, axis=1)
        scores = torch.einsum("bhqd,bhkd->bhqk", q, k) * (Dh ** -0.5)
        p = _softmax(scores, axis=-1)
        o = torch.einsum("bhqk,bhkd->bhqd", p, v)
        return (lambda x, *d: x.permute(*d))(o, 0, 2, 1, 3).reshape(B, Sq, Hq * Dh) @ wo

    def _B_ref(x, w1, w3, w2):
        gate = torch.einsum("bsd,df->bsf", x, w1)
        up = torch.einsum("bsd,df->bsf", x, w3)
        h = _silu(gate) * up
        out = torch.einsum("bsf,fd->bsd", h, w2)
        return out

    x = A_x
    x = x + _A_ref(_layernorm(x, axis=-1), A_k_cache, A_v_cache, A_wq, A_wo)
    x = x + _B_ref(_layernorm(x, axis=-1), B_w1, B_w3, B_w2)
    x = x + _A_ref(_layernorm(x, axis=-1), A_k_cache, A_v_cache, A_wq, A_wo)
    x = x + _B_ref(_layernorm(x, axis=-1), B_w1, B_w3, B_w2)
    return x
```

```cpp
#include <hip/hip_runtime.h>
#include <hip/hip_bf16.h>
#include <stdint.h>

typedef unsigned short bf16_t;
__device__ __forceinline__ float bf2f(bf16_t v) { return __uint_as_float(((unsigned)v) << 16); }
__device__ __forceinline__ bf16_t f2bf(float f) { return __builtin_bit_cast(bf16_t, (__bf16)f); }

constexpr int D = 512, HQ = 32, HKV = 8, SK = 65536, DH = 128, F = 1792;
constexpr int NSPLIT = 128;

__global__ void k_ln_gemv(const float* x, const bf16_t* W, int N, float* y) {
    __shared__ float xs[D];
    __shared__ float red[2];
    int t = threadIdx.x;
    for (int i = t; i < D; i += blockDim.x) xs[i] = x[i];
    __syncthreads();
    if (t == 0) {
        float m = 0; for (int i = 0; i < D; ++i) m += xs[i]; m /= D;
        float v = 0; for (int i = 0; i < D; ++i) { float d = xs[i] - m; v += d * d; } v /= D;
        red[0] = m; red[1] = 1.0f / sqrtf(v + 1e-5f);
    }
    __syncthreads();
    float m = red[0], r = red[1];
    int n = blockIdx.x * blockDim.x + t;
    if (n >= N) return;
    float acc = 0;
    for (int k = 0; k < D; ++k) acc += (xs[k] - m) * r * bf2f(W[(size_t)k * N + n]);
    y[n] = acc;
}
__global__ void k_init(const bf16_t* x, float* xf) { int t = threadIdx.x + blockIdx.x * blockDim.x; if (t < D) xf[t] = bf2f(x[t]); }
__global__ void k_fin(const float* xf, bf16_t* o) { int t = threadIdx.x + blockIdx.x * blockDim.x; if (t < D) o[t] = f2bf(xf[t]); }

__global__ void k_attn(const float* q, const bf16_t* K, const bf16_t* V, float* pm, float* pl, float* po) {
    int s = blockIdx.x, h = blockIdx.y, t = threadIdx.x;
    int kvh = h / (HQ / HKV);
    constexpr int KPS = SK / NSPLIT;
    __shared__ float qs[DH];
    __shared__ float sc[KPS];
    __shared__ float red[128];
    qs[t] = q[h * DH + t] * 0.08838834764831845f;
    __syncthreads();
    const bf16_t* Kb = K + ((size_t)kvh * SK + (size_t)s * KPS) * DH;
    const bf16_t* Vb = V + ((size_t)kvh * SK + (size_t)s * KPS) * DH;
    float mx = -INFINITY;
    for (int j = t; j < KPS; j += 128) {
        float a = 0;
        for (int d = 0; d < DH; ++d) a += qs[d] * bf2f(Kb[(size_t)j * DH + d]);
        sc[j] = a; mx = fmaxf(mx, a);
    }
    red[t] = mx; __syncthreads();
    for (int o = 64; o > 0; o >>= 1) { if (t < o) red[t] = fmaxf(red[t], red[t + o]); __syncthreads(); }
    mx = red[0]; __syncthreads();
    float l = 0;
    for (int j = t; j < KPS; j += 128) { float p = __expf(sc[j] - mx); sc[j] = p; l += p; }
    red[t] = l; __syncthreads();
    for (int o = 64; o > 0; o >>= 1) { if (t < o) red[t] += red[t + o]; __syncthreads(); }
    l = red[0];
    float acc = 0;
    for (int j = 0; j < KPS; ++j) acc += sc[j] * bf2f(Vb[(size_t)j * DH + t]);
    po[((size_t)h * NSPLIT + s) * DH + t] = acc;
    if (t == 0) { pm[h * NSPLIT + s] = mx; pl[h * NSPLIT + s] = l; }
}
__global__ void k_comb(const float* pm, const float* pl, const float* po, float* o) {
    int h = blockIdx.x, t = threadIdx.x;
    float M = -INFINITY;
    for (int s = 0; s < NSPLIT; ++s) M = fmaxf(M, pm[h * NSPLIT + s]);
    float L = 0, a = 0;
    for (int s = 0; s < NSPLIT; ++s) { float w = __expf(pm[h * NSPLIT + s] - M); L += w * pl[h * NSPLIT + s]; a += w * po[((size_t)h * NSPLIT + s) * DH + t]; }
    o[h * DH + t] = a / L;
}
__global__ void k_gemv_res(const float* v, int Kd, const bf16_t* W, float* x) {
    __shared__ float red[256];
    int n = blockIdx.x, t = threadIdx.x;
    float acc = 0;
    for (int k = t; k < Kd; k += 256) acc += v[k] * bf2f(W[(size_t)k * D + n]);
    red[t] = acc; __syncthreads();
    for (int o = 128; o > 0; o >>= 1) { if (t < o) red[t] += red[t + o]; __syncthreads(); }
    if (t == 0) x[n] += red[0];
}
__global__ void k_swiglu(const float* g, const float* u, float* h) {
    int t = threadIdx.x + blockIdx.x * blockDim.x;
    if (t < F) { float a = g[t]; h[t] = a / (1.0f + __expf(-a)) * u[t]; }
}

extern "C" void kernel_launch(void* const* d_in, const int* in_sizes, int n_in,
                              void* d_out, int out_size, void* d_ws, size_t ws_size,
                              hipStream_t stream) {
    const bf16_t* x = (const bf16_t*)d_in[0];
    const bf16_t* K = (const bf16_t*)d_in[1];
    const bf16_t* V = (const bf16_t*)d_in[2];
    const bf16_t* wq = (const bf16_t*)d_in[3];
    const bf16_t* wo = (const bf16_t*)d_in[4];
    const bf16_t* w1 = (const bf16_t*)d_in[6];
    const bf16_t* w3 = (const bf16_t*)d_in[7];
    const bf16_t* w2 = (const bf16_t*)d_in[8];
    float* ws = (float*)d_ws;
    float* xf = ws; float* q = xf + 512; float* o = q + 4096; float* g = o + 4096; float* u = g + 2048; float* h = u + 2048;
    float* pm = h + 2048; float* pl = pm + HQ * NSPLIT; float* po = pl + HQ * NSPLIT;
    k_init<<<2, 256, 0, stream>>>(x, xf);
    for (int it = 0; it < 2; ++it) {
        k_ln_gemv<<<4096 / 64, 64, 0, stream>>>(xf, wq, 4096, q);
        k_attn<<<dim3(NSPLIT, HQ), 128, 0, stream>>>(q, K, V, pm, pl, po);
        k_comb<<<HQ, 128, 0, stream>>>(pm, pl, po, o);
        k_gemv_res<<<D, 256, 0, stream>>>(o, 4096, wo, xf);
        k_ln_gemv<<<F / 64, 64, 0, stream>>>(xf, w1, F, g);
        k_ln_gemv<<<F / 64, 64, 0, stream>>>(xf, w3, F, u);
        k_swiglu<<<F / 256, 256, 0, stream>>>(g, u, h);
        k_gemv_res<<<D, 256, 0, stream>>>(h, F, w2, xf);
    }
    k_fin<<<2, 256, 0, stream>>>(xf, (bf16_t*)d_out);
}
```

```cpp
#include <hip/hip_runtime.h>
#include <stdint.h>

constexpr unsigned LDS_BYTES = 163584u;

__global__ void __launch_bounds__(256, 1)
mega(const void* x, const void* kc, const void* vc, const void* wq, const void* wo, const void* w1, const void* w3, const void* w2, void* out, void* ws) {
  unsigned tid = threadIdx.x;
  asm volatile(
    "v_mov_b32 v4, %[tid]\n\t"
    "v_lshrrev_b32 v5, 6, v4\n\t"
    "v_and_b32 v3, 63, v4\n\t"
    "v_readfirstlane_b32 s60, v5\n\t"
    "s_mov_b32 s59, %[bid]\n\t"
    "s_and_b32 s72, s59, 7\n\t"
    "s_lshr_b32 s73, s59, 3\n\t"
    "s_lshl_b32 s61, s72, 5\n\t"
    "s_add_u32 s61, s61, s73\n\t"
    "s_mov_b64 s[40:41], %[kc]\n\t"
    "s_and_b32 s41, s41, 0xffff\n\t"
    "s_mov_b32 s42, 0x8000000\n\t"
    "s_mov_b32 s43, 0x20000\n\t"
    "s_mov_b64 s[44:45], %[vc]\n\t"
    "s_and_b32 s45, s45, 0xffff\n\t"
    "s_mov_b32 s46, 0x8000000\n\t"
    "s_mov_b32 s47, 0x20000\n\t"
    "s_lshl_b32 s56, s59, 19\n\t"
    "s_mul_i32 s72, s60, 0x400\n\t"
    "s_add_u32 s56, s56, s72\n\t"
    "s_mul_i32 s34, s73, 0x10000\n\t"
    "s_bfe_u32 s80, s60, 0x10000\n\t"
    "s_lshl_b32 s80, s80, 13\n\t"
    "v_lshlrev_b32 v1, 4, v3\n\t"
    "v_bfe_u32 v5, v3, 2, 2\n\t"
    "v_lshlrev_b32 v0, 8, v5\n\t"
    "v_lshrrev_b32 v5, 4, v3\n\t"
    "v_lshl_add_u32 v0, v5, 6, v0\n\t"
    "v_and_b32 v5, 3, v3\n\t"
    "v_lshl_add_u32 v0, v5, 4, v0\n\t"
    "s_mul_i32 s72, s60, 0x6800\n\t"
    "s_add_u32 s72, s72, 0xdf00\n\t"
    "v_add_u32 v2, s72, v1\n\t"
    "s_add_u32 s32, s34, 0\n\t"
    "s_and_b32 s32, s32, 0x7ffff\n\t"
    "s_add_u32 s32, s32, s56\n\t"
    "s_add_u32 s57, s32, 0\n\t"
    "s_xor_b32 s57, s57, s80\n\t"
    "buffer_load_dwordx4 a[0:3], v0, s[40:43], s57 offen offset:0 nt\n\t"
    "s_add_u32 s57, s32, 0x1000\n\t"
    "s_xor_b32 s57, s57, s80\n\t"
    "buffer_load_dwordx4 a[4:7], v0, s[40:43], s57 offen offset:0 nt\n\t"
    "s_add_u32 s57, s32, 0x2000\n\t"
    "s_xor_b32 s57, s57, s80\n\t"
    "buffer_load_dwordx4 a[8:11], v0, s[40:43], s57 offen offset:0 nt\n\t"
    "s_add_u32 s57, s32, 0x3000\n\t"
    "s_xor_b32 s57, s57, s80\n\t"
    "buffer_load_dwordx4 a[12:15], v0, s[40:43], s57 offen offset:0 nt\n\t"
    "s_mov_b64 s[48:49], %[ws]\n\t"
    "s_and_b32 s49, s49, 0xffff\n\t"
    "s_mov_b32 s50, 0x270000\n\t"
    "s_mov_b32 s51, 0x20000\n\t"
    "s_and_b32 s72, s59, 7\n\t"
    "s_lshr_b32 s73, s59, 3\n\t"
    "s_lshl_b32 s61, s72, 5\n\t"
    "s_add_u32 s61, s61, s73\n\t"
    "s_lshl_b32 s70, s72, 3\n\t"
    "s_and_b32 s74, s73, 7\n\t"
    "s_add_u32 s70, s70, s74\n\t"
    "s_cmp_lt_u32 s73, 8\n\t"
    "s_cselect_b32 s71, 1, 0\n\t"
    "s_mul_i32 s69, s70, 0x7000\n\t"
    "s_add_u32 s69, s69, 0xb0000\n\t"
    "s_mov_b32 s64, 0x3e0293ee\n\t"
    "s_mov_b32 s65, 0xbe0293ee\n\t"
    "s_mov_b32 s66, 0x5040100\n\t"
    "s_mov_b32 s67, 0x7060302\n\t"
    "s_mov_b32 s68, 0x3f803f80\n\t"
    "s_mov_b32 s63, 0\n\t"
    "s_mov_b32 s62, 0\n\t"
    "s_mov_b64 s[36:37], 0xffff\n\t"
    "v_mov_b32 v36, -1\n\t"
    "v_mov_b32 v37, -1\n\t"
    "v_mov_b32 v38, -1\n\t"
    "v_mov_b32 v39, -1\n\t"
    "v_lshlrev_b32 v40, 4, v4\n\t"
    "v_cmp_gt_u32 vcc, 0xa00, v40\n\t"
    "v_mov_b32 v41, 0x7fffff00\n\t"
    "s_nop 0\n\t"
    "v_cndmask_b32 v40, v41, v40, vcc\n\t"
    "s_mul_i32 s72, s59, 0xa00\n\t"
    "s_add_u32 s72, s72, 0x2000\n\t"
    "buffer_store_dwordx4 v[36:39], v40, s[48:51], s72 offen sc1\n\t"
    "v_mov_b32 v41, 0\n\t"
    "s_mov_b32 s72, 0xa3000\n\t"
    "buffer_load_dwordx2 v[34:35], v41, s[48:51], s72 offen sc1\n\t"
    "s_mov_b64 s[52:53], %[wq]\n\t"
    "s_and_b32 s53, s53, 0xffff\n\t"
    "s_mov_b32 s54, 0x400000\n\t"
    "s_mov_b32 s55, 0x20000\n\t"
    "v_lshrrev_b32 v5, 1, v4\n\t"
    "v_lshlrev_b32 v32, 15, v5\n\t"
    "v_and_b32 v6, 1, v4\n\t"
    "v_lshl_add_u32 v32, v6, 4, v32\n\t"
    "s_lshl_b32 s72, s61, 5\n\t"
    "buffer_load_dwordx4 v[8:11], v32, s[52:55], s72 offen\n\t"
    "s_add_u32 s72, s72, 0x2000\n\t"
    "buffer_load_dwordx4 v[12:15], v32, s[52:55], s72 offen\n\t"
    "s_add_u32 s72, s72, 0x2000\n\t"
    "buffer_load_dwordx4 v[16:19], v32, s[52:55], s72 offen\n\t"
    "s_add_u32 s72, s72, 0x2000\n\t"
    "buffer_load_dwordx4 v[20:23], v32, s[52:55], s72 offen\n\t"
    "s_mov_b64 s[52:53], %[x]\n\t"
    "s_and_b32 s53, s53, 0xffff\n\t"
    "s_mov_b32 s54, 0x400\n\t"
    "s_mov_b32 s55, 0x20000\n\t"
    "v_and_b32 v7, 3, v3\n\t"
    "v_lshlrev_b32 v7, 6, v7\n\t"
    "v_lshrrev_b32 v33, 4, v3\n\t"
    "v_lshl_add_u32 v7, v33, 4, v7\n\t"
    "s_lshl_b32 s73, s60, 8\n\t"
    "buffer_load_dwordx4 v[52:55], v7, s[52:55], s73 offen\n\t"
    "v_bfe_u32 v7, v5, 3, 2\n\t"
    "v_lshlrev_b32 v33, 12, v7\n\t"
    "v_bfe_u32 v7, v5, 1, 2\n\t"
    "v_lshl_add_u32 v33, v7, 8, v33\n\t"
    "v_lshrrev_b32 v7, 5, v5\n\t"
    "v_lshl_add_u32 v33, v7, 10, v33\n\t"
    "v_and_b32 v7, 1, v5\n\t"
    "v_lshl_add_u32 v33, v7, 3, v33\n\t"
    "v_lshl_add_u32 v33, v6, 7, v33\n\t"
    "s_add_u32 s57, s32, 0x4000\n\t"
    "s_xor_b32 s57, s57, s80\n\t"
    "buffer_load_dwordx4 a[16:19], v0, s[40:43], s57 offen offset:0 nt\n\t"
    "s_add_u32 s57, s32, 0x5000\n\t"
    "s_xor_b32 s57, s57, s80\n\t"
    "buffer_load_dwordx4 a[20:23], v0, s[40:43], s57 offen offset:0 nt\n\t"
    "s_add_u32 s57, s32, 0x6000\n\t"
    "s_xor_b32 s57, s57, s80\n\t"
    "buffer_load_dwordx4 a[24:27], v0, s[40:43], s57 offen offset:0 nt\n\t"
    "s_add_u32 s57, s32, 0x7000\n\t"
    "s_xor_b32 s57, s57, s80\n\t"
    "buffer_load_dwordx4 a[28:31], v0, s[40:43], s57 offen offset:0 nt\n\t"
    "s_add_u32 s32, s34, 0x8000\n\t"
    "s_and_b32 s32, s32, 0x7ffff\n\t"
    "s_add_u32 s32, s32, s56\n\t"
    "s_add_u32 s57, s32, 0\n\t"
    "s_xor_b32 s57, s57, s80\n\t"
    "buffer_load_dwordx4 a[32:35], v0, s[40:43], s57 offen offset:0 nt\n\t"
    "s_add_u32 s57, s32, 0x1000\n\t"
    "s_xor_b32 s57, s57, s80\n\t"
    "buffer_load_dwordx4 a[36:39], v0, s[40:43], s57 offen offset:0 nt\n\t"
    "s_add_u32 s57, s32, 0x2000\n\t"
    "s_xor_b32 s57, s57, s80\n\t"
    "buffer_load_dwordx4 a[40:43], v0, s[40:43], s57 offen offset:0 nt\n\t"
    "s_add_u32 s57, s32, 0x3000\n\t"
    "s_xor_b32 s57, s57, s80\n\t"
    "buffer_load_dwordx4 a[44:47], v0, s[40:43], s57 offen offset:0 nt\n\t"
    "s_add_u32 s57, s32, 0x4000\n\t"
    "s_xor_b32 s57, s57, s80\n\t"
    "buffer_load_dwordx4 a[48:51], v0, s[40:43], s57 offen offset:0 nt\n\t"
    "s_add_u32 s57, s32, 0x5000\n\t"
    "s_xor_b32 s57, s57, s80\n\t"
    "buffer_load_dwordx4 a[52:55], v0, s[40:43], s57 offen offset:0 nt\n\t"
    "s_add_u32 s57, s32, 0x6000\n\t"
    "s_xor_b32 s57, s57, s80\n\t"
    "buffer_load_dwordx4 a[56:59], v0, s[40:43], s57 offen offset:0 nt\n\t"
    "s_add_u32 s57, s32, 0x7000\n\t"
    "s_xor_b32 s57, s57, s80\n\t"
    "buffer_load_dwordx4 a[60:63], v0, s[40:43], s57 offen offset:0 nt\n\t"
    "s_add_u32 s33, s34, 0\n\t"
    "s_and_b32 s33, s33, 0x7ffff\n\t"
    "s_add_u32 s33, s33, s56\n\t"
    "s_add_u32 s57, s33, 0\n\t"
    "s_xor_b32 s57, s57, s80\n\t"
    "buffer_load_dwordx4 v[56:59], v1, s[44:47], s57 offen offset:0 nt\n\t"
    "s_add_u32 s57, s33, 0x1000\n\t"
    "s_xor_b32 s57, s57, s80\n\t"
    "buffer_load_dwordx4 v[60:63], v1, s[44:47], s57 offen offset:0 nt\n\t"
    "s_add_u32 s57, s33, 0x2000\n\t"
    "s_xor_b32 s57, s57, s80\n\t"
    "buffer_load_dwordx4 v[64:67], v1, s[44:47], s57 offen offset:0 nt\n\t"
    "s_add_u32 s57, s33, 0x3000\n\t"
    "s_xor_b32 s57, s57, s80\n\t"
    "buffer_load_dwordx4 v[68:71], v1, s[44:47], s57 offen offset:0 nt\n\t"
    "s_waitcnt vmcnt(9)\n\t"
    "s_add_u32 s57, s33, 0x4000\n\t"
    "s_xor_b32 s57, s57, s80\n\t"
    "buffer_load_dwordx4 v[72:75], v1, s[44:47], s57 offen offset:0 nt\n\t"
    "v_readfirstlane_b32 s38, v34\n\t"
    "v_readfirstlane_b32 s39, v35\n\t"
    "s_nop 0\n\t"
    "s_add_u32 s38, s38, 1\n\t"
    "s_add_u32 s39, s39, 0x9e3779b1\n\t"
    "v_perm_b32 v24, v12, v8, s66\n\t"
    "v_perm_b32 v25, v20, v16, s66\n\t"
    "ds_write_b64 v33, v[24:25]\n\t"
    "v_perm_b32 v26, v12, v8, s67\n\t"
    "v_perm_b32 v27, v20, v16, s67\n\t"
    "ds_write_b64 v33, v[26:27] offset:16\n\t"
    "v_perm_b32 v28, v13, v9, s66\n\t"
    "v_perm_b32 v29, v21, v17, s66\n\t"
    "ds_write_b64 v33, v[28:29] offset:32\n\t"
    "v_perm_b32 v30, v13, v9, s67\n\t"
    "v_perm_b32 v31, v21, v17, s67\n\t"
    "ds_write_b64 v33, v[30:31] offset:48\n\t"
    "v_perm_b32 v24, v14, v10, s66\n\t"
    "v_perm_b32 v25, v22, v18, s66\n\t"
    "ds_write_b64 v33, v[24:25] offset:64\n\t"
    "v_perm_b32 v26, v14, v10, s67\n\t"
    "v_perm_b32 v27, v22, v18, s67\n\t"
    "ds_write_b64 v33, v[26:27] offset:80\n\t"
    "v_perm_b32 v28, v15, v11, s66\n\t"
    "v_perm_b32 v29, v23, v19, s66\n\t"
    "ds_write_b64 v33, v[28:29] offset:96\n\t"
    "v_perm_b32 v30, v15, v11, s67\n\t"
    "v_perm_b32 v31, v23, v19, s67\n\t"
    "ds_write_b64 v33, v[30:31] offset:112\n\t"
    "v_lshlrev_b32 v35, 4, v4\n\t"
    "s_waitcnt lgkmcnt(0)\n\t"
    "ds_read_b128 v[36:39], v35\n\t"
    "ds_read_b128 v[40:43], v35 offset:4096\n\t"
    "ds_read_b128 v[44:47], v35 offset:8192\n\t"
    "ds_read_b128 v[48:51], v35 offset:12288\n\t"
    "v_and_b32 v33, 15, v3\n\t"
    "v_lshlrev_b32 v33, 4, v33\n\t"
    "v_lshrrev_b32 v29, 5, v3\n\t"
    "v_lshlrev_b32 v29, 4, v29\n\t"
    "v_add_u32 v29, 0x100, v29\n\t"
    "v_cmp_gt_u32 vcc, 16, v3\n\t"
    "s_lshl_b32 s79, s60, 2\n\t"
    "s_add_u32 s79, s79, 0xdac0\n\t"
    "v_cndmask_b32 v32, v29, v33, vcc\n\t"
    "v_add_u32 v32, s79, v32\n\t"
    "v_add_u32 v33, 0xdac0, v33\n\t"
    "v_mov_b32 v5, 0xdac0\n\t"
    "s_lshl_b32 s79, s60, 2\n\t"
    "s_add_u32 s79, s79, 0x180\n\t"
    "v_add_u32 v29, s79, v33\n\t"
    "s_waitcnt lgkmcnt(0)\n\t"
    "v_mov_b32 v24, s68\n\t"
    "v_mov_b32 v25, s68\n\t"
    "v_mov_b32 v26, s68\n\t"
    "v_mov_b32 v27, s68\n\t"
    "s_nop 1\n\t"
    "v_mfma_f32_16x16x32_bf16 v[8:11], v[24:27], v[36:39], 0\n\t"
    "v_mfma_f32_16x16x32_bf16 v[8:11], v[24:27], v[40:43], v[8:11]\n\t"
    "v_mfma_f32_16x16x32_bf16 v[8:11], v[24:27], v[44:47], v[8:11]\n\t"
    "v_mfma_f32_16x16x32_bf16 v[8:11], v[24:27], v[48:51], v[8:11]\n\t"
    "s_nop 7\n\t"
    "ds_write_b32 v29, v8\n\t"
    "s_waitcnt lgkmcnt(0)\n\t"
    "s_barrier\n\t"
    "s_waitcnt vmcnt(9)\n\t"
    "s_add_u32 s57, s33, 0x5000\n\t"
    "s_xor_b32 s57, s57, s80\n\t"
    "buffer_load_dwordx4 v[76:79], v1, s[44:47], s57 offen offset:0 nt\n\t"
    "ds_read_b128 v[12:15], v33 offset:384\n\t"
    "s_waitcnt lgkmcnt(0)\n\t"
    "v_add_f32 v12, v12, v13\n\t"
    "v_add_f32 v14, v14, v15\n\t"
    "v_add_f32 v28, v12, v14\n\t"
    "v_mfma_f32_16x16x32_bf16 v[8:11], v[52:55], v[36:39], 0\n\t"
    "v_dot2_f32_bf16 v31, v52, s68, 0\n\t"
    "v_dot2_f32_bf16 v34, v52, v52, 0\n\t"
    "v_mfma_f32_16x16x32_bf16 v[12:15], v[52:55], v[40:43], 0\n\t"
    "v_dot2_f32_bf16 v31, v53, s68, v31\n\t"
    "v_dot2_f32_bf16 v34, v53, v53, v34\n\t"
    "v_mfma_f32_16x16x32_bf16 v[16:19], v[52:55], v[44:47], 0\n\t"
    "v_dot2_f32_bf16 v31, v54, s68, v31\n\t"
    "v_dot2_f32_bf16 v34, v54, v54, v34\n\t"
    "v_mfma_f32_16x16x32_bf16 v[20:23], v[52:55], v[48:51], 0\n\t"
    "v_dot2_f32_bf16 v31, v55, s68, v31\n\t"
    "v_dot2_f32_bf16 v34, v55, v55, v34\n\t"
    "s_nop 2\n\t"
    "v_permlane32_swap_b32 v31, v34\n\t"
    "v_add_f32 v31, v31, v34\n\t"
    "v_mov_b32 v34, v31\n\t"
    "v_add_f32 v29, v8, v13\n\t"
    "s_nop 0\n\t"
    "v_permlane16_swap_b32 v31, v34\n\t"
    "v_add_f32 v31, v31, v34\n\t"
    "v_add_f32 v30, v18, v23\n\t"
    "s_nop 0\n\t"
    "v_add_f32_dpp v31, v31, v31 quad_perm:[1,0,3,2] row_mask:0xf bank_mask:0xf\n\t"
    "v_add_f32 v29, v29, v30\n\t"
    "s_nop 0\n\t"
    "v_add_f32_dpp v31, v31, v31 quad_perm:[2,3,0,1] row_mask:0xf bank_mask:0xf\n\t"
    "v_cndmask_b32 v29, v31, v29, s[36:37]\n\t"
    "ds_write_b32 v32, v29\n\t"
    "s_waitcnt lgkmcnt(0)\n\t"
    "s_barrier\n\t"
    "ds_read_b128 v[12:15], v5 offset:256\n\t"
    "ds_read_b128 v[16:19], v5 offset:272\n\t"
    "ds_read_b128 v[8:11], v33\n\t"
    "s_waitcnt lgkmcnt(1)\n\t"
    "v_add_f32 v12, v12, v13\n\t"
    "v_add_f32 v14, v14, v15\n\t"
    "v_add_f32 v16, v16, v17\n\t"
    "v_add_f32 v18, v18, v19\n\t"
    "v_add_f32 v31, v12, v14\n\t"
    "v_add_f32 v34, v16, v18\n\t"
    "v_mul_f32 v31, 0x3b000000, v31\n\t"
    "v_mul_f32 v34, 0x3b000000, v34\n\t"
    "v_fma_f32 v34, -v31, v31, v34\n\t"
    "v_max_f32 v34, 0, v34\n\t"
    "v_add_f32 v34, 0x3727c5ac, v34\n\t"
    "v_rsq_f32 v34, v34\n\t"
    "s_waitcnt lgkmcnt(0)\n\t"
    "v_add_f32 v8, v8, v9\n\t"
    "v_add_f32 v10, v10, v11\n\t"
    "v_add_f32 v29, v8, v10\n\t"
    "v_fma_f32 v29, -v31, v28, v29\n\t"
    "v_mul_f32 v29, v29, v34\n\t"
    "s_cmp_eq_u32 s60, 0\n\t"
    "s_cselect_b32 s82, 0x5555, 0\n\t"
    "s_mov_b32 s83, 0\n\t"
    "v_lshrrev_b32 v7, 3, v3\n\t"
    "v_lshlrev_b32 v7, 6, v7\n\t"
    "v_and_b32 v30, 6, v3\n\t"
    "v_lshl_add_u32 v7, v30, 1, v7\n\t"
    "v_mov_b32 v30, 0x7fffff00\n\t"
    "v_cndmask_b32 v7, v30, v7, s[82:83]\n\t"
    "s_lshr_b32 s77, s61, 5\n\t"
    "s_lshl_b32 s77, s77, 10\n\t"
    "s_and_b32 s78, s61, 7\n\t"
    "s_lshl_b32 s78, s78, 7\n\t"
    "s_add_u32 s77, s77, s78\n\t"
    "s_bfe_u32 s78, s61, 0x20003\n\t"
    "s_lshl_b32 s78, s78, 4\n\t"
    "s_add_u32 s77, s77, s78\n\t"
    "s_add_u32 s77, s77, 0\n\t"
    "v_mov_b32_dpp v30, v29 quad_perm:[1,0,3,2] row_mask:0xf bank_mask:0xf\n\t"
    "v_cvt_pk_bf16_f32 v29, v29, v30\n\t"
    "v_min_u32 v29, -2, v29\n\t"
    "buffer_store_dword v29, v7, s[48:51], s77 offen sc1\n\t"
    "v_mov_b32 v12, s38\n\t"
    "v_mov_b32 v13, s39\n\t"
    "v_cmp_eq_u32 vcc, 0, v4\n\t"
    "v_mov_b32 v5, 0x7fffff00\n\t"
    "s_nop 0\n\t"
    "v_cndmask_b32 v5, v5, v4, vcc\n\t"
    "s_waitcnt vmcnt(10)\n\t"
    "s_add_u32 s57, s33, 0x6000\n\t"
    "s_xor_b32 s57, s57, s80\n\t"
    "buffer_load_dwordx4 v[80:83], v1, s[44:47], s57 offen offset:0 nt\n\t"
    "s_waitcnt vmcnt(10)\n\t"
    "s_add_u32 s57, s33, 0x7000\n\t"
    "s_xor_b32 s57, s57, s80\n\t"
    "buffer_load_dwordx4 v[84:87], v1, s[44:47], s57 offen offset:0 nt\n\t"
    "s_waitcnt vmcnt(10)\n\t"
    "s_add_u32 s32, s34, 0x10000\n\t"
    "s_and_b32 s32, s32, 0x7ffff\n\t"
    "s_add_u32 s32, s32, s56\n\t"
    "s_add_u32 s57, s32, 0\n\t"
    "s_xor_b32 s57, s57, s80\n\t"
    "buffer_load_dwordx4 a[64:67], v0, s[40:43], s57 offen offset:0 nt\n\t"
    "s_waitcnt vmcnt(10)\n\t"
    "s_add_u32 s57, s32, 0x1000\n\t"
    "s_xor_b32 s57, s57, s80\n\t"
    "buffer_load_dwordx4 a[68:71], v0, s[40:43], s57 offen offset:0 nt\n\t"
    "s_waitcnt vmcnt(10)\n\t"
    "s_add_u32 s57, s32, 0x2000\n\t"
    "s_xor_b32 s57, s57, s80\n\t"
    "buffer_load_dwordx4 a[72:75], v0, s[40:43], s57 offen offset:0 nt\n\t"
    "s_waitcnt vmcnt(10)\n\t"
    "s_add_u32 s57, s32, 0x3000\n\t"
    "s_xor_b32 s57, s57, s80\n\t"
    "buffer_load_dwordx4 a[76:79], v0, s[40:43], s57 offen offset:0 nt\n\t"
    "s_waitcnt vmcnt(10)\n\t"
    "s_add_u32 s57, s32, 0x4000\n\t"
    "s_xor_b32 s57, s57, s80\n\t"
    "buffer_load_dwordx4 a[80:83], v0, s[40:43], s57 offen offset:0 nt\n\t"
    "s_waitcnt vmcnt(10)\n\t"
    "s_add_u32 s57, s32, 0x5000\n\t"
    "s_xor_b32 s57, s57, s80\n\t"
    "buffer_load_dwordx4 a[84:87], v0, s[40:43], s57 offen offset:0 nt\n\t"
    "s_waitcnt vmcnt(10)\n\t"
    "s_add_u32 s57, s32, 0x6000\n\t"
    "s_xor_b32 s57, s57, s80\n\t"
    "buffer_load_dwordx4 a[88:91], v0, s[40:43], s57 offen offset:0 nt\n\t"
    "s_waitcnt vmcnt(10)\n\t"
    "s_add_u32 s57, s32, 0x7000\n\t"
    "s_xor_b32 s57, s57, s80\n\t"
    "buffer_load_dwordx4 a[92:95], v0, s[40:43], s57 offen offset:0 nt\n\t"
    "s_waitcnt vmcnt(9)\n\t"
    "s_add_u32 s33, s34, 0x8000\n\t"
    "s_and_b32 s33, s33, 0x7ffff\n\t"
    "s_add_u32 s33, s33, s56\n\t"
    "s_add_u32 s57, s33, 0\n\t"
    "s_xor_b32 s57, s57, s80\n\t"
    "buffer_load_dwordx4 v[88:91], v1, s[44:47], s57 offen offset:0 nt\n\t"
    "s_lshl_b32 s72, s59, 4\n\t"
    "s_add_u32 s72, s72, 0xa2000\n\t"
    "buffer_store_dwordx2 v[12:13], v5, s[48:51], s72 offen sc1\n\t"
    "s_waitcnt vmcnt(10)\n\t"
    "s_add_u32 s57, s33, 0x1000\n\t"
    "s_xor_b32 s57, s57, s80\n\t"
    "buffer_load_dwordx4 v[92:95], v1, s[44:47], s57 offen offset:0 nt\n\t"
    "s_waitcnt vmcnt(10)\n\t"
    "s_add_u32 s57, s33, 0x2000\n\t"
    "s_xor_b32 s57, s57, s80\n\t"
    "buffer_load_dwordx4 v[96:99], v1, s[44:47], s57 offen offset:0 nt\n\t"
    "s_waitcnt vmcnt(10)\n\t"
    "s_add_u32 s57, s33, 0x3000\n\t"
    "s_xor_b32 s57, s57, s80\n\t"
    "buffer_load_dwordx4 v[100:103], v1, s[44:47], s57 offen offset:0 nt\n\t"
    "s_waitcnt vmcnt(10)\n\t"
    "s_add_u32 s57, s33, 0x4000\n\t"
    "s_xor_b32 s57, s57, s80\n\t"
    "buffer_load_dwordx4 v[104:107], v1, s[44:47], s57 offen offset:0 nt\n\t"
    "s_waitcnt vmcnt(10)\n\t"
    "s_add_u32 s57, s33, 0x5000\n\t"
    "s_xor_b32 s57, s57, s80\n\t"
    "buffer_load_dwordx4 v[108:111], v1, s[44:47], s57 offen offset:0 nt\n\t"
    "s_waitcnt vmcnt(10)\n\t"
    "s_add_u32 s57, s33, 0x6000\n\t"
    "s_xor_b32 s57, s57, s80\n\t"
    "buffer_load_dwordx4 v[112:115], v1, s[44:47], s57 offen offset:0 nt\n\t"
    "s_waitcnt vmcnt(10)\n\t"
    "s_add_u32 s57, s33, 0x7000\n\t"
    "s_xor_b32 s57, s57, s80\n\t"
    "buffer_load_dwordx4 v[116:119], v1, s[44:47], s57 offen offset:0 nt\n\t"
    "s_waitcnt vmcnt(10)\n\t"
    "s_add_u32 s32, s34, 0x18000\n\t"
    "s_and_b32 s32, s32, 0x7ffff\n\t"
    "s_add_u32 s32, s32, s56\n\t"
    "s_add_u32 s57, s32, 0\n\t"
    "s_xor_b32 s57, s57, s80\n\t"
    "buffer_load_dwordx4 a[96:99], v0, s[40:43], s57 offen offset:0 nt\n\t"
    "s_waitcnt vmcnt(10)\n\t"
    "s_add_u32 s57, s32, 0x1000\n\t"
    "s_xor_b32 s57, s57, s80\n\t"
    "buffer_load_dwordx4 a[100:103], v0, s[40:43], s57 offen offset:0 nt\n\t"
    "s_waitcnt vmcnt(10)\n\t"
    "s_add_u32 s57, s32, 0x2000\n\t"
    "s_xor_b32 s57, s57, s80\n\t"
    "buffer_load_dwordx4 v[120:123], v0, s[40:43], s57 offen offset:0 nt\n\t"
    "s_waitcnt vmcnt(9)\n\t"
    "s_add_u32 s57, s32, 0x3000\n\t"
    "s_xor_b32 s57, s57, s80\n\t"
    "buffer_load_dwordx4 v[124:127], v0, s[40:43], s57 offen offset:0 nt\n\t"
    "s_waitcnt vmcnt(9)\n\t"
    "s_add_u32 s57, s32, 0x4000\n\t"
    "s_xor_b32 s57, s57, s80\n\t"
    "buffer_load_dwordx4 a[104:107], v0, s[40:43], s57 offen offset:0\n\t"
    "s_waitcnt vmcnt(9)\n\t"
    "s_add_u32 s57, s32, 0x5000\n\t"
    "s_xor_b32 s57, s57, s80\n\t"
    "buffer_load_dwordx4 a[108:111], v0, s[40:43], s57 offen offset:0\n\t"
    "s_waitcnt vmcnt(9)\n\t"
    "s_add_u32 s57, s32, 0x6000\n\t"
    "s_xor_b32 s57, s57, s80\n\t"
    "buffer_load_dwordx4 a[112:115], v0, s[40:43], s57 offen offset:0\n\t"
    "s_waitcnt vmcnt(9)\n\t"
    "s_add_u32 s57, s32, 0x7000\n\t"
    "s_xor_b32 s57, s57, s80\n\t"
    "buffer_load_dwordx4 a[116:119], v0, s[40:43], s57 offen offset:0\n\t"
    "s_waitcnt vmcnt(9)\n\t"
    "s_add_u32 s33, s34, 0x10000\n\t"
    "s_and_b32 s33, s33, 0x7ffff\n\t"
    "s_add_u32 s33, s33, s56\n\t"
    "s_add_u32 s57, s33, 0\n\t"
    "s_xor_b32 s57, s57, s80\n\t"
    "buffer_load_dwordx4 v[128:131], v1, s[44:47], s57 offen offset:0\n\t"
    "s_waitcnt vmcnt(9)\n\t"
    "s_add_u32 s57, s33, 0x1000\n\t"
    "s_xor_b32 s57, s57, s80\n\t"
    "buffer_load_dwordx4 v[132:135], v1, s[44:47], s57 offen offset:0\n\t"
    "s_mov_b32 s72, 0xa2000\n\t"
    "buffer_load_dwordx2 v[14:15], v35, s[48:51], s72 offen sc1\n\t"
    "s_mov_b32 s58, 0x100000\n\t"
    "s_waitcnt vmcnt(10)\n\t"
    "s_add_u32 s57, s33, 0x2000\n\t"
    "s_xor_b32 s57, s57, s80\n\t"
    "buffer_load_dwordx4 v[136:139], v1, s[44:47], s57 offen offset:0\n\t"
    "s_waitcnt vmcnt(10)\n\t"
    "s_add_u32 s57, s33, 0x3000\n\t"
    "s_xor_b32 s57, s57, s80\n\t"
    "buffer_load_dwordx4 v[140:143], v1, s[44:47], s57 offen offset:0\n\t"
    "s_waitcnt vmcnt(10)\n\t"
    "s_add_u32 s57, s33, 0x4000\n\t"
    "s_xor_b32 s57, s57, s80\n\t"
    "buffer_load_dwordx4 v[144:147], v1, s[44:47], s57 offen offset:0\n\t"
    "s_waitcnt vmcnt(10)\n\t"
    "s_add_u32 s57, s33, 0x5000\n\t"
    "s_xor_b32 s57, s57, s80\n\t"
    "buffer_load_dwordx4 v[148:151], v1, s[44:47], s57 offen offset:0\n\t"
    "s_waitcnt vmcnt(10)\n\t"
    "s_add_u32 s57, s33, 0x6000\n\t"
    "s_xor_b32 s57, s57, s80\n\t"
    "buffer_load_dwordx4 v[152:155], v1, s[44:47], s57 offen offset:0\n\t"
    "s_waitcnt vmcnt(10)\n\t"
    "s_add_u32 s57, s33, 0x7000\n\t"
    "s_xor_b32 s57, s57, s80\n\t"
    "buffer_load_dwordx4 v[156:159], v1, s[44:47], s57 offen offset:0\n\t"
    "s_waitcnt vmcnt(10)\n\t"
    "s_add_u32 s32, s34, 0x20000\n\t"
    "s_and_b32 s32, s32, 0x7ffff\n\t"
    "s_add_u32 s32, s32, s56\n\t"
    "s_add_u32 s57, s32, 0\n\t"
    "s_xor_b32 s57, s57, s80\n\t"
    "buffer_load_dwordx4 a[120:123], v0, s[40:43], s57 offen offset:0\n\t"
    "s_waitcnt vmcnt(10)\n\t"
    "s_add_u32 s57, s32, 0x1000\n\t"
    "s_xor_b32 s57, s57, s80\n\t"
    "buffer_load_dwordx4 a[124:127], v0, s[40:43], s57 offen offset:0\n\t"
    "s_waitcnt vmcnt(10)\n\t"
    "s_add_u32 s57, s32, 0x2000\n\t"
    "s_xor_b32 s57, s57, s80\n\t"
    "buffer_load_dwordx4 a[128:131], v0, s[40:43], s57 offen offset:0\n\t"
    "s_waitcnt vmcnt(10)\n\t"
    "s_add_u32 s57, s32, 0x3000\n\t"
    "s_xor_b32 s57, s57, s80\n\t"
    "buffer_load_dwordx4 a[132:135], v0, s[40:43], s57 offen offset:0\n\t"
    "s_waitcnt vmcnt(9)\n\t"
    "s_add_u32 s57, s32, 0x4000\n\t"
    "s_xor_b32 s57, s57, s80\n\t"
    "buffer_load_dwordx4 a[136:139], v0, s[40:43], s57 offen offset:0\n\t"
    "s_branch .Lp2_%=\n\t"
    ".Lp1_%=:\n\t"
    "buffer_load_dwordx2 v[14:15], v35, s[48:51], s72 offen sc1\n\t"
    "s_waitcnt vmcnt(0)\n\t"
    ".Lp2_%=:\n\t"
    "v_xor_b32 v6, s38, v14\n\t"
    "v_xor_b32 v7, s39, v15\n\t"
    "v_or_b32 v6, v6, v7\n\t"
    "v_cmp_ne_u32 vcc, 0, v6\n\t"
    "s_cbranch_vccz .Lp3_%=\n\t"
    "s_mov_b64 exec, vcc\n\t"
    "s_sub_u32 s58, s58, 1\n\t"
    "s_cbranch_scc1 .Lp3_%=\n\t"
    "s_sleep 3\n\t"
    "s_branch .Lp1_%=\n\t"
    ".Lp3_%=:\n\t"
    "s_mov_b64 exec, -1\n\t"
    "s_barrier\n\t"
    "v_mbcnt_lo_u32_b32 v37, -1, 0\n\t"
    "v_mbcnt_hi_u32_b32 v37, -1, v37\n\t"
    "v_add_u32 v39, s60, v37\n\t"
    "v_and_b32 v39, 3, v39\n\t"
    "v_lshlrev_b32 v38, 4, v39\n\t"
    "v_lshrrev_b32 v39, 2, v37\n\t"
    "v_lshl_add_u32 v38, v39, 6, v38\n\t"
    "s_lshr_b32 s76, s59, 5\n\t"
    "s_lshl_b32 s76, s76, 10\n\t"
    "s_add_u32 s76, s76, s62\n\t"
    "buffer_load_dwordx4 v[4:7], v38, s[48:51], s76 offen sc1\n\t"
    "v_mov_b32 v32, 0xff800000\n\t"
    "v_mov_b32 v33, 0\n\t"
    "v_mov_b32 v34, 0\n\t"
    "v_mov_b32 v8, 0\n\t"
    "v_mov_b32 v9, 0\n\t"
    "v_mov_b32 v10, 0\n\t"
    "v_mov_b32 v11, 0\n\t"
    "v_mov_b32 v12, 0\n\t"
    "v_mov_b32 v13, 0\n\t"
    "v_mov_b32 v14, 0\n\t"
    "v_mov_b32 v15, 0\n\t"
    "v_mov_b32 v16, 0\n\t"
    "v_mov_b32 v17, 0\n\t"
    "v_mov_b32 v18, 0\n\t"
    "v_mov_b32 v19, 0\n\t"
    "v_mov_b32 v20, 0\n\t"
    "v_mov_b32 v21, 0\n\t"
    "v_mov_b32 v22, 0\n\t"
    "v_mov_b32 v23, 0\n\t"
    "v_mov_b32 v24, 0\n\t"
    "v_mov_b32 v25, 0\n\t"
    "v_mov_b32 v26, 0\n\t"
    "v_mov_b32 v27, 0\n\t"
    "v_mov_b32 v28, 0\n\t"
    "v_mov_b32 v29, 0\n\t"
    "v_mov_b32 v30, 0\n\t"
    "v_mov_b32 v31, 0\n\t"
    "s_waitcnt vmcnt(10)\n\t"
    "s_add_u32 s57, s32, 0x5000\n\t"
    "s_xor_b32 s57, s57, s80\n\t"
    "buffer_load_dwordx4 a[140:143], v0, s[40:43], s57 offen offset:0\n\t"
    "s_waitcnt vmcnt(10)\n\t"
    "s_add_u32 s57, s32, 0x6000\n\t"
    "s_xor_b32 s57, s57, s80\n\t"
    "buffer_load_dwordx4 a[144:147], v0, s[40:43], s57 offen offset:0\n\t"
    "s_waitcnt vmcnt(10)\n\t"
    "s_add_u32 s57, s32, 0x7000\n\t"
    "s_xor_b32 s57, s57, s80\n\t"
    "buffer_load_dwordx4 a[148:151], v0, s[40:43], s57 offen offset:0\n\t"
    "s_waitcnt vmcnt(10)\n\t"
    "s_add_u32 s33, s34, 0x18000\n\t"
    "s_and_b32 s33, s33, 0x7ffff\n\t"
    "s_add_u32 s33, s33, s56\n\t"
    "s_add_u32 s57, s33, 0\n\t"
    "s_xor_b32 s57, s57, s80\n\t"
    "buffer_load_dwordx4 v[160:163], v1, s[44:47], s57 offen offset:0\n\t"
    "s_waitcnt vmcnt(10)\n\t"
    "s_add_u32 s57, s33, 0x1000\n\t"
    "s_xor_b32 s57, s57, s80\n\t"
    "buffer_load_dwordx4 v[164:167], v1, s[44:47], s57 offen offset:0\n\t"
    "s_waitcnt vmcnt(10)\n\t"
    "s_add_u32 s57, s33, 0x2000\n\t"
    "s_xor_b32 s57, s57, s80\n\t"
    "buffer_load_dwordx4 v[168:171], v1, s[44:47], s57 offen offset:0\n\t"
    "s_waitcnt vmcnt(10)\n\t"
    "s_add_u32 s57, s33, 0x3000\n\t"
    "s_xor_b32 s57, s57, s80\n\t"
    "buffer_load_dwordx4 v[172:175], v1, s[44:47], s57 offen offset:0\n\t"
    "s_waitcnt vmcnt(10)\n\t"
    "s_add_u32 s57, s33, 0x4000\n\t"
    "s_xor_b32 s57, s57, s80\n\t"
    "buffer_load_dwordx4 v[176:179], v1, s[44:47], s57 offen offset:0\n\t"
    "s_waitcnt vmcnt(10)\n\t"
    "s_add_u32 s57, s33, 0x5000\n\t"
    "s_xor_b32 s57, s57, s80\n\t"
    "buffer_load_dwordx4 v[180:183], v1, s[44:47], s57 offen offset:0\n\t"
    "s_waitcnt vmcnt(10)\n\t"
    "s_add_u32 s57, s33, 0x6000\n\t"
    "s_xor_b32 s57, s57, s80\n\t"
    "buffer_load_dwordx4 v[184:187], v1, s[44:47], s57 offen offset:0\n\t"
    "s_waitcnt vmcnt(9)\n\t"
    "s_add_u32 s57, s33, 0x7000\n\t"
    "s_xor_b32 s57, s57, s80\n\t"
    "buffer_load_dwordx4 v[188:191], v1, s[44:47], s57 offen offset:0\n\t"
    "v_mfma_f32_16x16x32_bf16 v[44:47], a[0:3], v[4:7], 0\n\t"
    "v_mfma_f32_16x16x32_bf16 v[48:51], a[4:7], v[4:7], 0\n\t"
    "v_mfma_f32_16x16x32_bf16 v[52:55], a[8:11], v[4:7], 0\n\t"
    "s_nop 5\n\t"
    "v_add_f32_dpp v40, v45, v44 row_ror:12 row_mask:0xf bank_mask:0xf\n\t"
    "v_add_f32_dpp v42, v47, v46 row_ror:12 row_mask:0xf bank_mask:0xf\n\t"
    "v_mfma_f32_16x16x32_bf16 v[44:47], a[12:15], v[4:7], 0\n\t"
    "v_add_f32_dpp v41, v50, v49 row_ror:12 row_mask:0xf bank_mask:0xf\n\t"
    "v_add_f32_dpp v43, v48, v51 row_ror:12 row_mask:0xf bank_mask:0xf\n\t"
    "v_add_f32_dpp v35, v42, v40 row_ror:8 row_mask:0xf bank_mask:0x1\n\t"
    "v_mfma_f32_16x16x32_bf16 v[48:51], a[16:19], v[4:7], 0\n\t"
    "v_add_f32_dpp v40, v55, v54 row_ror:12 row_mask:0xf bank_mask:0xf\n\t"
    "v_add_f32_dpp v42, v53, v52 row_ror:12 row_mask:0xf bank_mask:0xf\n\t"
    "v_add_f32_dpp v35, v43, v41 row_ror:8 row_mask:0xf bank_mask:0x2\n\t"
    "v_mfma_f32_16x16x32_bf16 v[52:55], a[20:23], v[4:7], 0\n\t"
    "v_add_f32_dpp v41, v44, v47 row_ror:12 row_mask:0xf bank_mask:0xf\n\t"
    "v_add_f32_dpp v43, v46, v45 row_ror:12 row_mask:0xf bank_mask:0xf\n\t"
    "v_add_f32_dpp v35, v42, v40 row_ror:8 row_mask:0xf bank_mask:0x4\n\t"
    "v_mfma_f32_16x16x32_bf16 v[44:47], a[24:27], v[4:7], 0\n\t"
    "v_add_f32_dpp v40, v49, v48 row_ror:12 row_mask:0xf bank_mask:0xf\n\t"
    "v_add_f32_dpp v42, v51, v50 row_ror:12 row_mask:0xf bank_mask:0xf\n\t"
    "v_add_f32_dpp v35, v43, v41 row_ror:8 row_mask:0xf bank_mask:0x8\n\t"
    "v_mfma_f32_16x16x32_bf16 v[48:51], a[28:31], v[4:7], 0\n\t"
    "v_add_f32_dpp v41, v54, v53 row_ror:12 row_mask:0xf bank_mask:0xf\n\t"
    "v_add_f32_dpp v43, v52, v55 row_ror:12 row_mask:0xf bank_mask:0xf\n\t"
    "v_add_f32_dpp v36, v42, v40 row_ror:8 row_mask:0xf bank_mask:0x1\n\t"
    "s_nop 0\n\t"
    "v_add_f32_dpp v40, v47, v46 row_ror:12 row_mask:0xf bank_mask:0xf\n\t"
    "v_add_f32_dpp v42, v45, v44 row_ror:12 row_mask:0xf bank_mask:0xf\n\t"
    "v_add_f32_dpp v36, v43, v41 row_ror:8 row_mask:0xf bank_mask:0x2\n\t"
    "s_nop 0\n\t"
    "v_add_f32_dpp v41, v48, v51 row_ror:12 row_mask:0xf bank_mask:0xf\n\t"
    "v_add_f32_dpp v43, v50, v49 row_ror:12 row_mask:0xf bank_mask:0xf\n\t"
    "v_add_f32_dpp v36, v42, v40 row_ror:8 row_mask:0xf bank_mask:0x4\n\t"
    "s_nop 0\n\t"
    "v_add_f32_dpp v36, v43, v41 row_ror:8 row_mask:0xf bank_mask:0x8\n\t"
    "v_max_f32 v37, v35, v36\n\t"
    "s_nop 1\n\t"
    "v_max_f32_dpp v37, v37, v37 row_ror:4 row_mask:0xf bank_mask:0xf\n\t"
    "s_nop 1\n\t"
    "v_max_f32_dpp v37, v37, v37 row_ror:8 row_mask:0xf bank_mask:0xf\n\t"
    "v_mov_b32 v38, v37\n\t"
    "s_nop 1\n\t"
    "v_permlane16_swap_b32 v37, v38\n\t"
    "v_max_f32 v37, v37, v38\n\t"
    "v_mov_b32 v38, v37\n\t"
    "s_nop 1\n\t"
    "v_permlane32_swap_b32 v37, v38\n\t"
    "v_max_f32 v37, v37, v38\n\t"
    "v_cmp_gt_f32 vcc, v37, v32\n\t"
    "s_cbranch_vccz .Lr4_%=\n\t"
    "v_max_f32 v37, v37, v32\n\t"
    "v_sub_f32 v38, v32, v37\n\t"
    "v_mul_f32 v38, s64, v38\n\t"
    "v_exp_f32 v38, v38\n\t"
    "v_mov_b32 v32, v37\n\t"
    "v_mul_f32 v34, s65, v37\n\t"
    "v_mul_f32 v33, v33, v38\n\t"
    "v_mul_f32_dpp v8, v38, v8 quad_perm:[0,0,0,0] row_mask:0xf bank_mask:0xf\n\t"
    "v_mul_f32_dpp v9, v38, v9 quad_perm:[1,1,1,1] row_mask:0xf bank_mask:0xf\n\t"
    "v_mul_f32_dpp v10, v38, v10 quad_perm:[2,2,2,2] row_mask:0xf bank_mask:0xf\n\t"
    "v_mul_f32_dpp v11, v38, v11 quad_perm:[3,3,3,3] row_mask:0xf bank_mask:0xf\n\t"
    "v_mul_f32_dpp v12, v38, v12 quad_perm:[0,0,0,0] row_mask:0xf bank_mask:0xf\n\t"
    "v_mul_f32_dpp v13, v38, v13 quad_perm:[1,1,1,1] row_mask:0xf bank_mask:0xf\n\t"
    "v_mul_f32_dpp v14, v38, v14 quad_perm:[2,2,2,2] row_mask:0xf bank_mask:0xf\n\t"
    "v_mul_f32_dpp v15, v38, v15 quad_perm:[3,3,3,3] row_mask:0xf bank_mask:0xf\n\t"
    ".Lr4_%=:\n\t"
    "v_fma_f32 v35, v35, s64, v34\n\t"
    "v_fma_f32 v36, v36, s64, v34\n\t"
    "v_exp_f32 v35, v35\n\t"
    "v_exp_f32 v36, v36\n\t"
    "s_nop 0\n\t"
    "v_cvt_pk_bf16_f32 v39, v35, v36\n\t"
    "v_dot2_f32_bf16 v33, v39, s68, v33\n\t"
    "s_waitcnt vmcnt(9)\n\t"
    "s_add_u32 s32, s34, 0x28000\n\t"
    "s_and_b32 s32, s32, 0x7ffff\n\t"
    "s_add_u32 s32, s32, s56\n\t"
    "s_add_u32 s57, s32, 0\n\t"
    "s_xor_b32 s57, s57, s80\n\t"
    "buffer_load_dwordx4 a[152:155], v0, s[40:43], s57 offen offset:0\n\t"
    "v_mbcnt_lo_u32_b32 v228, -1, 0\n\t"
    "v_mbcnt_hi_u32_b32 v228, -1, v228\n\t"
    "s_lshl_b32 s72, s60, 6\n\t"
    "v_add_u32 v229, s72, v228\n\t"
    "v_lshlrev_b32 v227, 4, v229\n\t"
    "s_mov_b64 s[52:53], %[w2]\n\t"
    "s_and_b32 s53, s53, 0xffff\n\t"
    "s_mov_b32 s54, 0x1c0000\n\t"
    "s_mov_b32 s55, 0x20000\n\t"
    "v_and_b32 v230, 3, v228\n\t"
    "v_lshlrev_b32 v225, 13, v230\n\t"
    "v_lshrrev_b32 v231, 2, v228\n\t"
    "v_lshl_add_u32 v225, v231, 4, v225\n\t"
    "v_mul_u32_u24 v226, 0x7000, v231\n\t"
    "v_lshl_add_u32 v226, v230, 4, v226\n\t"
    "s_lshr_b32 s73, s61, 2\n\t"
    "s_lshl_b32 s72, s73, 15\n\t"
    "s_and_b32 s74, s61, 3\n\t"
    "s_lshl_b32 s75, s74, 8\n\t"
    "s_add_u32 s72, s72, s75\n\t"
    "s_mul_i32 s78, s74, 0x70000\n\t"
    "s_add_u32 s78, s78, 0xb0000\n\t"
    "s_lshr_b32 s75, s73, 3\n\t"
    "s_lshl_b32 s75, s75, 12\n\t"
    "s_add_u32 s78, s78, s75\n\t"
    "s_bfe_u32 s75, s73, 0x20001\n\t"
    "s_lshl_b32 s75, s75, 8\n\t"
    "s_add_u32 s78, s78, s75\n\t"
    "s_and_b32 s75, s73, 1\n\t"
    "s_lshl_b32 s75, s75, 6\n\t"
    "s_add_u32 s78, s78, s75\n\t"
    "s_bfe_u32 s75, s59, 0x20003\n\t"
    "s_cmp_eq_u32 s75, s60\n\t"
    "s_cselect_b32 s75, 0xe0, 0\n\t"
    "s_cmp_lt_u32 s61, s75\n\t"
    "s_cselect_b32 s72, s72, 0x7fffff00\n\t"
    "s_cselect_b32 s78, s78, 0x7fffff00\n\t"
    "buffer_load_dwordx4 v[252:255], v225, s[52:55], s72 offen nt\n\t"
    "v_mov_b32_dpp v16, v39 quad_perm:[0,1,2,3] row_mask:0xf bank_mask:0x1\n\t"
    "v_mov_b32_dpp v17, v39 row_ror:12 row_mask:0xf bank_mask:0x1\n\t"
    "v_mov_b32_dpp v18, v39 row_ror:8 row_mask:0xf bank_mask:0x1\n\t"
    "v_mov_b32_dpp v19, v39 row_ror:4 row_mask:0xf bank_mask:0x1\n\t"
    "v_mov_b32_dpp v20, v39 row_ror:4 row_mask:0xf bank_mask:0x2\n\t"
    "v_mov_b32_dpp v21, v39 quad_perm:[0,1,2,3] row_mask:0xf bank_mask:0x2\n\t"
    "v_mov_b32_dpp v22, v39 row_ror:12 row_mask:0xf bank_mask:0x2\n\t"
    "v_mov_b32_dpp v23, v39 row_ror:8 row_mask:0xf bank_mask:0x2\n\t"
    "v_mov_b32_dpp v24, v39 row_ror:8 row_mask:0xf bank_mask:0x4\n\t"
    "v_mov_b32_dpp v25, v39 row_ror:4 row_mask:0xf bank_mask:0x4\n\t"
    "v_mov_b32_dpp v26, v39 quad_perm:[0,1,2,3] row_mask:0xf bank_mask:0x4\n\t"
    "v_mov_b32_dpp v27, v39 row_ror:12 row_mask:0xf bank_mask:0x4\n\t"
    "v_mov_b32_dpp v28, v39 row_ror:12 row_mask:0xf bank_mask:0x8\n\t"
    "v_mov_b32_dpp v29, v39 row_ror:8 row_mask:0xf bank_mask:0x8\n\t"
    "v_mov_b32_dpp v30, v39 row_ror:4 row_mask:0xf bank_mask:0x8\n\t"
    "v_mov_b32_dpp v31, v39 quad_perm:[0,1,2,3] row_mask:0xf bank_mask:0x8\n\t"
    "v_mfma_f32_16x16x32_bf16 v[44:47], a[32:35], v[4:7], 0\n\t"
    "v_mfma_f32_16x16x32_bf16 v[48:51], a[36:39], v[4:7], 0\n\t"
    "v_mfma_f32_16x16x32_bf16 v[52:55], a[40:43], v[4:7], 0\n\t"
    "s_nop 5\n\t"
    "v_add_f32_dpp v40, v45, v44 row_ror:12 row_mask:0xf bank_mask:0xf\n\t"
    "v_add_f32_dpp v42, v47, v46 row_ror:12 row_mask:0xf bank_mask:0xf\n\t"
    "v_mfma_f32_16x16x32_bf16 v[44:47], a[44:47], v[4:7], 0\n\t"
    "v_add_f32_dpp v41, v50, v49 row_ror:12 row_mask:0xf bank_mask:0xf\n\t"
    "v_add_f32_dpp v43, v48, v51 row_ror:12 row_mask:0xf bank_mask:0xf\n\t"
    "v_add_f32_dpp v35, v42, v40 row_ror:8 row_mask:0xf bank_mask:0x1\n\t"
    "v_mfma_f32_16x16x32_bf16 v[48:51], a[48:51], v[4:7], 0\n\t"
    "s_waitcnt vmcnt(10)\n\t"
    "s_add_u32 s57, s32, 0x1000\n\t"
    "s_xor_b32 s57, s57, s80\n\t"
    "buffer_load_dwordx4 a[156:159], v0, s[40:43], s57 offen offset:0\n\t"
    "v_add_f32_dpp v40, v55, v54 row_ror:12 row_mask:0xf bank_mask:0xf\n\t"
    "v_add_f32_dpp v42, v53, v52 row_ror:12 row_mask:0xf bank_mask:0xf\n\t"
    "v_add_f32_dpp v35, v43, v41 row_ror:8 row_mask:0xf bank_mask:0x2\n\t"
    "v_mfma_f32_16x16x32_bf16 v[52:55], a[52:55], v[4:7], 0\n\t"
    "v_add_f32_dpp v41, v44, v47 row_ror:12 row_mask:0xf bank_mask:0xf\n\t"
    "v_add_f32_dpp v43, v46, v45 row_ror:12 row_mask:0xf bank_mask:0xf\n\t"
    "v_add_f32_dpp v35, v42, v40 row_ror:8 row_mask:0xf bank_mask:0x4\n\t"
    "v_mfma_f32_16x16x32_bf16 v[44:47], a[56:59], v[4:7], 0\n\t"
    "v_add_f32_dpp v40, v49, v48 row_ror:12 row_mask:0xf bank_mask:0xf\n\t"
    "v_add_f32_dpp v42, v51, v50 row_ror:12 row_mask:0xf bank_mask:0xf\n\t"
    "v_add_f32_dpp v35, v43, v41 row_ror:8 row_mask:0xf bank_mask:0x8\n\t"
    "v_mfma_f32_16x16x32_bf16 v[48:51], a[60:63], v[4:7], 0\n\t"
    "v_add_f32_dpp v41, v54, v53 row_ror:12 row_mask:0xf bank_mask:0xf\n\t"
    "v_add_f32_dpp v43, v52, v55 row_ror:12 row_mask:0xf bank_mask:0xf\n\t"
    "v_add_f32_dpp v36, v42, v40 row_ror:8 row_mask:0xf bank_mask:0x1\n\t"
    "s_nop 0\n\t"
    "v_add_f32_dpp v40, v47, v46 row_ror:12 row_mask:0xf bank_mask:0xf\n\t"
    "v_add_f32_dpp v42, v45, v44 row_ror:12 row_mask:0xf bank_mask:0xf\n\t"
    "v_add_f32_dpp v36, v43, v41 row_ror:8 row_mask:0xf bank_mask:0x2\n\t"
    "s_nop 0\n\t"
    "v_add_f32_dpp v41, v48, v51 row_ror:12 row_mask:0xf bank_mask:0xf\n\t"
    "v_add_f32_dpp v43, v50, v49 row_ror:12 row_mask:0xf bank_mask:0xf\n\t"
    "v_add_f32_dpp v36, v42, v40 row_ror:8 row_mask:0xf bank_mask:0x4\n\t"
    "s_nop 0\n\t"
    "v_add_f32_dpp v36, v43, v41 row_ror:8 row_mask:0xf bank_mask:0x8\n\t"
    "v_perm_b32 v44, v72, v56, s66\n\t"
    "v_perm_b32 v45, v76, v60, s66\n\t"
    "v_perm_b32 v46, v80, v64, s66\n\t"
    "v_perm_b32 v47, v84, v68, s66\n\t"
    "v_perm_b32 v48, v72, v56, s67\n\t"
    "v_perm_b32 v49, v76, v60, s67\n\t"
    "v_perm_b32 v50, v80, v64, s67\n\t"
    "v_perm_b32 v51, v84, v68, s67\n\t"
    "v_max_f32 v37, v35, v36\n\t"
    "v_mfma_f32_16x16x32_bf16 v[8:11], v[16:19], v[44:47], v[8:11]\n\t"
    "ds_write_b128 v2, v[44:47]\n\t"
    "v_perm_b32 v52, v73, v57, s66\n\t"
    "v_perm_b32 v53, v77, v61, s66\n\t"
    "v_perm_b32 v54, v81, v65, s66\n\t"
    "v_perm_b32 v55, v85, v69, s66\n\t"
    "v_max_f32_dpp v37, v37, v37 row_ror:4 row_mask:0xf bank_mask:0xf\n\t"
    "v_mfma_f32_16x16x32_bf16 v[8:11], v[20:23], v[48:51], v[8:11]\n\t"
    "ds_write_b128 v2, v[48:51] offset:1024\n\t"
    "v_perm_b32 v44, v73, v57, s67\n\t"
    "v_perm_b32 v45, v77, v61, s67\n\t"
    "v_perm_b32 v46, v81, v65, s67\n\t"
    "v_perm_b32 v47, v85, v69, s67\n\t"
    "v_max_f32_dpp v37, v37, v37 row_ror:8 row_mask:0xf bank_mask:0xf\n\t"
    "v_mfma_f32_16x16x32_bf16 v[8:11], v[24:27], v[52:55], v[8:11]\n\t"
    "ds_write_b128 v2, v[52:55] offset:2048\n\t"
    "v_perm_b32 v48, v74, v58, s66\n\t"
    "v_perm_b32 v49, v78, v62, s66\n\t"
    "v_perm_b32 v50, v82, v66, s66\n\t"
    "v_perm_b32 v51, v86, v70, s66\n\t"
    "v_mov_b32 v38, v37\n\t"
    "v_mfma_f32_16x16x32_bf16 v[8:11], v[28:31], v[44:47], v[8:11]\n\t"
    "ds_write_b128 v2, v[44:47] offset:3072\n\t"
    "v_perm_b32 v52, v74, v58, s67\n\t"
    "v_perm_b32 v53, v78, v62, s67\n\t"
    "v_perm_b32 v54, v82, v66, s67\n\t"
    "v_perm_b32 v55, v86, v70, s67\n\t"
    "v_permlane16_swap_b32 v37, v38\n\t"
    "v_mfma_f32_16x16x32_bf16 v[12:15], v[16:19], v[48:51], v[12:15]\n\t"
    "ds_write_b128 v2, v[48:51] offset:4096\n\t"
    "v_perm_b32 v44, v75, v59, s66\n\t"
    "v_perm_b32 v45, v79, v63, s66\n\t"
    "v_perm_b32 v46, v83, v67, s66\n\t"
    "v_perm_b32 v47, v87, v71, s66\n\t"
    "v_max_f32 v37, v37, v38\n\t"
    "v_mfma_f32_16x16x32_bf16 v[12:15], v[20:23], v[52:55], v[12:15]\n\t"
    "ds_write_b128 v2, v[52:55] offset:5120\n\t"
    "s_waitcnt vmcnt(10)\n\t"
    "s_add_u32 s57, s32, 0x2000\n\t"
    "s_xor_b32 s57, s57, s80\n\t"
    "buffer_load_dwordx4 a[160:163], v0, s[40:43], s57 offen offset:0\n\t"
    "buffer_load_dwordx4 v[248:251], v225, s[52:55], s72 offen offset:1024 nt\n\t"
    "v_perm_b32 v48, v75, v59, s67\n\t"
    "v_perm_b32 v49, v79, v63, s67\n\t"
    "v_perm_b32 v50, v83, v67, s67\n\t"
    "v_perm_b32 v51, v87, v71, s67\n\t"
    "v_mov_b32 v38, v37\n\t"
    "v_mfma_f32_16x16x32_bf16 v[12:15], v[24:27], v[44:47], v[12:15]\n\t"
    "ds_write_b128 v2, v[44:47] offset:6144\n\t"
    "v_permlane32_swap_b32 v37, v38\n\t"
    "v_mfma_f32_16x16x32_bf16 v[12:15], v[28:31], v[48:51], v[12:15]\n\t"
    "ds_write_b128 v2, v[48:51] offset:7168\n\t"
    "v_max_f32 v37, v37, v38\n\t"
    "v_cmp_gt_f32 vcc, v37, v32\n\t"
    "s_cbranch_vccz .Lr5_%=\n\t"
    "v_max_f32 v37, v37, v32\n\t"
    "v_sub_f32 v38, v32, v37\n\t"
    "v_mul_f32 v38, s64, v38\n\t"
    "v_exp_f32 v38, v38\n\t"
    "v_mov_b32 v32, v37\n\t"
    "v_mul_f32 v34, s65, v37\n\t"
    "v_mul_f32 v33, v33, v38\n\t"
    "v_mul_f32_dpp v8, v38, v8 quad_perm:[0,0,0,0] row_mask:0xf bank_mask:0xf\n\t"
    "v_mul_f32_dpp v9, v38, v9 quad_perm:[1,1,1,1] row_mask:0xf bank_mask:0xf\n\t"
    "v_mul_f32_dpp v10, v38, v10 quad_perm:[2,2,2,2] row_mask:0xf bank_mask:0xf\n\t"
    "v_mul_f32_dpp v11, v38, v11 quad_perm:[3,3,3,3] row_mask:0xf bank_mask:0xf\n\t"
    "v_mul_f32_dpp v12, v38, v12 quad_perm:[0,0,0,0] row_mask:0xf bank_mask:0xf\n\t"
    "v_mul_f32_dpp v13, v38, v13 quad_perm:[1,1,1,1] row_mask:0xf bank_mask:0xf\n\t"
    "v_mul_f32_dpp v14, v38, v14 quad_perm:[2,2,2,2] row_mask:0xf bank_mask:0xf\n\t"
    "v_mul_f32_dpp v15, v38, v15 quad_perm:[3,3,3,3] row_mask:0xf bank_mask:0xf\n\t"
    ".Lr5_%=:\n\t"
    "v_fma_f32 v35, v35, s64, v34\n\t"
    "v_fma_f32 v36, v36, s64, v34\n\t"
    "v_exp_f32 v35, v35\n\t"
    "v_exp_f32 v36, v36\n\t"
    "s_nop 0\n\t"
    "v_cvt_pk_bf16_f32 v39, v35, v36\n\t"
    "v_dot2_f32_bf16 v33, v39, s68, v33\n\t"
    "s_nop 0\n\t"
    "v_mov_b32_dpp v16, v39 quad_perm:[0,1,2,3] row_mask:0xf bank_mask:0x1\n\t"
    "v_mov_b32_dpp v17, v39 row_ror:12 row_mask:0xf bank_mask:0x1\n\t"
    "v_mov_b32_dpp v18, v39 row_ror:8 row_mask:0xf bank_mask:0x1\n\t"
    "v_mov_b32_dpp v19, v39 row_ror:4 row_mask:0xf bank_mask:0x1\n\t"
    "v_mov_b32_dpp v20, v39 row_ror:4 row_mask:0xf bank_mask:0x2\n\t"
    "v_mov_b32_dpp v21, v39 quad_perm:[0,1,2,3] row_mask:0xf bank_mask:0x2\n\t"
    "v_mov_b32_dpp v22, v39 row_ror:12 row_mask:0xf bank_mask:0x2\n\t"
    "v_mov_b32_dpp v23, v39 row_ror:8 row_mask:0xf bank_mask:0x2\n\t"
    "v_mov_b32_dpp v24, v39 row_ror:8 row_mask:0xf bank_mask:0x4\n\t"
    "v_mov_b32_dpp v25, v39 row_ror:4 row_mask:0xf bank_mask:0x4\n\t"
    "v_mov_b32_dpp v26, v39 quad_perm:[0,1,2,3] row_mask:0xf bank_mask:0x4\n\t"
    "v_mov_b32_dpp v27, v39 row_ror:12 row_mask:0xf bank_mask:0x4\n\t"
    "v_mov_b32_dpp v28, v39 row_ror:12 row_mask:0xf bank_mask:0x8\n\t"
    "v_mov_b32_dpp v29, v39 row_ror:8 row_mask:0xf bank_mask:0x8\n\t"
    "v_mov_b32_dpp v30, v39 row_ror:4 row_mask:0xf bank_mask:0x8\n\t"
    "v_mov_b32_dpp v31, v39 quad_perm:[0,1,2,3] row_mask:0xf bank_mask:0x8\n\t"
    "v_mfma_f32_16x16x32_bf16 v[44:47], a[64:67], v[4:7], 0\n\t"
    "ds_write_b128 v2, a[64:67] offset:8192\n\t"
    "v_mfma_f32_16x16x32_bf16 v[48:51], a[68:71], v[4:7], 0\n\t"
    "ds_write_b128 v2, a[68:71] offset:9216\n\t"
    "v_mfma_f32_16x16x32_bf16 v[52:55], a[72:75], v[4:7], 0\n\t"
    "ds_write_b128 v2, a[72:75] offset:10240\n\t"
    "s_nop 2\n\t"
    "v_add_f32_dpp v40, v45, v44 row_ror:12 row_mask:0xf bank_mask:0xf\n\t"
    "v_add_f32_dpp v42, v47, v46 row_ror:12 row_mask:0xf bank_mask:0xf\n\t"
    "v_mfma_f32_16x16x32_bf16 v[44:47], a[76:79], v[4:7], 0\n\t"
    "ds_write_b128 v2, a[76:79] offset:11264\n\t"
    "v_add_f32_dpp v41, v50, v49 row_ror:12 row_mask:0xf bank_mask:0xf\n\t"
    "v_add_f32_dpp v43, v48, v51 row_ror:12 row_mask:0xf bank_mask:0xf\n\t"
    "v_add_f32_dpp v35, v42, v40 row_ror:8 row_mask:0xf bank_mask:0x1\n\t"
    "v_mfma_f32_16x16x32_bf16 v[48:51], a[80:83], v[4:7], 0\n\t"
    "ds_write_b128 v2, a[80:83] offset:12288\n\t"
    "s_waitcnt vmcnt(11)\n\t"
    "s_add_u32 s57, s32, 0x3000\n\t"
    "s_xor_b32 s57, s57, s80\n\t"
    "buffer_load_dwordx4 a[164:167], v0, s[40:43], s57 offen offset:0\n\t"
    "v_add_f32_dpp v40, v55, v54 row_ror:12 row_mask:0xf bank_mask:0xf\n\t"
    "v_add_f32_dpp v42, v53, v52 row_ror:12 row_mask:0xf bank_mask:0xf\n\t"
    "v_add_f32_dpp v35, v43, v41 row_ror:8 row_mask:0xf bank_mask:0x2\n\t"
    "v_mfma_f32_16x16x32_bf16 v[52:55], a[84:87], v[4:7], 0\n\t"
    "ds_write_b128 v2, a[84:87] offset:13312\n\t"
    "v_add_f32_dpp v41, v44, v47 row_ror:12 row_mask:0xf bank_mask:0xf\n\t"
    "v_add_f32_dpp v43, v46, v45 row_ror:12 row_mask:0xf bank_mask:0xf\n\t"
    "v_add_f32_dpp v35, v42, v40 row_ror:8 row_mask:0xf bank_mask:0x4\n\t"
    "v_mfma_f32_16x16x32_bf16 v[44:47], a[88:91], v[4:7], 0\n\t"
    "ds_write_b128 v2, a[88:91] offset:14336\n\t"
    "v_add_f32_dpp v40, v49, v48 row_ror:12 row_mask:0xf bank_mask:0xf\n\t"
    "v_add_f32_dpp v42, v51, v50 row_ror:12 row_mask:0xf bank_mask:0xf\n\t"
    "v_add_f32_dpp v35, v43, v41 row_ror:8 row_mask:0xf bank_mask:0x8\n\t"
    "v_mfma_f32_16x16x32_bf16 v[48:51], a[92:95], v[4:7], 0\n\t"
    "ds_write_b128 v2, a[92:95] offset:15360\n\t"
    "v_add_f32_dpp v41, v54, v53 row_ror:12 row_mask:0xf bank_mask:0xf\n\t"
    "v_add_f32_dpp v43, v52, v55 row_ror:12 row_mask:0xf bank_mask:0xf\n\t"
    "v_add_f32_dpp v36, v42, v40 row_ror:8 row_mask:0xf bank_mask:0x1\n\t"
    "v_add_f32_dpp v40, v47, v46 row_ror:12 row_mask:0xf bank_mask:0xf\n\t"
    "v_add_f32_dpp v42, v45, v44 row_ror:12 row_mask:0xf bank_mask:0xf\n\t"
    "v_add_f32_dpp v36, v43, v41 row_ror:8 row_mask:0xf bank_mask:0x2\n\t"
    "s_nop 0\n\t"
    "v_add_f32_dpp v41, v48, v51 row_ror:12 row_mask:0xf bank_mask:0xf\n\t"
    "v_add_f32_dpp v43, v50, v49 row_ror:12 row_mask:0xf bank_mask:0xf\n\t"
    "v_add_f32_dpp v36, v42, v40 row_ror:8 row_mask:0xf bank_mask:0x4\n\t"
    "s_nop 0\n\t"
    "v_add_f32_dpp v36, v43, v41 row_ror:8 row_mask:0xf bank_mask:0x8\n\t"
    "v_perm_b32 v44, v104, v88, s66\n\t"
    "v_perm_b32 v45, v108, v92, s66\n\t"
    "v_perm_b32 v46, v112, v96, s66\n\t"
    "v_perm_b32 v47, v116, v100, s66\n\t"
    "v_perm_b32 v48, v104, v88, s67\n\t"
    "v_perm_b32 v49, v108, v92, s67\n\t"
    "v_perm_b32 v50, v112, v96, s67\n\t"
    "v_perm_b32 v51, v116, v100, s67\n\t"
    "v_max_f32 v37, v35, v36\n\t"
    "v_mfma_f32_16x16x32_bf16 v[8:11], v[16:19], v[44:47], v[8:11]\n\t"
    "ds_write_b128 v2, v[44:47] offset:16384\n\t"
    "v_perm_b32 v52, v105, v89, s66\n\t"
    "v_perm_b32 v53, v109, v93, s66\n\t"
    "v_perm_b32 v54, v113, v97, s66\n\t"
    "v_perm_b32 v55, v117, v101, s66\n\t"
    "v_max_f32_dpp v37, v37, v37 row_ror:4 row_mask:0xf bank_mask:0xf\n\t"
    "v_mfma_f32_16x16x32_bf16 v[8:11], v[20:23], v[48:51], v[8:11]\n\t"
    "ds_write_b128 v2, v[48:51] offset:17408\n\t"
    "v_perm_b32 v44, v105, v89, s67\n\t"
    "v_perm_b32 v45, v109, v93, s67\n\t"
    "v_perm_b32 v46, v113, v97, s67\n\t"
    "v_perm_b32 v47, v117, v101, s67\n\t"
    "v_max_f32_dpp v37, v37, v37 row_ror:8 row_mask:0xf bank_mask:0xf\n\t"
    "v_mfma_f32_16x16x32_bf16 v[8:11], v[24:27], v[52:55], v[8:11]\n\t"
    "ds_write_b128 v2, v[52:55] offset:18432\n\t"
    "v_perm_b32 v48, v106, v90, s66\n\t"
    "v_perm_b32 v49, v110, v94, s66\n\t"
    "v_perm_b32 v50, v114, v98, s66\n\t"
    "v_perm_b32 v51, v118, v102, s66\n\t"
    "v_mov_b32 v38, v37\n\t"
    "v_mfma_f32_16x16x32_bf16 v[8:11], v[28:31], v[44:47], v[8:11]\n\t"
    "ds_write_b128 v2, v[44:47] offset:19456\n\t"
    "v_perm_b32 v52, v106, v90, s67\n\t"
    "v_perm_b32 v53, v110, v94, s67\n\t"
    "v_perm_b32 v54, v114, v98, s67\n\t"
    "v_perm_b32 v55, v118, v102, s67\n\t"
    "v_permlane16_swap_b32 v37, v38\n\t"
    "v_mfma_f32_16x16x32_bf16 v[12:15], v[16:19], v[48:51], v[12:15]\n\t"
    "ds_write_b128 v2, v[48:51] offset:20480\n\t"
    "s_waitcnt vmcnt(11)\n\t"
    "s_add_u32 s57, s32, 0x4000\n\t"
    "s_xor_b32 s57, s57, s80\n\t"
    "buffer_load_dwordx4 a[168:171], v0, s[40:43], s57 offen offset:0\n\t"
    "buffer_load_dwordx4 v[244:247], v225, s[52:55], s72 offen offset:2048 nt\n\t"
    "v_perm_b32 v44, v107, v91, s66\n\t"
    "v_perm_b32 v45, v111, v95, s66\n\t"
    "v_perm_b32 v46, v115, v99, s66\n\t"
    "v_perm_b32 v47, v119, v103, s66\n\t"
    "v_max_f32 v37, v37, v38\n\t"
    "v_mfma_f32_16x16x32_bf16 v[12:15], v[20:23], v[52:55], v[12:15]\n\t"
    "ds_write_b128 v2, v[52:55] offset:21504\n\t"
    "v_perm_b32 v48, v107, v91, s67\n\t"
    "v_perm_b32 v49, v111, v95, s67\n\t"
    "v_perm_b32 v50, v115, v99, s67\n\t"
    "v_perm_b32 v51, v119, v103, s67\n\t"
    "v_mov_b32 v38, v37\n\t"
    "v_mfma_f32_16x16x32_bf16 v[12:15], v[24:27], v[44:47], v[12:15]\n\t"
    "ds_write_b128 v2, v[44:47] offset:22528\n\t"
    "v_permlane32_swap_b32 v37, v38\n\t"
    "v_mfma_f32_16x16x32_bf16 v[12:15], v[28:31], v[48:51], v[12:15]\n\t"
    "ds_write_b128 v2, v[48:51] offset:23552\n\t"
    "v_max_f32 v37, v37, v38\n\t"
    "v_cmp_gt_f32 vcc, v37, v32\n\t"
    "s_cbranch_vccz .Lr6_%=\n\t"
    "v_max_f32 v37, v37, v32\n\t"
    "v_sub_f32 v38, v32, v37\n\t"
    "v_mul_f32 v38, s64, v38\n\t"
    "v_exp_f32 v38, v38\n\t"
    "v_mov_b32 v32, v37\n\t"
    "v_mul_f32 v34, s65, v37\n\t"
    "v_mul_f32 v33, v33, v38\n\t"
    "v_mul_f32_dpp v8, v38, v8 quad_perm:[0,0,0,0] row_mask:0xf bank_mask:0xf\n\t"
    "v_mul_f32_dpp v9, v38, v9 quad_perm:[1,1,1,1] row_mask:0xf bank_mask:0xf\n\t"
    "v_mul_f32_dpp v10, v38, v10 quad_perm:[2,2,2,2] row_mask:0xf bank_mask:0xf\n\t"
    "v_mul_f32_dpp v11, v38, v11 quad_perm:[3,3,3,3] row_mask:0xf bank_mask:0xf\n\t"
    "v_mul_f32_dpp v12, v38, v12 quad_perm:[0,0,0,0] row_mask:0xf bank_mask:0xf\n\t"
    "v_mul_f32_dpp v13, v38, v13 quad_perm:[1,1,1,1] row_mask:0xf bank_mask:0xf\n\t"
    "v_mul_f32_dpp v14, v38, v14 quad_perm:[2,2,2,2] row_mask:0xf bank_mask:0xf\n\t"
    "v_mul_f32_dpp v15, v38, v15 quad_perm:[3,3,3,3] row_mask:0xf bank_mask:0xf\n\t"
    ".Lr6_%=:\n\t"
    "v_fma_f32 v35, v35, s64, v34\n\t"
    "v_fma_f32 v36, v36, s64, v34\n\t"
    "v_exp_f32 v35, v35\n\t"
    "v_exp_f32 v36, v36\n\t"
    "s_nop 0\n\t"
    "v_cvt_pk_bf16_f32 v39, v35, v36\n\t"
    "v_dot2_f32_bf16 v33, v39, s68, v33\n\t"
    "s_nop 0\n\t"
    "v_mov_b32_dpp v16, v39 quad_perm:[0,1,2,3] row_mask:0xf bank_mask:0x1\n\t"
    "v_mov_b32_dpp v17, v39 row_ror:12 row_mask:0xf bank_mask:0x1\n\t"
    "v_mov_b32_dpp v18, v39 row_ror:8 row_mask:0xf bank_mask:0x1\n\t"
    "v_mov_b32_dpp v19, v39 row_ror:4 row_mask:0xf bank_mask:0x1\n\t"
    "v_mov_b32_dpp v20, v39 row_ror:4 row_mask:0xf bank_mask:0x2\n\t"
    "v_mov_b32_dpp v21, v39 quad_perm:[0,1,2,3] row_mask:0xf bank_mask:0x2\n\t"
    "v_mov_b32_dpp v22, v39 row_ror:12 row_mask:0xf bank_mask:0x2\n\t"
    "v_mov_b32_dpp v23, v39 row_ror:8 row_mask:0xf bank_mask:0x2\n\t"
    "v_mov_b32_dpp v24, v39 row_ror:8 row_mask:0xf bank_mask:0x4\n\t"
    "v_mov_b32_dpp v25, v39 row_ror:4 row_mask:0xf bank_mask:0x4\n\t"
    "v_mov_b32_dpp v26, v39 quad_perm:[0,1,2,3] row_mask:0xf bank_mask:0x4\n\t"
    "v_mov_b32_dpp v27, v39 row_ror:12 row_mask:0xf bank_mask:0x4\n\t"
    "v_mov_b32_dpp v28, v39 row_ror:12 row_mask:0xf bank_mask:0x8\n\t"
    "v_mov_b32_dpp v29, v39 row_ror:8 row_mask:0xf bank_mask:0x8\n\t"
    "v_mov_b32_dpp v30, v39 row_ror:4 row_mask:0xf bank_mask:0x8\n\t"
    "v_mov_b32_dpp v31, v39 quad_perm:[0,1,2,3] row_mask:0xf bank_mask:0x8\n\t"
    "v_mfma_f32_16x16x32_bf16 v[44:47], a[96:99], v[4:7], 0\n\t"
    "ds_write_b128 v2, a[96:99] offset:24576\n\t"
    "v_mfma_f32_16x16x32_bf16 v[48:51], a[100:103], v[4:7], 0\n\t"
    "ds_write_b128 v2, a[100:103] offset:25600\n\t"
    "v_mfma_f32_16x16x32_bf16 v[52:55], v[120:123], v[4:7], 0\n\t"
    "s_nop 3\n\t"
    "v_add_f32_dpp v40, v45, v44 row_ror:12 row_mask:0xf bank_mask:0xf\n\t"
    "v_add_f32_dpp v42, v47, v46 row_ror:12 row_mask:0xf bank_mask:0xf\n\t"
    "v_mfma_f32_16x16x32_bf16 v[44:47], v[124:127], v[4:7], 0\n\t"
    "s_waitcnt vmcnt(12)\n\t"
    "s_add_u32 s57, s32, 0x5000\n\t"
    "s_xor_b32 s57, s57, s80\n\t"
    "buffer_load_dwordx4 a[172:175], v0, s[40:43], s57 offen offset:0\n\t"
    "v_add_f32_dpp v41, v50, v49 row_ror:12 row_mask:0xf bank_mask:0xf\n\t"
    "v_add_f32_dpp v43, v48, v51 row_ror:12 row_mask:0xf bank_mask:0xf\n\t"
    "v_add_f32_dpp v35, v42, v40 row_ror:8 row_mask:0xf bank_mask:0x1\n\t"
    "v_mfma_f32_16x16x32_bf16 v[48:51], a[104:107], v[4:7], 0\n\t"
    "v_add_f32_dpp v40, v55, v54 row_ror:12 row_mask:0xf bank_mask:0xf\n\t"
    "v_add_f32_dpp v42, v53, v52 row_ror:12 row_mask:0xf bank_mask:0xf\n\t"
    "v_add_f32_dpp v35, v43, v41 row_ror:8 row_mask:0xf bank_mask:0x2\n\t"
    "v_mfma_f32_16x16x32_bf16 v[52:55], a[108:111], v[4:7], 0\n\t"
    "v_add_f32_dpp v41, v44, v47 row_ror:12 row_mask:0xf bank_mask:0xf\n\t"
    "v_add_f32_dpp v43, v46, v45 row_ror:12 row_mask:0xf bank_mask:0xf\n\t"
    "v_add_f32_dpp v35, v42, v40 row_ror:8 row_mask:0xf bank_mask:0x4\n\t"
    "v_mfma_f32_16x16x32_bf16 v[44:47], a[112:115], v[4:7], 0\n\t"
    "v_add_f32_dpp v40, v49, v48 row_ror:12 row_mask:0xf bank_mask:0xf\n\t"
    "v_add_f32_dpp v42, v51, v50 row_ror:12 row_mask:0xf bank_mask:0xf\n\t"
    "v_add_f32_dpp v35, v43, v41 row_ror:8 row_mask:0xf bank_mask:0x8\n\t"
    "v_mfma_f32_16x16x32_bf16 v[48:51], a[116:119], v[4:7], 0\n\t"
    "v_add_f32_dpp v41, v54, v53 row_ror:12 row_mask:0xf bank_mask:0xf\n\t"
    "v_add_f32_dpp v43, v52, v55 row_ror:12 row_mask:0xf bank_mask:0xf\n\t"
    "v_add_f32_dpp v36, v42, v40 row_ror:8 row_mask:0xf bank_mask:0x1\n\t"
    "s_nop 0\n\t"
    "v_add_f32_dpp v40, v47, v46 row_ror:12 row_mask:0xf bank_mask:0xf\n\t"
    "v_add_f32_dpp v42, v45, v44 row_ror:12 row_mask:0xf bank_mask:0xf\n\t"
    "v_add_f32_dpp v36, v43, v41 row_ror:8 row_mask:0xf bank_mask:0x2\n\t"
    "s_nop 0\n\t"
    "v_add_f32_dpp v41, v48, v51 row_ror:12 row_mask:0xf bank_mask:0xf\n\t"
    "v_add_f32_dpp v43, v50, v49 row_ror:12 row_mask:0xf bank_mask:0xf\n\t"
    "v_add_f32_dpp v36, v42, v40 row_ror:8 row_mask:0xf bank_mask:0x4\n\t"
    "s_nop 0\n\t"
    "v_add_f32_dpp v36, v43, v41 row_ror:8 row_mask:0xf bank_mask:0x8\n\t"
    "v_perm_b32 v44, v144, v128, s66\n\t"
    "v_perm_b32 v45, v148, v132, s66\n\t"
    "v_perm_b32 v46, v152, v136, s66\n\t"
    "v_perm_b32 v47, v156, v140, s66\n\t"
    "v_perm_b32 v48, v144, v128, s67\n\t"
    "v_perm_b32 v49, v148, v132, s67\n\t"
    "v_perm_b32 v50, v152, v136, s67\n\t"
    "v_perm_b32 v51, v156, v140, s67\n\t"
    "v_max_f32 v37, v35, v36\n\t"
    "v_mfma_f32_16x16x32_bf16 v[8:11], v[16:19], v[44:47], v[8:11]\n\t"
    "v_perm_b32 v52, v145, v129, s66\n\t"
    "v_perm_b32 v53, v149, v133, s66\n\t"
    "v_perm_b32 v54, v153, v137, s66\n\t"
    "v_perm_b32 v55, v157, v141, s66\n\t"
    "v_max_f32_dpp v37, v37, v37 row_ror:4 row_mask:0xf bank_mask:0xf\n\t"
    "v_mfma_f32_16x16x32_bf16 v[8:11], v[20:23], v[48:51], v[8:11]\n\t"
    "v_perm_b32 v44, v145, v129, s67\n\t"
    "v_perm_b32 v45, v149, v133, s67\n\t"
    "v_perm_b32 v46, v153, v137, s67\n\t"
    "v_perm_b32 v47, v157, v141, s67\n\t"
    "v_max_f32_dpp v37, v37, v37 row_ror:8 row_mask:0xf bank_mask:0xf\n\t"
    "v_mfma_f32_16x16x32_bf16 v[8:11], v[24:27], v[52:55], v[8:11]\n\t"
    "v_perm_b32 v48, v146, v130, s66\n\t"
    "v_perm_b32 v49, v150, v134, s66\n\t"
    "v_perm_b32 v50, v154, v138, s66\n\t"
    "v_perm_b32 v51, v158, v142, s66\n\t"
    "v_mov_b32 v38, v37\n\t"
    "v_mfma_f32_16x16x32_bf16 v[8:11], v[28:31], v[44:47], v[8:11]\n\t"
    "v_perm_b32 v52, v146, v130, s67\n\t"
    "v_perm_b32 v53, v150, v134, s67\n\t"
    "v_perm_b32 v54, v154, v138, s67\n\t"
    "v_perm_b32 v55, v158, v142, s67\n\t"
    "v_permlane16_swap_b32 v37, v38\n\t"
    "v_mfma_f32_16x16x32_bf16 v[12:15], v[16:19], v[48:51], v[12:15]\n\t"
    "v_perm_b32 v44, v147, v131, s66\n\t"
    "v_perm_b32 v45, v151, v135, s66\n\t"
    "v_perm_b32 v46, v155, v139, s66\n\t"
    "v_perm_b32 v47, v159, v143, s66\n\t"
    "v_max_f32 v37, v37, v38\n\t"
    "v_mfma_f32_16x16x32_bf16 v[12:15], v[20:23], v[52:55], v[12:15]\n\t"
    "s_waitcnt vmcnt(12)\n\t"
    "s_add_u32 s57, s32, 0x6000\n\t"
    "s_xor_b32 s57, s57, s80\n\t"
    "buffer_load_dwordx4 a[176:179], v0, s[40:43], s57 offen offset:0\n\t"
    "buffer_load_dwordx4 v[240:243], v225, s[52:55], s72 offen offset:3072 nt\n\t"
    "v_perm_b32 v48, v147, v131, s67\n\t"
    "v_perm_b32 v49, v151, v135, s67\n\t"
    "v_perm_b32 v50, v155, v139, s67\n\t"
    "v_perm_b32 v51, v159, v143, s67\n\t"
    "v_mov_b32 v38, v37\n\t"
    "v_mfma_f32_16x16x32_bf16 v[12:15], v[24:27], v[44:47], v[12:15]\n\t"
    "s_nop 0\n\t"
    "v_permlane32_swap_b32 v37, v38\n\t"
    "v_mfma_f32_16x16x32_bf16 v[12:15], v[28:31], v[48:51], v[12:15]\n\t"
    "v_max_f32 v37, v37, v38\n\t"
    "v_cmp_gt_f32 vcc, v37, v32\n\t"
    "s_cbranch_vccz .Lr7_%=\n\t"
    "v_max_f32 v37, v37, v32\n\t"
    "v_sub_f32 v38, v32, v37\n\t"
    "v_mul_f32 v38, s64, v38\n\t"
    "v_exp_f32 v38, v38\n\t"
    "v_mov_b32 v32, v37\n\t"
    "v_mul_f32 v34, s65, v37\n\t"
    "v_mul_f32 v33, v33, v38\n\t"
    "v_mul_f32_dpp v8, v38, v8 quad_perm:[0,0,0,0] row_mask:0xf bank_mask:0xf\n\t"
    "v_mul_f32_dpp v9, v38, v9 quad_perm:[1,1,1,1] row_mask:0xf bank_mask:0xf\n\t"
    "v_mul_f32_dpp v10, v38, v10 quad_perm:[2,2,2,2] row_mask:0xf bank_mask:0xf\n\t"
    "v_mul_f32_dpp v11, v38, v11 quad_perm:[3,3,3,3] row_mask:0xf bank_mask:0xf\n\t"
    "v_mul_f32_dpp v12, v38, v12 quad_perm:[0,0,0,0] row_mask:0xf bank_mask:0xf\n\t"
    "v_mul_f32_dpp v13, v38, v13 quad_perm:[1,1,1,1] row_mask:0xf bank_mask:0xf\n\t"
    "v_mul_f32_dpp v14, v38, v14 quad_perm:[2,2,2,2] row_mask:0xf bank_mask:0xf\n\t"
    "v_mul_f32_dpp v15, v38, v15 quad_perm:[3,3,3,3] row_mask:0xf bank_mask:0xf\n\t"
    ".Lr7_%=:\n\t"
    "v_fma_f32 v35, v35, s64, v34\n\t"
    "v_fma_f32 v36, v36, s64, v34\n\t"
    "v_exp_f32 v35, v35\n\t"
    "v_exp_f32 v36, v36\n\t"
    "s_nop 0\n\t"
    "v_cvt_pk_bf16_f32 v39, v35, v36\n\t"
    "v_dot2_f32_bf16 v33, v39, s68, v33\n\t"
    "s_nop 0\n\t"
    "v_mov_b32_dpp v16, v39 quad_perm:[0,1,2,3] row_mask:0xf bank_mask:0x1\n\t"
    "v_mov_b32_dpp v17, v39 row_ror:12 row_mask:0xf bank_mask:0x1\n\t"
    "v_mov_b32_dpp v18, v39 row_ror:8 row_mask:0xf bank_mask:0x1\n\t"
    "v_mov_b32_dpp v19, v39 row_ror:4 row_mask:0xf bank_mask:0x1\n\t"
    "v_mov_b32_dpp v20, v39 row_ror:4 row_mask:0xf bank_mask:0x2\n\t"
    "v_mov_b32_dpp v21, v39 quad_perm:[0,1,2,3] row_mask:0xf bank_mask:0x2\n\t"
    "v_mov_b32_dpp v22, v39 row_ror:12 row_mask:0xf bank_mask:0x2\n\t"
    "v_mov_b32_dpp v23, v39 row_ror:8 row_mask:0xf bank_mask:0x2\n\t"
    "v_mov_b32_dpp v24, v39 row_ror:8 row_mask:0xf bank_mask:0x4\n\t"
    "v_mov_b32_dpp v25, v39 row_ror:4 row_mask:0xf bank_mask:0x4\n\t"
    "v_mov_b32_dpp v26, v39 quad_perm:[0,1,2,3] row_mask:0xf bank_mask:0x4\n\t"
    "v_mov_b32_dpp v27, v39 row_ror:12 row_mask:0xf bank_mask:0x4\n\t"
    "v_mov_b32_dpp v28, v39 row_ror:12 row_mask:0xf bank_mask:0x8\n\t"
    "v_mov_b32_dpp v29, v39 row_ror:8 row_mask:0xf bank_mask:0x8\n\t"
    "v_mov_b32_dpp v30, v39 row_ror:4 row_mask:0xf bank_mask:0x8\n\t"
    "v_mov_b32_dpp v31, v39 quad_perm:[0,1,2,3] row_mask:0xf bank_mask:0x8\n\t"
    "v_mfma_f32_16x16x32_bf16 v[44:47], a[120:123], v[4:7], 0\n\t"
    "v_mfma_f32_16x16x32_bf16 v[48:51], a[124:127], v[4:7], 0\n\t"
    "v_mfma_f32_16x16x32_bf16 v[52:55], a[128:131], v[4:7], 0\n\t"
    "s_nop 5\n\t"
    "v_add_f32_dpp v40, v45, v44 row_ror:12 row_mask:0xf bank_mask:0xf\n\t"
    "v_add_f32_dpp v42, v47, v46 row_ror:12 row_mask:0xf bank_mask:0xf\n\t"
    "v_mfma_f32_16x16x32_bf16 v[44:47], a[132:135], v[4:7], 0\n\t"
    "v_add_f32_dpp v41, v50, v49 row_ror:12 row_mask:0xf bank_mask:0xf\n\t"
    "v_add_f32_dpp v43, v48, v51 row_ror:12 row_mask:0xf bank_mask:0xf\n\t"
    "v_add_f32_dpp v35, v42, v40 row_ror:8 row_mask:0xf bank_mask:0x1\n\t"
    "v_mfma_f32_16x16x32_bf16 v[48:51], a[136:139], v[4:7], 0\n\t"
    "v_add_f32_dpp v40, v55, v54 row_ror:12 row_mask:0xf bank_mask:0xf\n\t"
    "v_add_f32_dpp v42, v53, v52 row_ror:12 row_mask:0xf bank_mask:0xf\n\t"
    "v_add_f32_dpp v35, v43, v41 row_ror:8 row_mask:0xf bank_mask:0x2\n\t"
    "v_mfma_f32_16x16x32_bf16 v[52:55], a[140:143], v[4:7], 0\n\t"
    "v_add_f32_dpp v41, v44, v47 row_ror:12 row_mask:0xf bank_mask:0xf\n\t"
    "v_add_f32_dpp v43, v46, v45 row_ror:12 row_mask:0xf bank_mask:0xf\n\t"
    "v_add_f32_dpp v35, v42, v40 row_ror:8 row_mask:0xf bank_mask:0x4\n\t"
    "v_mfma_f32_16x16x32_bf16 v[44:47], a[144:147], v[4:7], 0\n\t"
    "v_add_f32_dpp v40, v49, v48 row_ror:12 row_mask:0xf bank_mask:0xf\n\t"
    "v_add_f32_dpp v42, v51, v50 row_ror:12 row_mask:0xf bank_mask:0xf\n\t"
    "v_add_f32_dpp v35, v43, v41 row_ror:8 row_mask:0xf bank_mask:0x8\n\t"
    "v_mfma_f32_16x16x32_bf16 v[48:51], a[148:151], v[4:7], 0\n\t"
    "s_waitcnt vmcnt(13)\n\t"
    "s_add_u32 s57, s32, 0x7000\n\t"
    "s_xor_b32 s57, s57, s80\n\t"
    "buffer_load_dwordx4 a[180:183], v0, s[40:43], s57 offen offset:0\n\t"
    "v_add_f32_dpp v41, v54, v53 row_ror:12 row_mask:0xf bank_mask:0xf\n\t"
    "v_add_f32_dpp v43, v52, v55 row_ror:12 row_mask:0xf bank_mask:0xf\n\t"
    "v_add_f32_dpp v36, v42, v40 row_ror:8 row_mask:0xf bank_mask:0x1\n\t"
    "v_add_f32_dpp v40, v47, v46 row_ror:12 row_mask:0xf bank_mask:0xf\n\t"
    "v_add_f32_dpp v42, v45, v44 row_ror:12 row_mask:0xf bank_mask:0xf\n\t"
    "v_add_f32_dpp v36, v43, v41 row_ror:8 row_mask:0xf bank_mask:0x2\n\t"
    "v_add_f32_dpp v41, v48, v51 row_ror:12 row_mask:0xf bank_mask:0xf\n\t"
    "v_add_f32_dpp v43, v50, v49 row_ror:12 row_mask:0xf bank_mask:0xf\n\t"
    "v_add_f32_dpp v36, v42, v40 row_ror:8 row_mask:0xf bank_mask:0x4\n\t"
    "s_nop 0\n\t"
    "v_add_f32_dpp v36, v43, v41 row_ror:8 row_mask:0xf bank_mask:0x8\n\t"
    "s_waitcnt vmcnt(13)\n\t"
    "s_add_u32 s33, s34, 0x20000\n\t"
    "s_and_b32 s33, s33, 0x7ffff\n\t"
    "s_add_u32 s33, s33, s56\n\t"
    "s_add_u32 s57, s33, 0\n\t"
    "s_xor_b32 s57, s57, s80\n\t"
    "buffer_load_dwordx4 v[192:195], v1, s[44:47], s57 offen offset:0\n\t"
    "s_add_u32 s72, s72, 0x1000\n\t"
    "buffer_load_dwordx4 v[220:223], v225, s[52:55], s72 offen nt\n\t"
    "s_waitcnt vmcnt(14)\n\t"
    "s_add_u32 s57, s33, 0x1000\n\t"
    "s_xor_b32 s57, s57, s80\n\t"
    "buffer_load_dwordx4 v[196:199], v1, s[44:47], s57 offen offset:0\n\t"
    "v_perm_b32 v44, v176, v160, s66\n\t"
    "v_perm_b32 v45, v180, v164, s66\n\t"
    "v_perm_b32 v46, v184, v168, s66\n\t"
    "v_perm_b32 v47, v188, v172, s66\n\t"
    "v_perm_b32 v48, v176, v160, s67\n\t"
    "v_perm_b32 v49, v180, v164, s67\n\t"
    "v_perm_b32 v50, v184, v168, s67\n\t"
    "v_perm_b32 v51, v188, v172, s67\n\t"
    "v_max_f32 v37, v35, v36\n\t"
    "v_mfma_f32_16x16x32_bf16 v[8:11], v[16:19], v[44:47], v[8:11]\n\t"
    "v_perm_b32 v52, v177, v161, s66\n\t"
    "v_perm_b32 v53, v181, v165, s66\n\t"
    "v_perm_b32 v54, v185, v169, s66\n\t"
    "v_perm_b32 v55, v189, v173, s66\n\t"
    "v_max_f32_dpp v37, v37, v37 row_ror:4 row_mask:0xf bank_mask:0xf\n\t"
    "v_mfma_f32_16x16x32_bf16 v[8:11], v[20:23], v[48:51], v[8:11]\n\t"
    "v_perm_b32 v44, v177, v161, s67\n\t"
    "v_perm_b32 v45, v181, v165, s67\n\t"
    "v_perm_b32 v46, v185, v169, s67\n\t"
    "v_perm_b32 v47, v189, v173, s67\n\t"
    "v_max_f32_dpp v37, v37, v37 row_ror:8 row_mask:0xf bank_mask:0xf\n\t"
    "v_mfma_f32_16x16x32_bf16 v[8:11], v[24:27], v[52:55], v[8:11]\n\t"
    "v_perm_b32 v48, v178, v162, s66\n\t"
    "v_perm_b32 v49, v182, v166, s66\n\t"
    "v_perm_b32 v50, v186, v170, s66\n\t"
    "v_perm_b32 v51, v190, v174, s66\n\t"
    "v_mov_b32 v38, v37\n\t"
    "v_mfma_f32_16x16x32_bf16 v[8:11], v[28:31], v[44:47], v[8:11]\n\t"
    "v_perm_b32 v52, v178, v162, s67\n\t"
    "v_perm_b32 v53, v182, v166, s67\n\t"
    "v_perm_b32 v54, v186, v170, s67\n\t"
    "v_perm_b32 v55, v190, v174, s67\n\t"
    "v_permlane16_swap_b32 v37, v38\n\t"
    "v_mfma_f32_16x16x32_bf16 v[12:15], v[16:19], v[48:51], v[12:15]\n\t"
    "v_perm_b32 v44, v179, v163, s66\n\t"
    "v_perm_b32 v45, v183, v167, s66\n\t"
    "v_perm_b32 v46, v187, v171, s66\n\t"
    "v_perm_b32 v47, v191, v175, s66\n\t"
    "v_max_f32 v37, v37, v38\n\t"
    "v_mfma_f32_16x16x32_bf16 v[12:15], v[20:23], v[52:55], v[12:15]\n\t"
    "v_perm_b32 v48, v179, v163, s67\n\t"
    "v_perm_b32 v49, v183, v167, s67\n\t"
    "v_perm_b32 v50, v187, v171, s67\n\t"
    "v_perm_b32 v51, v191, v175, s67\n\t"
    "v_mov_b32 v38, v37\n\t"
    "v_mfma_f32_16x16x32_bf16 v[12:15], v[24:27], v[44:47], v[12:15]\n\t"
    "s_nop 0\n\t"
    "v_permlane32_swap_b32 v37, v38\n\t"
    "v_mfma_f32_16x16x32_bf16 v[12:15], v[28:31], v[48:51], v[12:15]\n\t"
    "v_max_f32 v37, v37, v38\n\t"
    "v_cmp_gt_f32 vcc, v37, v32\n\t"
    "s_cbranch_vccz .Lr8_%=\n\t"
    "v_max_f32 v37, v37, v32\n\t"
    "v_sub_f32 v38, v32, v37\n\t"
    "v_mul_f32 v38, s64, v38\n\t"
    "v_exp_f32 v38, v38\n\t"
    "v_mov_b32 v32, v37\n\t"
    "v_mul_f32 v34, s65, v37\n\t"
    "v_mul_f32 v33, v33, v38\n\t"
    "v_mul_f32_dpp v8, v38, v8 quad_perm:[0,0,0,0] row_mask:0xf bank_mask:0xf\n\t"
    "v_mul_f32_dpp v9, v38, v9 quad_perm:[1,1,1,1] row_mask:0xf bank_mask:0xf\n\t"
    "v_mul_f32_dpp v10, v38, v10 quad_perm:[2,2,2,2] row_mask:0xf bank_mask:0xf\n\t"
    "v_mul_f32_dpp v11, v38, v11 quad_perm:[3,3,3,3] row_mask:0xf bank_mask:0xf\n\t"
    "v_mul_f32_dpp v12, v38, v12 quad_perm:[0,0,0,0] row_mask:0xf bank_mask:0xf\n\t"
    "v_mul_f32_dpp v13, v38, v13 quad_perm:[1,1,1,1] row_mask:0xf bank_mask:0xf\n\t"
    "v_mul_f32_dpp v14, v38, v14 quad_perm:[2,2,2,2] row_mask:0xf bank_mask:0xf\n\t"
    "v_mul_f32_dpp v15, v38, v15 quad_perm:[3,3,3,3] row_mask:0xf bank_mask:0xf\n\t"
    ".Lr8_%=:\n\t"
    "v_fma_f32 v35, v35, s64, v34\n\t"
    "v_fma_f32 v36, v36, s64, v34\n\t"
    "v_exp_f32 v35, v35\n\t"
    "v_exp_f32 v36, v36\n\t"
    "s_nop 0\n\t"
    "v_cvt_pk_bf16_f32 v39, v35, v36\n\t"
    "v_dot2_f32_bf16 v33, v39, s68, v33\n\t"
    "s_nop 0\n\t"
    "v_mov_b32_dpp v16, v39 quad_perm:[0,1,2,3] row_mask:0xf bank_mask:0x1\n\t"
    "v_mov_b32_dpp v17, v39 row_ror:12 row_mask:0xf bank_mask:0x1\n\t"
    "v_mov_b32_dpp v18, v39 row_ror:8 row_mask:0xf bank_mask:0x1\n\t"
    "v_mov_b32_dpp v19, v39 row_ror:4 row_mask:0xf bank_mask:0x1\n\t"
    "v_mov_b32_dpp v20, v39 row_ror:4 row_mask:0xf bank_mask:0x2\n\t"
    "v_mov_b32_dpp v21, v39 quad_perm:[0,1,2,3] row_mask:0xf bank_mask:0x2\n\t"
    "v_mov_b32_dpp v22, v39 row_ror:12 row_mask:0xf bank_mask:0x2\n\t"
    "v_mov_b32_dpp v23, v39 row_ror:8 row_mask:0xf bank_mask:0x2\n\t"
    "s_waitcnt vmcnt(14)\n\t"
    "s_add_u32 s57, s33, 0x2000\n\t"
    "s_xor_b32 s57, s57, s80\n\t"
    "buffer_load_dwordx4 v[200:203], v1, s[44:47], s57 offen offset:0\n\t"
    "buffer_load_dwordx4 v[216:219], v225, s[52:55], s72 offen offset:1024 nt\n\t"
    "v_mov_b32_dpp v24, v39 row_ror:8 row_mask:0xf bank_mask:0x4\n\t"
    "v_mov_b32_dpp v25, v39 row_ror:4 row_mask:0xf bank_mask:0x4\n\t"
    "v_mov_b32_dpp v26, v39 quad_perm:[0,1,2,3] row_mask:0xf bank_mask:0x4\n\t"
    "v_mov_b32_dpp v27, v39 row_ror:12 row_mask:0xf bank_mask:0x4\n\t"
    "v_mov_b32_dpp v28, v39 row_ror:12 row_mask:0xf bank_mask:0x8\n\t"
    "v_mov_b32_dpp v29, v39 row_ror:8 row_mask:0xf bank_mask:0x8\n\t"
    "v_mov_b32_dpp v30, v39 row_ror:4 row_mask:0xf bank_mask:0x8\n\t"
    "v_mov_b32_dpp v31, v39 quad_perm:[0,1,2,3] row_mask:0xf bank_mask:0x8\n\t"
    "v_mfma_f32_16x16x32_bf16 v[44:47], a[152:155], v[4:7], 0\n\t"
    "s_waitcnt vmcnt(14)\n\t"
    "s_add_u32 s57, s33, 0x3000\n\t"
    "s_xor_b32 s57, s57, s80\n\t"
    "buffer_load_dwordx4 v[204:207], v1, s[44:47], s57 offen offset:0\n\t"
    "v_mfma_f32_16x16x32_bf16 v[48:51], a[156:159], v[4:7], 0\n\t"
    "s_waitcnt vmcnt(14)\n\t"
    "s_add_u32 s57, s33, 0x4000\n\t"
    "s_xor_b32 s57, s57, s80\n\t"
    "buffer_load_dwordx4 v[56:59], v1, s[44:47], s57 offen offset:0\n\t"
    "buffer_load_dwordx4 v[212:215], v225, s[52:55], s72 offen offset:2048 nt\n\t"
    "v_mfma_f32_16x16x32_bf16 v[52:55], a[160:163], v[4:7], 0\n\t"
    "v_add_f32_dpp v40, v45, v44 row_ror:12 row_mask:0xf bank_mask:0xf\n\t"
    "v_add_f32_dpp v42, v47, v46 row_ror:12 row_mask:0xf bank_mask:0xf\n\t"
    "s_waitcnt vmcnt(14)\n\t"
    "s_add_u32 s57, s33, 0x5000\n\t"
    "s_xor_b32 s57, s57, s80\n\t"
    "buffer_load_dwordx4 v[60:63], v1, s[44:47], s57 offen offset:0\n\t"
    "v_mfma_f32_16x16x32_bf16 v[44:47], a[164:167], v[4:7], 0\n\t"
    "v_add_f32_dpp v41, v50, v49 row_ror:12 row_mask:0xf bank_mask:0xf\n\t"
    "v_add_f32_dpp v43, v48, v51 row_ror:12 row_mask:0xf bank_mask:0xf\n\t"
    "v_add_f32_dpp v35, v42, v40 row_ror:8 row_mask:0xf bank_mask:0x1\n\t"
    "s_waitcnt vmcnt(14)\n\t"
    "s_add_u32 s57, s33, 0x6000\n\t"
    "s_xor_b32 s57, s57, s80\n\t"
    "buffer_load_dwordx4 v[64:67], v1, s[44:47], s57 offen offset:0\n\t"
    "buffer_load_dwordx4 v[208:211], v225, s[52:55], s72 offen offset:3072 nt\n\t"
    "v_mfma_f32_16x16x32_bf16 v[48:51], a[168:171], v[4:7], 0\n\t"
    "v_add_f32_dpp v40, v55, v54 row_ror:12 row_mask:0xf bank_mask:0xf\n\t"
    "v_add_f32_dpp v42, v53, v52 row_ror:12 row_mask:0xf bank_mask:0xf\n\t"
    "v_add_f32_dpp v35, v43, v41 row_ror:8 row_mask:0xf bank_mask:0x2\n\t"
    "s_waitcnt vmcnt(14)\n\t"
    "s_add_u32 s57, s33, 0x7000\n\t"
    "s_xor_b32 s57, s57, s80\n\t"
    "buffer_load_dwordx4 v[68:71], v1, s[44:47], s57 offen offset:0\n\t"
    "v_mfma_f32_16x16x32_bf16 v[52:55], a[172:175], v[4:7], 0\n\t"
    "v_add_f32_dpp v41, v44, v47 row_ror:12 row_mask:0xf bank_mask:0xf\n\t"
    "v_add_f32_dpp v43, v46, v45 row_ror:12 row_mask:0xf bank_mask:0xf\n\t"
    "v_add_f32_dpp v35, v42, v40 row_ror:8 row_mask:0xf bank_mask:0x4\n\t"
    "s_waitcnt vmcnt(14)\n\t"
    "s_add_u32 s32, s34, 0x30000\n\t"
    "s_and_b32 s32, s32, 0x7ffff\n\t"
    "s_add_u32 s32, s32, s56\n\t"
    "s_add_u32 s57, s32, 0\n\t"
    "s_xor_b32 s57, s57, s80\n\t"
    "buffer_load_dwordx4 a[184:187], v0, s[40:43], s57 offen offset:0\n\t"
    "v_mfma_f32_16x16x32_bf16 v[44:47], a[176:179], v[4:7], 0\n\t"
    "v_add_f32_dpp v40, v49, v48 row_ror:12 row_mask:0xf bank_mask:0xf\n\t"
    "v_add_f32_dpp v42, v51, v50 row_ror:12 row_mask:0xf bank_mask:0xf\n\t"
    "v_add_f32_dpp v35, v43, v41 row_ror:8 row_mask:0xf bank_mask:0x8\n\t"
    "s_waitcnt vmcnt(13)\n\t"
    "s_add_u32 s57, s32, 0x1000\n\t"
    "s_xor_b32 s57, s57, s80\n\t"
    "buffer_load_dwordx4 a[188:191], v0, s[40:43], s57 offen offset:0\n\t"
    "v_mfma_f32_16x16x32_bf16 v[48:51], a[180:183], v[4:7], 0\n\t"
    "v_add_f32_dpp v41, v54, v53 row_ror:12 row_mask:0xf bank_mask:0xf\n\t"
    "v_add_f32_dpp v43, v52, v55 row_ror:12 row_mask:0xf bank_mask:0xf\n\t"
    "v_add_f32_dpp v36, v42, v40 row_ror:8 row_mask:0xf bank_mask:0x1\n\t"
    "v_add_f32_dpp v40, v47, v46 row_ror:12 row_mask:0xf bank_mask:0xf\n\t"
    "v_add_f32_dpp v42, v45, v44 row_ror:12 row_mask:0xf bank_mask:0xf\n\t"
    "v_add_f32_dpp v36, v43, v41 row_ror:8 row_mask:0xf bank_mask:0x2\n\t"
    "s_nop 1\n\t"
    "v_add_f32_dpp v41, v48, v51 row_ror:12 row_mask:0xf bank_mask:0xf\n\t"
    "v_add_f32_dpp v43, v50, v49 row_ror:12 row_mask:0xf bank_mask:0xf\n\t"
    "v_add_f32_dpp v36, v42, v40 row_ror:8 row_mask:0xf bank_mask:0x4\n\t"
    "s_nop 0\n\t"
    "v_add_f32_dpp v36, v43, v41 row_ror:8 row_mask:0xf bank_mask:0x8\n\t"
    "s_waitcnt vmcnt(13)\n\t"
    "s_add_u32 s57, s32, 0x2000\n\t"
    "s_xor_b32 s57, s57, s80\n\t"
    "buffer_load_dwordx4 a[192:195], v0, s[40:43], s57 offen offset:0\n\t"
    "s_waitcnt vmcnt(12)\n\t"
    "s_add_u32 s57, s32, 0x3000\n\t"
    "s_xor_b32 s57, s57, s80\n\t"
    "buffer_load_dwordx4 a[196:199], v0, s[40:43], s57 offen offset:0\n\t"
    "s_waitcnt vmcnt(12)\n\t"
    "s_add_u32 s57, s32, 0x4000\n\t"
    "s_xor_b32 s57, s57, s80\n\t"
    "buffer_load_dwordx4 a[200:203], v0, s[40:43], s57 offen offset:0\n\t"
    "s_waitcnt vmcnt(11)\n\t"
    "s_add_u32 s57, s32, 0x5000\n\t"
    "s_xor_b32 s57, s57, s80\n\t"
    "buffer_load_dwordx4 a[204:207], v0, s[40:43], s57 offen offset:0\n\t"
    "s_waitcnt vmcnt(11)\n\t"
    "s_add_u32 s57, s32, 0x6000\n\t"
    "s_xor_b32 s57, s57, s80\n\t"
    "buffer_load_dwordx4 a[208:211], v0, s[40:43], s57 offen offset:0\n\t"
    "s_waitcnt vmcnt(10)\n\t"
    "s_add_u32 s57, s32, 0x7000\n\t"
    "s_xor_b32 s57, s57, s80\n\t"
    "buffer_load_dwordx4 a[212:215], v0, s[40:43], s57 offen offset:0\n\t"
    "s_waitcnt vmcnt(10)\n\t"
    "s_add_u32 s33, s34, 0x28000\n\t"
    "s_and_b32 s33, s33, 0x7ffff\n\t"
    "s_add_u32 s33, s33, s56\n\t"
    "s_add_u32 s57, s33, 0\n\t"
    "s_xor_b32 s57, s57, s80\n\t"
    "buffer_load_dwordx4 v[72:75], v1, s[44:47], s57 offen offset:0\n\t"
    "s_waitcnt vmcnt(9)\n\t"
    "s_add_u32 s57, s33, 0x1000\n\t"
    "s_xor_b32 s57, s57, s80\n\t"
    "buffer_load_dwordx4 v[76:79], v1, s[44:47], s57 offen offset:0\n\t"
    "v_perm_b32 v236, v248, v252, s66\n\t"
    "v_perm_b32 v237, v240, v244, s66\n\t"
    "v_perm_b32 v238, v216, v220, s66\n\t"
    "v_perm_b32 v239, v208, v212, s66\n\t"
    "buffer_store_dwordx4 v[236:239], v226, s[48:51], s78 offen sc1\n\t"
    "v_perm_b32 v44, v56, v192, s66\n\t"
    "v_perm_b32 v45, v60, v196, s66\n\t"
    "v_perm_b32 v46, v64, v200, s66\n\t"
    "v_perm_b32 v47, v68, v204, s66\n\t"
    "v_perm_b32 v48, v56, v192, s67\n\t"
    "v_perm_b32 v49, v60, v196, s67\n\t"
    "v_perm_b32 v50, v64, v200, s67\n\t"
    "v_perm_b32 v51, v68, v204, s67\n\t"
    "v_max_f32 v37, v35, v36\n\t"
    "v_mfma_f32_16x16x32_bf16 v[8:11], v[16:19], v[44:47], v[8:11]\n\t"
    "v_perm_b32 v52, v57, v193, s66\n\t"
    "v_perm_b32 v53, v61, v197, s66\n\t"
    "v_perm_b32 v54, v65, v201, s66\n\t"
    "v_perm_b32 v55, v69, v205, s66\n\t"
    "v_max_f32_dpp v37, v37, v37 row_ror:4 row_mask:0xf bank_mask:0xf\n\t"
    "v_mfma_f32_16x16x32_bf16 v[8:11], v[20:23], v[48:51], v[8:11]\n\t"
    "v_perm_b32 v44, v57, v193, s67\n\t"
    "v_perm_b32 v45, v61, v197, s67\n\t"
    "v_perm_b32 v46, v65, v201, s67\n\t"
    "v_perm_b32 v47, v69, v205, s67\n\t"
    "v_max_f32_dpp v37, v37, v37 row_ror:8 row_mask:0xf bank_mask:0xf\n\t"
    "v_mfma_f32_16x16x32_bf16 v[8:11], v[24:27], v[52:55], v[8:11]\n\t"
    "v_perm_b32 v48, v58, v194, s66\n\t"
    "v_perm_b32 v49, v62, v198, s66\n\t"
    "v_perm_b32 v50, v66, v202, s66\n\t"
    "v_perm_b32 v51, v70, v206, s66\n\t"
    "v_mov_b32 v38, v37\n\t"
    "v_mfma_f32_16x16x32_bf16 v[8:11], v[28:31], v[44:47], v[8:11]\n\t"
    "v_perm_b32 v52, v58, v194, s67\n\t"
    "v_perm_b32 v53, v62, v198, s67\n\t"
    "v_perm_b32 v54, v66, v202, s67\n\t"
    "v_perm_b32 v55, v70, v206, s67\n\t"
    "v_permlane16_swap_b32 v37, v38\n\t"
    "v_mfma_f32_16x16x32_bf16 v[12:15], v[16:19], v[48:51], v[12:15]\n\t"
    "v_perm_b32 v44, v59, v195, s66\n\t"
    "v_perm_b32 v45, v63, v199, s66\n\t"
    "v_perm_b32 v46, v67, v203, s66\n\t"
    "v_perm_b32 v47, v71, v207, s66\n\t"
    "v_max_f32 v37, v37, v38\n\t"
    "v_mfma_f32_16x16x32_bf16 v[12:15], v[20:23], v[52:55], v[12:15]\n\t"
    "v_perm_b32 v48, v59, v195, s67\n\t"
    "v_perm_b32 v49, v63, v199, s67\n\t"
    "v_perm_b32 v50, v67, v203, s67\n\t"
    "v_perm_b32 v51, v71, v207, s67\n\t"
    "v_mov_b32 v38, v37\n\t"
    "v_mfma_f32_16x16x32_bf16 v[12:15], v[24:27], v[44:47], v[12:15]\n\t"
    "s_nop 0\n\t"
    "v_permlane32_swap_b32 v37, v38\n\t"
    "v_mfma_f32_16x16x32_bf16 v[12:15], v[28:31], v[48:51], v[12:15]\n\t"
    "v_max_f32 v37, v37, v38\n\t"
    "v_cmp_gt_f32 vcc, v37, v32\n\t"
    "s_cbranch_vccz .Lr9_%=\n\t"
    "v_max_f32 v37, v37, v32\n\t"
    "v_sub_f32 v38, v32, v37\n\t"
    "v_mul_f32 v38, s64, v38\n\t"
    "v_exp_f32 v38, v38\n\t"
    "v_mov_b32 v32, v37\n\t"
    "v_mul_f32 v34, s65, v37\n\t"
    "v_mul_f32 v33, v33, v38\n\t"
    "v_mul_f32_dpp v8, v38, v8 quad_perm:[0,0,0,0] row_mask:0xf bank_mask:0xf\n\t"
    "v_mul_f32_dpp v9, v38, v9 quad_perm:[1,1,1,1] row_mask:0xf bank_mask:0xf\n\t"
    "v_mul_f32_dpp v10, v38, v10 quad_perm:[2,2,2,2] row_mask:0xf bank_mask:0xf\n\t"
    "v_mul_f32_dpp v11, v38, v11 quad_perm:[3,3,3,3] row_mask:0xf bank_mask:0xf\n\t"
    "v_mul_f32_dpp v12, v38, v12 quad_perm:[0,0,0,0] row_mask:0xf bank_mask:0xf\n\t"
    "v_mul_f32_dpp v13, v38, v13 quad_perm:[1,1,1,1] row_mask:0xf bank_mask:0xf\n\t"
    "v_mul_f32_dpp v14, v38, v14 quad_perm:[2,2,2,2] row_mask:0xf bank_mask:0xf\n\t"
    "v_mul_f32_dpp v15, v38, v15 quad_perm:[3,3,3,3] row_mask:0xf bank_mask:0xf\n\t"
    ".Lr9_%=:\n\t"
    "v_fma_f32 v35, v35, s64, v34\n\t"
    "v_fma_f32 v36, v36, s64, v34\n\t"
    "v_exp_f32 v35, v35\n\t"
    "v_exp_f32 v36, v36\n\t"
    "s_nop 0\n\t"
    "v_cvt_pk_bf16_f32 v39, v35, v36\n\t"
    "v_dot2_f32_bf16 v33, v39, s68, v33\n\t"
    "s_nop 0\n\t"
    "v_mov_b32_dpp v16, v39 quad_perm:[0,1,2,3] row_mask:0xf bank_mask:0x1\n\t"
    "v_mov_b32_dpp v17, v39 row_ror:12 row_mask:0xf bank_mask:0x1\n\t"
    "v_mov_b32_dpp v18, v39 row_ror:8 row_mask:0xf bank_mask:0x1\n\t"
    "v_mov_b32_dpp v19, v39 row_ror:4 row_mask:0xf bank_mask:0x1\n\t"
    "s_waitcnt vmcnt(10)\n\t"
    "s_add_u32 s57, s33, 0x2000\n\t"
    "s_xor_b32 s57, s57, s80\n\t"
    "buffer_load_dwordx4 v[80:83], v1, s[44:47], s57 offen offset:0\n\t"
    "v_perm_b32 v232, v248, v252, s67\n\t"
    "v_perm_b32 v233, v240, v244, s67\n\t"
    "v_perm_b32 v234, v216, v220, s67\n\t"
    "v_perm_b32 v235, v208, v212, s67\n\t"
    "buffer_store_dwordx4 v[232:235], v226, s[48:51], s78 offen offset:128 sc1\n\t"
    "v_mov_b32_dpp v20, v39 row_ror:4 row_mask:0xf bank_mask:0x2\n\t"
    "v_mov_b32_dpp v21, v39 quad_perm:[0,1,2,3] row_mask:0xf bank_mask:0x2\n\t"
    "v_mov_b32_dpp v22, v39 row_ror:12 row_mask:0xf bank_mask:0x2\n\t"
    "v_mov_b32_dpp v23, v39 row_ror:8 row_mask:0xf bank_mask:0x2\n\t"
    "v_mov_b32_dpp v24, v39 row_ror:8 row_mask:0xf bank_mask:0x4\n\t"
    "v_mov_b32_dpp v25, v39 row_ror:4 row_mask:0xf bank_mask:0x4\n\t"
    "v_mov_b32_dpp v26, v39 quad_perm:[0,1,2,3] row_mask:0xf bank_mask:0x4\n\t"
    "v_mov_b32_dpp v27, v39 row_ror:12 row_mask:0xf bank_mask:0x4\n\t"
    "v_mov_b32_dpp v28, v39 row_ror:12 row_mask:0xf bank_mask:0x8\n\t"
    "v_mov_b32_dpp v29, v39 row_ror:8 row_mask:0xf bank_mask:0x8\n\t"
    "v_mov_b32_dpp v30, v39 row_ror:4 row_mask:0xf bank_mask:0x8\n\t"
    "v_mov_b32_dpp v31, v39 quad_perm:[0,1,2,3] row_mask:0xf bank_mask:0x8\n\t"
    "v_mfma_f32_16x16x32_bf16 v[44:47], a[184:187], v[4:7], 0\n\t"
    "s_waitcnt vmcnt(11)\n\t"
    "s_add_u32 s57, s33, 0x3000\n\t"
    "s_xor_b32 s57, s57, s80\n\t"
    "buffer_load_dwordx4 v[84:87], v1, s[44:47], s57 offen offset:0\n\t"
    "v_perm_b32 v236, v249, v253, s66\n\t"
    "v_perm_b32 v237, v241, v245, s66\n\t"
    "v_perm_b32 v238, v217, v221, s66\n\t"
    "v_perm_b32 v239, v209, v213, s66\n\t"
    "buffer_store_dwordx4 v[236:239], v226, s[48:51], s78 offen offset:1024 sc1\n\t"
    "v_mfma_f32_16x16x32_bf16 v[48:51], a[188:191], v[4:7], 0\n\t"
    "s_waitcnt vmcnt(12)\n\t"
    "s_add_u32 s57, s33, 0x4000\n\t"
    "s_xor_b32 s57, s57, s80\n\t"
    "buffer_load_dwordx4 v[88:91], v1, s[44:47], s57 offen offset:0\n\t"
    "v_perm_b32 v232, v249, v253, s67\n\t"
    "v_perm_b32 v233, v241, v245, s67\n\t"
    "v_perm_b32 v234, v217, v221, s67\n\t"
    "v_perm_b32 v235, v209, v213, s67\n\t"
    "buffer_store_dwordx4 v[232:235], v226, s[48:51], s78 offen offset:1152 sc1\n\t"
    "v_mfma_f32_16x16x32_bf16 v[52:55], a[192:195], v[4:7], 0\n\t"
    "v_add_f32_dpp v40, v45, v44 row_ror:12 row_mask:0xf bank_mask:0xf\n\t"
    "v_add_f32_dpp v42, v47, v46 row_ror:12 row_mask:0xf bank_mask:0xf\n\t"
    "s_waitcnt vmcnt(13)\n\t"
    "s_add_u32 s57, s33, 0x5000\n\t"
    "s_xor_b32 s57, s57, s80\n\t"
    "buffer_load_dwordx4 v[92:95], v1, s[44:47], s57 offen offset:0\n\t"
    "v_perm_b32 v236, v250, v254, s66\n\t"
    "v_perm_b32 v237, v242, v246, s66\n\t"
    "v_perm_b32 v238, v218, v222, s66\n\t"
    "v_perm_b32 v239, v210, v214, s66\n\t"
    "buffer_store_dwordx4 v[236:239], v226, s[48:51], s78 offen offset:2048 sc1\n\t"
    "v_mfma_f32_16x16x32_bf16 v[44:47], a[196:199], v[4:7], 0\n\t"
    "v_add_f32_dpp v41, v50, v49 row_ror:12 row_mask:0xf bank_mask:0xf\n\t"
    "v_add_f32_dpp v43, v48, v51 row_ror:12 row_mask:0xf bank_mask:0xf\n\t"
    "v_add_f32_dpp v35, v42, v40 row_ror:8 row_mask:0xf bank_mask:0x1\n\t"
    "s_waitcnt vmcnt(14)\n\t"
    "s_add_u32 s57, s33, 0x6000\n\t"
    "s_xor_b32 s57, s57, s80\n\t"
    "buffer_load_dwordx4 v[96:99], v1, s[44:47], s57 offen offset:0\n\t"
    "v_perm_b32 v232, v250, v254, s67\n\t"
    "v_perm_b32 v233, v242, v246, s67\n\t"
    "v_perm_b32 v234, v218, v222, s67\n\t"
    "v_perm_b32 v235, v210, v214, s67\n\t"
    "buffer_store_dwordx4 v[232:235], v226, s[48:51], s78 offen offset:2176 sc1\n\t"
    "v_mfma_f32_16x16x32_bf16 v[48:51], a[200:203], v[4:7], 0\n\t"
    "v_add_f32_dpp v40, v55, v54 row_ror:12 row_mask:0xf bank_mask:0xf\n\t"
    "v_add_f32_dpp v42, v53, v52 row_ror:12 row_mask:0xf bank_mask:0xf\n\t"
    "v_add_f32_dpp v35, v43, v41 row_ror:8 row_mask:0xf bank_mask:0x2\n\t"
    "s_waitcnt vmcnt(15)\n\t"
    "s_add_u32 s57, s33, 0x7000\n\t"
    "s_xor_b32 s57, s57, s80\n\t"
    "buffer_load_dwordx4 v[100:103], v1, s[44:47], s57 offen offset:0\n\t"
    "v_perm_b32 v236, v251, v255, s66\n\t"
    "v_perm_b32 v237, v243, v247, s66\n\t"
    "v_perm_b32 v238, v219, v223, s66\n\t"
    "v_perm_b32 v239, v211, v215, s66\n\t"
    "buffer_store_dwordx4 v[236:239], v226, s[48:51], s78 offen offset:3072 sc1\n\t"
    "v_mfma_f32_16x16x32_bf16 v[52:55], a[204:207], v[4:7], 0\n\t"
    "v_add_f32_dpp v41, v44, v47 row_ror:12 row_mask:0xf bank_mask:0xf\n\t"
    "v_add_f32_dpp v43, v46, v45 row_ror:12 row_mask:0xf bank_mask:0xf\n\t"
    "v_add_f32_dpp v35, v42, v40 row_ror:8 row_mask:0xf bank_mask:0x4\n\t"
    "s_waitcnt vmcnt(16)\n\t"
    "s_add_u32 s32, s34, 0x38000\n\t"
    "s_and_b32 s32, s32, 0x7ffff\n\t"
    "s_add_u32 s32, s32, s56\n\t"
    "s_add_u32 s57, s32, 0\n\t"
    "s_xor_b32 s57, s57, s80\n\t"
    "buffer_load_dwordx4 a[216:219], v0, s[40:43], s57 offen offset:0\n\t"
    "v_perm_b32 v232, v251, v255, s67\n\t"
    "v_perm_b32 v233, v243, v247, s67\n\t"
    "v_perm_b32 v234, v219, v223, s67\n\t"
    "v_perm_b32 v235, v211, v215, s67\n\t"
    "buffer_store_dwordx4 v[232:235], v226, s[48:51], s78 offen offset:3200 sc1\n\t"
    "v_mfma_f32_16x16x32_bf16 v[44:47], a[208:211], v[4:7], 0\n\t"
    "v_add_f32_dpp v40, v49, v48 row_ror:12 row_mask:0xf bank_mask:0xf\n\t"
    "v_add_f32_dpp v42, v51, v50 row_ror:12 row_mask:0xf bank_mask:0xf\n\t"
    "v_add_f32_dpp v35, v43, v41 row_ror:8 row_mask:0xf bank_mask:0x8\n\t"
    "s_waitcnt vmcnt(17)\n\t"
    "s_add_u32 s57, s32, 0x1000\n\t"
    "s_xor_b32 s57, s57, s80\n\t"
    "buffer_load_dwordx4 a[220:223], v0, s[40:43], s57 offen offset:0\n\t"
    "s_mov_b64 s[52:53], %[wo]\n\t"
    "s_and_b32 s53, s53, 0xffff\n\t"
    "s_mov_b32 s54, 0x400000\n\t"
    "s_mov_b32 s55, 0x20000\n\t"
    "v_lshrrev_b32 v230, 3, v228\n\t"
    "v_lshlrev_b32 v225, 14, v230\n\t"
    "v_bfe_u32 v231, v228, 1, 2\n\t"
    "v_lshl_add_u32 v225, v231, 10, v225\n\t"
    "v_and_b32 v224, 1, v228\n\t"
    "v_lshl_add_u32 v225, v224, 4, v225\n\t"
    "s_lshr_b32 s72, s61, 5\n\t"
    "s_lshl_b32 s72, s72, 2\n\t"
    "s_add_u32 s72, s72, s60\n\t"
    "s_lshl_b32 s72, s72, 17\n\t"
    "s_add_u32 s73, s61, 8\n\t"
    "s_and_b32 s73, s73, 31\n\t"
    "s_lshl_b32 s73, s73, 5\n\t"
    "s_add_u32 s72, s72, s73\n\t"
    "v_lshlrev_b32 v226, 12, v231\n\t"
    "v_lshl_add_u32 v226, v224, 7, v226\n\t"
    "v_lshrrev_b32 v231, 4, v229\n\t"
    "v_lshl_add_u32 v226, v231, 8, v226\n\t"
    "v_and_b32 v230, 1, v230\n\t"
    "v_lshl_add_u32 v226, v230, 3, v226\n\t"
    "buffer_load_dwordx4 v[252:255], v225, s[52:55], s72 offen\n\t"
    "s_add_u32 s57, s32, 0x2000\n\t"
    "s_xor_b32 s57, s57, s80\n\t"
    "buffer_load_dwordx4 a[224:227], v0, s[40:43], s57 offen offset:0\n\t"
    "s_add_u32 s57, s32, 0x3000\n\t"
    "s_xor_b32 s57, s57, s80\n\t"
    "buffer_load_dwordx4 a[228:231], v0, s[40:43], s57 offen offset:0\n\t"
    "s_add_u32 s57, s32, 0x4000\n\t"
    "s_xor_b32 s57, s57, s80\n\t"
    "buffer_load_dwordx4 a[232:235], v0, s[40:43], s57 offen offset:0\n\t"
    "s_add_u32 s72, s72, 0x1000\n\t"
    "buffer_load_dwordx4 v[248:251], v225, s[52:55], s72 offen\n\t"
    "s_add_u32 s57, s32, 0x5000\n\t"
    "s_xor_b32 s57, s57, s80\n\t"
    "buffer_load_dwordx4 a[236:239], v0, s[40:43], s57 offen offset:0\n\t"
    "s_add_u32 s57, s32, 0x6000\n\t"
    "s_xor_b32 s57, s57, s80\n\t"
    "buffer_load_dwordx4 a[240:243], v0, s[40:43], s57 offen offset:0\n\t"
    "s_add_u32 s57, s32, 0x7000\n\t"
    "s_xor_b32 s57, s57, s80\n\t"
    "buffer_load_dwordx4 a[244:247], v0, s[40:43], s57 offen offset:0\n\t"
    "s_add_u32 s72, s72, 0x1000\n\t"
    "buffer_load_dwordx4 v[244:247], v225, s[52:55], s72 offen\n\t"
    "s_add_u32 s33, s34, 0x30000\n\t"
    "s_and_b32 s33, s33, 0x7ffff\n\t"
    "s_add_u32 s33, s33, s56\n\t"
    "s_add_u32 s57, s33, 0\n\t"
    "s_xor_b32 s57, s57, s80\n\t"
    "buffer_load_dwordx4 v[104:107], v1, s[44:47], s57 offen offset:0\n\t"
    "s_add_u32 s57, s33, 0x1000\n\t"
    "s_xor_b32 s57, s57, s80\n\t"
    "buffer_load_dwordx4 v[108:111], v1, s[44:47], s57 offen offset:0\n\t"
    "s_add_u32 s57, s33, 0x2000\n\t"
    "s_xor_b32 s57, s57, s80\n\t"
    "buffer_load_dwordx4 v[112:115], v1, s[44:47], s57 offen offset:0\n\t"
    "s_add_u32 s72, s72, 0x1000\n\t"
    "buffer_load_dwordx4 v[240:243], v225, s[52:55], s72 offen\n\t"
    "s_add_u32 s57, s33, 0x3000\n\t"
    "s_xor_b32 s57, s57, s80\n\t"
    "buffer_load_dwordx4 v[116:119], v1, s[44:47], s57 offen offset:0\n\t"
    "s_add_u32 s57, s33, 0x4000\n\t"
    "s_xor_b32 s57, s57, s80\n\t"
    "buffer_load_dwordx4 v[128:131], v1, s[44:47], s57 offen offset:0\n\t"
    "s_add_u32 s57, s33, 0x5000\n\t"
    "s_xor_b32 s57, s57, s80\n\t"
    "buffer_load_dwordx4 v[132:135], v1, s[44:47], s57 offen offset:0\n\t"
    "v_mfma_f32_16x16x32_bf16 v[48:51], a[212:215], v[4:7], 0\n\t"
    "v_add_f32_dpp v41, v54, v53 row_ror:12 row_mask:0xf bank_mask:0xf\n\t"
    "v_add_f32_dpp v43, v52, v55 row_ror:12 row_mask:0xf bank_mask:0xf\n\t"
    "v_add_f32_dpp v36, v42, v40 row_ror:8 row_mask:0xf bank_mask:0x1\n\t"
    "v_add_f32_dpp v40, v47, v46 row_ror:12 row_mask:0xf bank_mask:0xf\n\t"
    "v_add_f32_dpp v42, v45, v44 row_ror:12 row_mask:0xf bank_mask:0xf\n\t"
    "v_add_f32_dpp v36, v43, v41 row_ror:8 row_mask:0xf bank_mask:0x2\n\t"
    "s_nop 1\n\t"
    "v_add_f32_dpp v41, v48, v51 row_ror:12 row_mask:0xf bank_mask:0xf\n\t"
    "v_add_f32_dpp v43, v50, v49 row_ror:12 row_mask:0xf bank_mask:0xf\n\t"
    "v_add_f32_dpp v36, v42, v40 row_ror:8 row_mask:0xf bank_mask:0x4\n\t"
    "s_nop 0\n\t"
    "v_add_f32_dpp v36, v43, v41 row_ror:8 row_mask:0xf bank_mask:0x8\n\t"
    "s_waitcnt vmcnt(33)\n\t"
    "s_waitcnt vmcnt(32)\n\t"
    "s_waitcnt vmcnt(30)\n\t"
    "s_waitcnt vmcnt(28)\n\t"
    "s_waitcnt vmcnt(26)\n\t"
    "s_waitcnt vmcnt(24)\n\t"
    "s_waitcnt vmcnt(22)\n\t"
    "s_waitcnt vmcnt(20)\n\t"
    "v_perm_b32 v44, v88, v72, s66\n\t"
    "v_perm_b32 v45, v92, v76, s66\n\t"
    "v_perm_b32 v46, v96, v80, s66\n\t"
    "v_perm_b32 v47, v100, v84, s66\n\t"
    "v_perm_b32 v48, v88, v72, s67\n\t"
    "v_perm_b32 v49, v92, v76, s67\n\t"
    "v_perm_b32 v50, v96, v80, s67\n\t"
    "v_perm_b32 v51, v100, v84, s67\n\t"
    "v_max_f32 v37, v35, v36\n\t"
    "v_mfma_f32_16x16x32_bf16 v[8:11], v[16:19], v[44:47], v[8:11]\n\t"
    "v_perm_b32 v52, v89, v73, s66\n\t"
    "v_perm_b32 v53, v93, v77, s66\n\t"
    "v_perm_b32 v54, v97, v81, s66\n\t"
    "v_perm_b32 v55, v101, v85, s66\n\t"
    "v_max_f32_dpp v37, v37, v37 row_ror:4 row_mask:0xf bank_mask:0xf\n\t"
    "v_mfma_f32_16x16x32_bf16 v[8:11], v[20:23], v[48:51], v[8:11]\n\t"
    "v_perm_b32 v44, v89, v73, s67\n\t"
    "v_perm_b32 v45, v93, v77, s67\n\t"
    "v_perm_b32 v46, v97, v81, s67\n\t"
    "v_perm_b32 v47, v101, v85, s67\n\t"
    "v_max_f32_dpp v37, v37, v37 row_ror:8 row_mask:0xf bank_mask:0xf\n\t"
    "v_mfma_f32_16x16x32_bf16 v[8:11], v[24:27], v[52:55], v[8:11]\n\t"
    "v_perm_b32 v48, v90, v74, s66\n\t"
    "v_perm_b32 v49, v94, v78, s66\n\t"
    "v_perm_b32 v50, v98, v82, s66\n\t"
    "v_perm_b32 v51, v102, v86, s66\n\t"
    "v_mov_b32 v38, v37\n\t"
    "v_mfma_f32_16x16x32_bf16 v[8:11], v[28:31], v[44:47], v[8:11]\n\t"
    "v_perm_b32 v52, v90, v74, s67\n\t"
    "v_perm_b32 v53, v94, v78, s67\n\t"
    "v_perm_b32 v54, v98, v82, s67\n\t"
    "v_perm_b32 v55, v102, v86, s67\n\t"
    "v_permlane16_swap_b32 v37, v38\n\t"
    "v_mfma_f32_16x16x32_bf16 v[12:15], v[16:19], v[48:51], v[12:15]\n\t"
    "v_perm_b32 v44, v91, v75, s66\n\t"
    "v_perm_b32 v45, v95, v79, s66\n\t"
    "v_perm_b32 v46, v99, v83, s66\n\t"
    "v_perm_b32 v47, v103, v87, s66\n\t"
    "v_max_f32 v37, v37, v38\n\t"
    "v_mfma_f32_16x16x32_bf16 v[12:15], v[20:23], v[52:55], v[12:15]\n\t"
    "v_perm_b32 v48, v91, v75, s67\n\t"
    "v_perm_b32 v49, v95, v79, s67\n\t"
    "v_perm_b32 v50, v99, v83, s67\n\t"
    "v_perm_b32 v51, v103, v87, s67\n\t"
    "v_mov_b32 v38, v37\n\t"
    "v_mfma_f32_16x16x32_bf16 v[12:15], v[24:27], v[44:47], v[12:15]\n\t"
    "s_nop 0\n\t"
    "v_permlane32_swap_b32 v37, v38\n\t"
    "v_mfma_f32_16x16x32_bf16 v[12:15], v[28:31], v[48:51], v[12:15]\n\t"
    "v_max_f32 v37, v37, v38\n\t"
    "v_cmp_gt_f32 vcc, v37, v32\n\t"
    "s_cbranch_vccz .Lr10_%=\n\t"
    "v_max_f32 v37, v37, v32\n\t"
    "v_sub_f32 v38, v32, v37\n\t"
    "v_mul_f32 v38, s64, v38\n\t"
    "v_exp_f32 v38, v38\n\t"
    "v_mov_b32 v32, v37\n\t"
    "v_mul_f32 v34, s65, v37\n\t"
    "v_mul_f32 v33, v33, v38\n\t"
    "v_mul_f32_dpp v8, v38, v8 quad_perm:[0,0,0,0] row_mask:0xf bank_mask:0xf\n\t"
    "v_mul_f32_dpp v9, v38, v9 quad_perm:[1,1,1,1] row_mask:0xf bank_mask:0xf\n\t"
    "v_mul_f32_dpp v10, v38, v10 quad_perm:[2,2,2,2] row_mask:0xf bank_mask:0xf\n\t"
    "v_mul_f32_dpp v11, v38, v11 quad_perm:[3,3,3,3] row_mask:0xf bank_mask:0xf\n\t"
    "v_mul_f32_dpp v12, v38, v12 quad_perm:[0,0,0,0] row_mask:0xf bank_mask:0xf\n\t"
    "v_mul_f32_dpp v13, v38, v13 quad_perm:[1,1,1,1] row_mask:0xf bank_mask:0xf\n\t"
    "v_mul_f32_dpp v14, v38, v14 quad_perm:[2,2,2,2] row_mask:0xf bank_mask:0xf\n\t"
    "v_mul_f32_dpp v15, v38, v15 quad_perm:[3,3,3,3] row_mask:0xf bank_mask:0xf\n\t"
    ".Lr10_%=:\n\t"
    "v_fma_f32 v35, v35, s64, v34\n\t"
    "v_fma_f32 v36, v36, s64, v34\n\t"
    "v_exp_f32 v35, v35\n\t"
    "v_exp_f32 v36, v36\n\t"
    "s_nop 0\n\t"
    "v_cvt_pk_bf16_f32 v39, v35, v36\n\t"
    "v_dot2_f32_bf16 v33, v39, s68, v33\n\t"
    "s_nop 0\n\t"
    "v_mov_b32_dpp v16, v39 quad_perm:[0,1,2,3] row_mask:0xf bank_mask:0x1\n\t"
    "v_mov_b32_dpp v17, v39 row_ror:12 row_mask:0xf bank_mask:0x1\n\t"
    "v_mov_b32_dpp v18, v39 row_ror:8 row_mask:0xf bank_mask:0x1\n\t"
    "v_mov_b32_dpp v19, v39 row_ror:4 row_mask:0xf bank_mask:0x1\n\t"
    "v_mov_b32_dpp v20, v39 row_ror:4 row_mask:0xf bank_mask:0x2\n\t"
    "v_mov_b32_dpp v21, v39 quad_perm:[0,1,2,3] row_mask:0xf bank_mask:0x2\n\t"
    "v_mov_b32_dpp v22, v39 row_ror:12 row_mask:0xf bank_mask:0x2\n\t"
    "v_mov_b32_dpp v23, v39 row_ror:8 row_mask:0xf bank_mask:0x2\n\t"
    "v_mov_b32_dpp v24, v39 row_ror:8 row_mask:0xf bank_mask:0x4\n\t"
    "v_mov_b32_dpp v25, v39 row_ror:4 row_mask:0xf bank_mask:0x4\n\t"
    "v_mov_b32_dpp v26, v39 quad_perm:[0,1,2,3] row_mask:0xf bank_mask:0x4\n\t"
    "v_mov_b32_dpp v27, v39 row_ror:12 row_mask:0xf bank_mask:0x4\n\t"
    "v_mov_b32_dpp v28, v39 row_ror:12 row_mask:0xf bank_mask:0x8\n\t"
    "v_mov_b32_dpp v29, v39 row_ror:8 row_mask:0xf bank_mask:0x8\n\t"
    "v_mov_b32_dpp v30, v39 row_ror:4 row_mask:0xf bank_mask:0x8\n\t"
    "v_mov_b32_dpp v31, v39 quad_perm:[0,1,2,3] row_mask:0xf bank_mask:0x8\n\t"
    "s_waitcnt vmcnt(18)\n\t"
    "v_mfma_f32_16x16x32_bf16 v[44:47], a[216:219], v[4:7], 0\n\t"
    "s_waitcnt vmcnt(16)\n\t"
    "v_mfma_f32_16x16x32_bf16 v[48:51], a[220:223], v[4:7], 0\n\t"
    "s_waitcnt vmcnt(14)\n\t"
    "v_mfma_f32_16x16x32_bf16 v[52:55], a[224:227], v[4:7], 0\n\t"
    "s_nop 3\n\t"
    "v_add_f32_dpp v40, v45, v44 row_ror:12 row_mask:0xf bank_mask:0xf\n\t"
    "v_add_f32_dpp v42, v47, v46 row_ror:12 row_mask:0xf bank_mask:0xf\n\t"
    "s_waitcnt vmcnt(13)\n\t"
    "v_mfma_f32_16x16x32_bf16 v[44:47], a[228:231], v[4:7], 0\n\t"
    "v_add_f32_dpp v41, v50, v49 row_ror:12 row_mask:0xf bank_mask:0xf\n\t"
    "v_add_f32_dpp v43, v48, v51 row_ror:12 row_mask:0xf bank_mask:0xf\n\t"
    "v_add_f32_dpp v35, v42, v40 row_ror:8 row_mask:0xf bank_mask:0x1\n\t"
    "s_waitcnt vmcnt(12)\n\t"
    "s_add_u32 s57, s33, 0x6000\n\t"
    "s_xor_b32 s57, s57, s80\n\t"
    "buffer_load_dwordx4 v[136:139], v1, s[44:47], s57 offen offset:0\n\t"
    "v_mfma_f32_16x16x32_bf16 v[48:51], a[232:235], v[4:7], 0\n\t"
    "v_add_f32_dpp v40, v55, v54 row_ror:12 row_mask:0xf bank_mask:0xf\n\t"
    "v_add_f32_dpp v42, v53, v52 row_ror:12 row_mask:0xf bank_mask:0xf\n\t"
    "v_add_f32_dpp v35, v43, v41 row_ror:8 row_mask:0xf bank_mask:0x2\n\t"
    "s_waitcnt vmcnt(11)\n\t"
    "s_add_u32 s57, s33, 0x7000\n\t"
    "s_xor_b32 s57, s57, s80\n\t"
    "buffer_load_dwordx4 v[140:143], v1, s[44:47], s57 offen offset:0\n\t"
    "v_mfma_f32_16x16x32_bf16 v[52:55], a[236:239], v[4:7], 0\n\t"
    "v_add_f32_dpp v41, v44, v47 row_ror:12 row_mask:0xf bank_mask:0xf\n\t"
    "v_add_f32_dpp v43, v46, v45 row_ror:12 row_mask:0xf bank_mask:0xf\n\t"
    "v_add_f32_dpp v35, v42, v40 row_ror:8 row_mask:0xf bank_mask:0x4\n\t"
    "s_waitcnt vmcnt(11)\n\t"
    "s_add_u32 s32, s34, 0x40000\n\t"
    "s_and_b32 s32, s32, 0x7ffff\n\t"
    "s_add_u32 s32, s32, s56\n\t"
    "s_add_u32 s57, s32, 0\n\t"
    "s_xor_b32 s57, s57, s80\n\t"
    "buffer_load_dwordx4 a[248:251], v0, s[40:43], s57 offen offset:0 nt\n\t"
    "v_mfma_f32_16x16x32_bf16 v[44:47], a[240:243], v[4:7], 0\n\t"
    "v_add_f32_dpp v40, v49, v48 row_ror:12 row_mask:0xf bank_mask:0xf\n\t"
    "v_add_f32_dpp v42, v51, v50 row_ror:12 row_mask:0xf bank_mask:0xf\n\t"
    "v_add_f32_dpp v35, v43, v41 row_ror:8 row_mask:0xf bank_mask:0x8\n\t"
    "s_waitcnt vmcnt(11)\n\t"
    "s_add_u32 s57, s32, 0x1000\n\t"
    "s_xor_b32 s57, s57, s80\n\t"
    "buffer_load_dwordx4 a[252:255], v0, s[40:43], s57 offen offset:0 nt\n\t"
    "v_mfma_f32_16x16x32_bf16 v[48:51], a[244:247], v[4:7], 0\n\t"
    "v_add_f32_dpp v41, v54, v53 row_ror:12 row_mask:0xf bank_mask:0xf\n\t"
    "v_add_f32_dpp v43, v52, v55 row_ror:12 row_mask:0xf bank_mask:0xf\n\t"
    "v_add_f32_dpp v36, v42, v40 row_ror:8 row_mask:0xf bank_mask:0x1\n\t"
    "v_add_f32_dpp v40, v47, v46 row_ror:12 row_mask:0xf bank_mask:0xf\n\t"
    "v_add_f32_dpp v42, v45, v44 row_ror:12 row_mask:0xf bank_mask:0xf\n\t"
    "v_add_f32_dpp v36, v43, v41 row_ror:8 row_mask:0xf bank_mask:0x2\n\t"
    "s_nop 1\n\t"
    "v_add_f32_dpp v41, v48, v51 row_ror:12 row_mask:0xf bank_mask:0xf\n\t"
    "v_add_f32_dpp v43, v50, v49 row_ror:12 row_mask:0xf bank_mask:0xf\n\t"
    "v_add_f32_dpp v36, v42, v40 row_ror:8 row_mask:0xf bank_mask:0x4\n\t"
    "s_nop 0\n\t"
    "v_add_f32_dpp v36, v43, v41 row_ror:8 row_mask:0xf bank_mask:0x8\n\t"
    "s_waitcnt vmcnt(10)\n\t"
    "s_add_u32 s57, s32, 0x2000\n\t"
    "s_xor_b32 s57, s57, s80\n\t"
    "buffer_load_dwordx4 a[64:67], v0, s[40:43], s57 offen offset:0 nt\n\t"
    "s_waitcnt vmcnt(10)\n\t"
    "s_add_u32 s57, s32, 0x3000\n\t"
    "s_xor_b32 s57, s57, s80\n\t"
    "buffer_load_dwordx4 a[68:71], v0, s[40:43], s57 offen offset:0 nt\n\t"
    "s_waitcnt vmcnt(10)\n\t"
    "s_add_u32 s57, s32, 0x4000\n\t"
    "s_xor_b32 s57, s57, s80\n\t"
    "buffer_load_dwordx4 a[72:75], v0, s[40:43], s57 offen offset:0 nt\n\t"
    "s_waitcnt vmcnt(9)\n\t"
    "s_add_u32 s57, s32, 0x5000\n\t"
    "s_xor_b32 s57, s57, s80\n\t"
    "buffer_load_dwordx4 a[76:79], v0, s[40:43], s57 offen offset:0 nt\n\t"
    "v_perm_b32 v236, v248, v252, s66\n\t"
    "v_perm_b32 v237, v240, v244, s66\n\t"
    "ds_write_b64 v226, v[236:237] offset:16384\n\t"
    "v_perm_b32 v238, v248, v252, s67\n\t"
    "v_perm_b32 v239, v240, v244, s67\n\t"
    "ds_write_b64 v226, v[238:239] offset:16400\n\t"
    "v_perm_b32 v232, v249, v253, s66\n\t"
    "v_perm_b32 v233, v241, v245, s66\n\t"
    "ds_write_b64 v226, v[232:233] offset:16416\n\t"
    "v_perm_b32 v234, v249, v253, s67\n\t"
    "v_perm_b32 v235, v241, v245, s67\n\t"
    "ds_write_b64 v226, v[234:235] offset:16432\n\t"
    "v_perm_b32 v236, v250, v254, s66\n\t"
    "v_perm_b32 v237, v242, v246, s66\n\t"
    "ds_write_b64 v226, v[236:237] offset:16448\n\t"
    "v_perm_b32 v238, v250, v254, s67\n\t"
    "v_perm_b32 v239, v242, v246, s67\n\t"
    "ds_write_b64 v226, v[238:239] offset:16464\n\t"
    "v_perm_b32 v232, v251, v255, s66\n\t"
    "v_perm_b32 v233, v243, v247, s66\n\t"
    "ds_write_b64 v226, v[232:233] offset:16480\n\t"
    "v_perm_b32 v234, v251, v255, s67\n\t"
    "v_perm_b32 v235, v243, v247, s67\n\t"
    "ds_write_b64 v226, v[234:235] offset:16496\n\t"
    "s_cmp_lt_u32 s60, 2\n\t"
    "s_cselect_b64 s[74:75], %[w1], %[w3]\n\t"
    "s_mov_b64 s[52:53], s[74:75]\n\t"
    "s_and_b32 s53, s53, 0xffff\n\t"
    "s_mov_b32 s54, 0x1c0000\n\t"
    "s_mov_b32 s55, 0x20000\n\t"
    "v_and_b32 v230, 0x7f, v229\n\t"
    "v_mul_u32_u24 v225, 0x3800, v230\n\t"
    "s_min_u32 s72, s61, 0xdf\n\t"
    "s_lshl_b32 s72, s72, 4\n\t"
    "v_lshrrev_b32 v231, 7, v229\n\t"
    "v_bfe_u32 v224, v230, 3, 2\n\t"
    "v_lshlrev_b32 v226, 12, v224\n\t"
    "v_bfe_u32 v224, v230, 1, 2\n\t"
    "v_lshl_add_u32 v226, v224, 8, v226\n\t"
    "v_lshrrev_b32 v224, 5, v230\n\t"
    "v_lshl_add_u32 v226, v224, 10, v226\n\t"
    "v_and_b32 v224, 1, v230\n\t"
    "v_lshl_add_u32 v226, v224, 3, v226\n\t"
    "v_lshl_add_u32 v226, v231, 7, v226\n\t"
    "buffer_load_dwordx4 v[252:255], v225, s[52:55], s72 offen\n\t"
    "s_add_u32 s57, s32, 0x6000\n\t"
    "s_xor_b32 s57, s57, s80\n\t"
    "buffer_load_dwordx4 a[80:83], v0, s[40:43], s57 offen offset:0 nt\n\t"
    "s_add_u32 s57, s32, 0x7000\n\t"
    "s_xor_b32 s57, s57, s80\n\t"
    "buffer_load_dwordx4 a[84:87], v0, s[40:43], s57 offen offset:0 nt\n\t"
    "s_add_u32 s33, s34, 0x38000\n\t"
    "s_and_b32 s33, s33, 0x7ffff\n\t"
    "s_add_u32 s33, s33, s56\n\t"
    "s_add_u32 s57, s33, 0\n\t"
    "s_xor_b32 s57, s57, s80\n\t"
    "buffer_load_dwordx4 v[144:147], v1, s[44:47], s57 offen offset:0\n\t"
    "s_add_u32 s72, s72, 0xe00\n\t"
    "buffer_load_dwordx4 v[248:251], v225, s[52:55], s72 offen\n\t"
    "s_add_u32 s57, s33, 0x1000\n\t"
    "s_xor_b32 s57, s57, s80\n\t"
    "buffer_load_dwordx4 v[148:151], v1, s[44:47], s57 offen offset:0\n\t"
    "s_add_u32 s57, s33, 0x2000\n\t"
    "s_xor_b32 s57, s57, s80\n\t"
    "buffer_load_dwordx4 v[152:155], v1, s[44:47], s57 offen offset:0\n\t"
    "s_add_u32 s57, s33, 0x3000\n\t"
    "s_xor_b32 s57, s57, s80\n\t"
    "buffer_load_dwordx4 v[156:159], v1, s[44:47], s57 offen offset:0\n\t"
    "s_add_u32 s72, s72, 0xe00\n\t"
    "buffer_load_dwordx4 v[244:247], v225, s[52:55], s72 offen\n\t"
    "s_add_u32 s57, s33, 0x4000\n\t"
    "s_xor_b32 s57, s57, s80\n\t"
    "buffer_load_dwordx4 v[160:163], v1, s[44:47], s57 offen offset:0\n\t"
    "s_add_u32 s57, s33, 0x5000\n\t"
    "s_xor_b32 s57, s57, s80\n\t"
    "buffer_load_dwordx4 v[164:167], v1, s[44:47], s57 offen offset:0\n\t"
    "s_add_u32 s57, s33, 0x6000\n\t"
    "s_xor_b32 s57, s57, s80\n\t"
    "buffer_load_dwordx4 v[168:171], v1, s[44:47], s57 offen offset:0\n\t"
    "s_add_u32 s72, s72, 0xe00\n\t"
    "buffer_load_dwordx4 v[240:243], v225, s[52:55], s72 offen\n\t"
    "s_add_u32 s57, s33, 0x7000\n\t"
    "s_xor_b32 s57, s57, s80\n\t"
    "buffer_load_dwordx4 v[172:175], v1, s[44:47], s57 offen offset:0\n\t"
    "s_add_u32 s32, s34, 0x48000\n\t"
    "s_and_b32 s32, s32, 0x7ffff\n\t"
    "s_add_u32 s32, s32, s56\n\t"
    "s_add_u32 s57, s32, 0\n\t"
    "s_xor_b32 s57, s57, s80\n\t"
    "buffer_load_dwordx4 a[88:91], v0, s[40:43], s57 offen offset:0 nt\n\t"
    "s_add_u32 s57, s32, 0x1000\n\t"
    "s_xor_b32 s57, s57, s80\n\t"
    "buffer_load_dwordx4 a[92:95], v0, s[40:43], s57 offen offset:0 nt\n\t"
    "s_waitcnt vmcnt(25)\n\t"
    "s_waitcnt vmcnt(24)\n\t"
    "s_waitcnt vmcnt(23)\n\t"
    "s_waitcnt vmcnt(22)\n\t"
    "v_perm_b32 v44, v128, v104, s66\n\t"
    "v_perm_b32 v45, v132, v108, s66\n\t"
    "v_perm_b32 v46, v136, v112, s66\n\t"
    "v_perm_b32 v47, v140, v116, s66\n\t"
    "v_perm_b32 v48, v128, v104, s67\n\t"
    "v_perm_b32 v49, v132, v108, s67\n\t"
    "v_perm_b32 v50, v136, v112, s67\n\t"
    "v_perm_b32 v51, v140, v116, s67\n\t"
    "v_max_f32 v37, v35, v36\n\t"
    "v_mfma_f32_16x16x32_bf16 v[8:11], v[16:19], v[44:47], v[8:11]\n\t"
    "v_perm_b32 v52, v129, v105, s66\n\t"
    "v_perm_b32 v53, v133, v109, s66\n\t"
    "v_perm_b32 v54, v137, v113, s66\n\t"
    "v_perm_b32 v55, v141, v117, s66\n\t"
    "v_max_f32_dpp v37, v37, v37 row_ror:4 row_mask:0xf bank_mask:0xf\n\t"
    "v_mfma_f32_16x16x32_bf16 v[8:11], v[20:23], v[48:51], v[8:11]\n\t"
    "v_perm_b32 v44, v129, v105, s67\n\t"
    "v_perm_b32 v45, v133, v109, s67\n\t"
    "v_perm_b32 v46, v137, v113, s67\n\t"
    "v_perm_b32 v47, v141, v117, s67\n\t"
    "v_max_f32_dpp v37, v37, v37 row_ror:8 row_mask:0xf bank_mask:0xf\n\t"
    "v_mfma_f32_16x16x32_bf16 v[8:11], v[24:27], v[52:55], v[8:11]\n\t"
    "v_perm_b32 v48, v130, v106, s66\n\t"
    "v_perm_b32 v49, v134, v110, s66\n\t"
    "v_perm_b32 v50, v138, v114, s66\n\t"
    "v_perm_b32 v51, v142, v118, s66\n\t"
    "v_mov_b32 v38, v37\n\t"
    "v_mfma_f32_16x16x32_bf16 v[8:11], v[28:31], v[44:47], v[8:11]\n\t"
    "v_perm_b32 v52, v130, v106, s67\n\t"
    "v_perm_b32 v53, v134, v110, s67\n\t"
    "v_perm_b32 v54, v138, v114, s67\n\t"
    "v_perm_b32 v55, v142, v118, s67\n\t"
    "v_permlane16_swap_b32 v37, v38\n\t"
    "v_mfma_f32_16x16x32_bf16 v[12:15], v[16:19], v[48:51], v[12:15]\n\t"
    "v_perm_b32 v44, v131, v107, s66\n\t"
    "v_perm_b32 v45, v135, v111, s66\n\t"
    "v_perm_b32 v46, v139, v115, s66\n\t"
    "v_perm_b32 v47, v143, v119, s66\n\t"
    "v_max_f32 v37, v37, v38\n\t"
    "v_mfma_f32_16x16x32_bf16 v[12:15], v[20:23], v[52:55], v[12:15]\n\t"
    "v_perm_b32 v48, v131, v107, s67\n\t"
    "v_perm_b32 v49, v135, v111, s67\n\t"
    "v_perm_b32 v50, v139, v115, s67\n\t"
    "v_perm_b32 v51, v143, v119, s67\n\t"
    "v_mov_b32 v38, v37\n\t"
    "v_mfma_f32_16x16x32_bf16 v[12:15], v[24:27], v[44:47], v[12:15]\n\t"
    "s_nop 0\n\t"
    "v_permlane32_swap_b32 v37, v38\n\t"
    "v_mfma_f32_16x16x32_bf16 v[12:15], v[28:31], v[48:51], v[12:15]\n\t"
    "v_max_f32 v37, v37, v38\n\t"
    "v_cmp_gt_f32 vcc, v37, v32\n\t"
    "s_cbranch_vccz .Lr11_%=\n\t"
    "v_max_f32 v37, v37, v32\n\t"
    "v_sub_f32 v38, v32, v37\n\t"
    "v_mul_f32 v38, s64, v38\n\t"
    "v_exp_f32 v38, v38\n\t"
    "v_mov_b32 v32, v37\n\t"
    "v_mul_f32 v34, s65, v37\n\t"
    "v_mul_f32 v33, v33, v38\n\t"
    "v_mul_f32_dpp v8, v38, v8 quad_perm:[0,0,0,0] row_mask:0xf bank_mask:0xf\n\t"
    "v_mul_f32_dpp v9, v38, v9 quad_perm:[1,1,1,1] row_mask:0xf bank_mask:0xf\n\t"
    "v_mul_f32_dpp v10, v38, v10 quad_perm:[2,2,2,2] row_mask:0xf bank_mask:0xf\n\t"
    "v_mul_f32_dpp v11, v38, v11 quad_perm:[3,3,3,3] row_mask:0xf bank_mask:0xf\n\t"
    "v_mul_f32_dpp v12, v38, v12 quad_perm:[0,0,0,0] row_mask:0xf bank_mask:0xf\n\t"
    "v_mul_f32_dpp v13, v38, v13 quad_perm:[1,1,1,1] row_mask:0xf bank_mask:0xf\n\t"
    "v_mul_f32_dpp v14, v38, v14 quad_perm:[2,2,2,2] row_mask:0xf bank_mask:0xf\n\t"
    "v_mul_f32_dpp v15, v38, v15 quad_perm:[3,3,3,3] row_mask:0xf bank_mask:0xf\n\t"
    ".Lr11_%=:\n\t"
    "v_fma_f32 v35, v35, s64, v34\n\t"
    "v_fma_f32 v36, v36, s64, v34\n\t"
    "v_exp_f32 v35, v35\n\t"
    "v_exp_f32 v36, v36\n\t"
    "s_nop 0\n\t"
    "v_cvt_pk_bf16_f32 v39, v35, v36\n\t"
    "v_dot2_f32_bf16 v33, v39, s68, v33\n\t"
    "s_nop 0\n\t"
    "v_mov_b32_dpp v16, v39 quad_perm:[0,1,2,3] row_mask:0xf bank_mask:0x1\n\t"
    "v_mov_b32_dpp v17, v39 row_ror:12 row_mask:0xf bank_mask:0x1\n\t"
    "v_mov_b32_dpp v18, v39 row_ror:8 row_mask:0xf bank_mask:0x1\n\t"
    "v_mov_b32_dpp v19, v39 row_ror:4 row_mask:0xf bank_mask:0x1\n\t"
    "v_mov_b32_dpp v20, v39 row_ror:4 row_mask:0xf bank_mask:0x2\n\t"
    "v_mov_b32_dpp v21, v39 quad_perm:[0,1,2,3] row_mask:0xf bank_mask:0x2\n\t"
    "v_mov_b32_dpp v22, v39 row_ror:12 row_mask:0xf bank_mask:0x2\n\t"
    "v_mov_b32_dpp v23, v39 row_ror:8 row_mask:0xf bank_mask:0x2\n\t"
    "v_mov_b32_dpp v24, v39 row_ror:8 row_mask:0xf bank_mask:0x4\n\t"
    "v_mov_b32_dpp v25, v39 row_ror:4 row_mask:0xf bank_mask:0x4\n\t"
    "v_mov_b32_dpp v26, v39 quad_perm:[0,1,2,3] row_mask:0xf bank_mask:0x4\n\t"
    "v_mov_b32_dpp v27, v39 row_ror:12 row_mask:0xf bank_mask:0x4\n\t"
    "v_mov_b32_dpp v28, v39 row_ror:12 row_mask:0xf bank_mask:0x8\n\t"
    "v_mov_b32_dpp v29, v39 row_ror:8 row_mask:0xf bank_mask:0x8\n\t"
    "v_mov_b32_dpp v30, v39 row_ror:4 row_mask:0xf bank_mask:0x8\n\t"
    "v_mov_b32_dpp v31, v39 quad_perm:[0,1,2,3] row_mask:0xf bank_mask:0x8\n\t"
    "s_waitcnt vmcnt(21)\n\t"
    "v_mfma_f32_16x16x32_bf16 v[44:47], a[248:251], v[4:7], 0\n\t"
    "s_waitcnt vmcnt(20)\n\t"
    "v_mfma_f32_16x16x32_bf16 v[48:51], a[252:255], v[4:7], 0\n\t"
    "s_waitcnt vmcnt(19)\n\t"
    "v_mfma_f32_16x16x32_bf16 v[52:55], a[64:67], v[4:7], 0\n\t"
    "s_nop 3\n\t"
    "v_add_f32_dpp v40, v45, v44 row_ror:12 row_mask:0xf bank_mask:0xf\n\t"
    "v_add_f32_dpp v42, v47, v46 row_ror:12 row_mask:0xf bank_mask:0xf\n\t"
    "s_waitcnt vmcnt(18)\n\t"
    "v_mfma_f32_16x16x32_bf16 v[44:47], a[68:71], v[4:7], 0\n\t"
    "v_add_f32_dpp v41, v50, v49 row_ror:12 row_mask:0xf bank_mask:0xf\n\t"
    "v_add_f32_dpp v43, v48, v51 row_ror:12 row_mask:0xf bank_mask:0xf\n\t"
    "v_add_f32_dpp v35, v42, v40 row_ror:8 row_mask:0xf bank_mask:0x1\n\t"
    "s_waitcnt vmcnt(17)\n\t"
    "v_mfma_f32_16x16x32_bf16 v[48:51], a[72:75], v[4:7], 0\n\t"
    "v_add_f32_dpp v40, v55, v54 row_ror:12 row_mask:0xf bank_mask:0xf\n\t"
    "v_add_f32_dpp v42, v53, v52 row_ror:12 row_mask:0xf bank_mask:0xf\n\t"
    "v_add_f32_dpp v35, v43, v41 row_ror:8 row_mask:0xf bank_mask:0x2\n\t"
    "s_waitcnt vmcnt(16)\n\t"
    "v_mfma_f32_16x16x32_bf16 v[52:55], a[76:79], v[4:7], 0\n\t"
    "v_add_f32_dpp v41, v44, v47 row_ror:12 row_mask:0xf bank_mask:0xf\n\t"
    "v_add_f32_dpp v43, v46, v45 row_ror:12 row_mask:0xf bank_mask:0xf\n\t"
    "v_add_f32_dpp v35, v42, v40 row_ror:8 row_mask:0xf bank_mask:0x4\n\t"
    "s_waitcnt vmcnt(14)\n\t"
    "v_mfma_f32_16x16x32_bf16 v[44:47], a[80:83], v[4:7], 0\n\t"
    "v_add_f32_dpp v40, v49, v48 row_ror:12 row_mask:0xf bank_mask:0xf\n\t"
    "v_add_f32_dpp v42, v51, v50 row_ror:12 row_mask:0xf bank_mask:0xf\n\t"
    "v_add_f32_dpp v35, v43, v41 row_ror:8 row_mask:0xf bank_mask:0x8\n\t"
    "s_waitcnt vmcnt(13)\n\t"
    "v_mfma_f32_16x16x32_bf16 v[48:51], a[84:87], v[4:7], 0\n\t"
    "v_add_f32_dpp v41, v54, v53 row_ror:12 row_mask:0xf bank_mask:0xf\n\t"
    "v_add_f32_dpp v43, v52, v55 row_ror:12 row_mask:0xf bank_mask:0xf\n\t"
    "v_add_f32_dpp v36, v42, v40 row_ror:8 row_mask:0xf bank_mask:0x1\n\t"
    "v_add_f32_dpp v40, v47, v46 row_ror:12 row_mask:0xf bank_mask:0xf\n\t"
    "v_add_f32_dpp v42, v45, v44 row_ror:12 row_mask:0xf bank_mask:0xf\n\t"
    "v_add_f32_dpp v36, v43, v41 row_ror:8 row_mask:0xf bank_mask:0x2\n\t"
    "s_nop 1\n\t"
    "v_add_f32_dpp v41, v48, v51 row_ror:12 row_mask:0xf bank_mask:0xf\n\t"
    "v_add_f32_dpp v43, v50, v49 row_ror:12 row_mask:0xf bank_mask:0xf\n\t"
    "v_add_f32_dpp v36, v42, v40 row_ror:8 row_mask:0xf bank_mask:0x4\n\t"
    "s_nop 0\n\t"
    "v_add_f32_dpp v36, v43, v41 row_ror:8 row_mask:0xf bank_mask:0x8\n\t"
    "s_waitcnt vmcnt(12)\n\t"
    "s_add_u32 s57, s32, 0x2000\n\t"
    "s_xor_b32 s57, s57, s80\n\t"
    "buffer_load_dwordx4 a[96:99], v0, s[40:43], s57 offen offset:0 nt\n\t"
    "s_waitcnt vmcnt(11)\n\t"
    "s_add_u32 s57, s32, 0x3000\n\t"
    "s_xor_b32 s57, s57, s80\n\t"
    "buffer_load_dwordx4 a[100:103], v0, s[40:43], s57 offen offset:0 nt\n\t"
    "s_waitcnt vmcnt(11)\n\t"
    "s_add_u32 s57, s32, 0x4000\n\t"
    "s_xor_b32 s57, s57, s80\n\t"
    "buffer_load_dwordx4 a[104:107], v0, s[40:43], s57 offen offset:0 nt\n\t"
    "s_waitcnt vmcnt(11)\n\t"
    "s_add_u32 s57, s32, 0x5000\n\t"
    "s_xor_b32 s57, s57, s80\n\t"
    "buffer_load_dwordx4 a[108:111], v0, s[40:43], s57 offen offset:0 nt\n\t"
    "s_waitcnt vmcnt(10)\n\t"
    "s_add_u32 s57, s32, 0x6000\n\t"
    "s_xor_b32 s57, s57, s80\n\t"
    "buffer_load_dwordx4 a[112:115], v0, s[40:43], s57 offen offset:0 nt\n\t"
    "s_waitcnt vmcnt(10)\n\t"
    "s_add_u32 s57, s32, 0x7000\n\t"
    "s_xor_b32 s57, s57, s80\n\t"
    "buffer_load_dwordx4 a[116:119], v0, s[40:43], s57 offen offset:0 nt\n\t"
    "s_waitcnt vmcnt(10)\n\t"
    "s_add_u32 s33, s34, 0x40000\n\t"
    "s_and_b32 s33, s33, 0x7ffff\n\t"
    "s_add_u32 s33, s33, s56\n\t"
    "s_add_u32 s57, s33, 0\n\t"
    "s_xor_b32 s57, s57, s80\n\t"
    "buffer_load_dwordx4 v[176:179], v1, s[44:47], s57 offen offset:0 nt\n\t"
    "s_waitcnt vmcnt(9)\n\t"
    "s_add_u32 s57, s33, 0x1000\n\t"
    "s_xor_b32 s57, s57, s80\n\t"
    "buffer_load_dwordx4 v[180:183], v1, s[44:47], s57 offen offset:0 nt\n\t"
    "v_perm_b32 v236, v248, v252, s66\n\t"
    "v_perm_b32 v237, v240, v244, s66\n\t"
    "ds_write_b64 v226, v[236:237] offset:32768\n\t"
    "v_perm_b32 v238, v248, v252, s67\n\t"
    "v_perm_b32 v239, v240, v244, s67\n\t"
    "ds_write_b64 v226, v[238:239] offset:32784\n\t"
    "v_perm_b32 v232, v249, v253, s66\n\t"
    "v_perm_b32 v233, v241, v245, s66\n\t"
    "ds_write_b64 v226, v[232:233] offset:32800\n\t"
    "v_perm_b32 v234, v249, v253, s67\n\t"
    "v_perm_b32 v235, v241, v245, s67\n\t"
    "ds_write_b64 v226, v[234:235] offset:32816\n\t"
    "v_perm_b32 v236, v250, v254, s66\n\t"
    "v_perm_b32 v237, v242, v246, s66\n\t"
    "ds_write_b64 v226, v[236:237] offset:32832\n\t"
    "v_perm_b32 v238, v250, v254, s67\n\t"
    "v_perm_b32 v239, v242, v246, s67\n\t"
    "ds_write_b64 v226, v[238:239] offset:32848\n\t"
    "v_perm_b32 v232, v251, v255, s66\n\t"
    "v_perm_b32 v233, v243, v247, s66\n\t"
    "ds_write_b64 v226, v[232:233] offset:32864\n\t"
    "v_perm_b32 v234, v251, v255, s67\n\t"
    "v_perm_b32 v235, v243, v247, s67\n\t"
    "ds_write_b64 v226, v[234:235] offset:32880\n\t"
    "s_waitcnt lgkmcnt(0)\n\t"
    "s_barrier\n\t"
    "ds_read_b128 v[252:255], v227 offset:32768\n\t"
    "ds_read_b128 v[248:251], v227 offset:36864\n\t"
    "ds_read_b128 v[244:247], v227 offset:40960\n\t"
    "ds_read_b128 v[240:243], v227 offset:45056\n\t"
    "v_and_b32 v230, 15, v228\n\t"
    "s_lshl_b32 s79, s60, 2\n\t"
    "s_add_u32 s79, s79, 0xdd40\n\t"
    "v_lshl_add_u32 v230, v230, 4, s79\n\t"
    "s_waitcnt lgkmcnt(0)\n\t"
    "v_mov_b32 v232, s68\n\t"
    "v_mov_b32 v233, s68\n\t"
    "v_mov_b32 v234, s68\n\t"
    "v_mov_b32 v235, s68\n\t"
    "s_nop 1\n\t"
    "v_mfma_f32_16x16x32_bf16 v[236:239], v[232:235], v[252:255], 0\n\t"
    "v_mfma_f32_16x16x32_bf16 v[236:239], v[232:235], v[248:251], v[236:239]\n\t"
    "v_mfma_f32_16x16x32_bf16 v[236:239], v[232:235], v[244:247], v[236:239]\n\t"
    "v_mfma_f32_16x16x32_bf16 v[236:239], v[232:235], v[240:243], v[236:239]\n\t"
    "s_nop 7\n\t"
    "ds_write_b32 v230, v236\n\t"
    "v_perm_b32 v44, v160, v144, s66\n\t"
    "v_perm_b32 v45, v164, v148, s66\n\t"
    "v_perm_b32 v46, v168, v152, s66\n\t"
    "v_perm_b32 v47, v172, v156, s66\n\t"
    "v_perm_b32 v48, v160, v144, s67\n\t"
    "v_perm_b32 v49, v164, v148, s67\n\t"
    "v_perm_b32 v50, v168, v152, s67\n\t"
    "v_perm_b32 v51, v172, v156, s67\n\t"
    "v_max_f32 v37, v35, v36\n\t"
    "v_mfma_f32_16x16x32_bf16 v[8:11], v[16:19], v[44:47], v[8:11]\n\t"
    "s_waitcnt vmcnt(9)\n\t"
    "s_add_u32 s57, s33, 0x2000\n\t"
    "s_xor_b32 s57, s57, s80\n\t"
    "buffer_load_dwordx4 v[184:187], v1, s[44:47], s57 offen offset:0 nt\n\t"
    "v_perm_b32 v52, v161, v145, s66\n\t"
    "v_perm_b32 v53, v165, v149, s66\n\t"
    "v_perm_b32 v54, v169, v153, s66\n\t"
    "v_perm_b32 v55, v173, v157, s66\n\t"
    "v_max_f32_dpp v37, v37, v37 row_ror:4 row_mask:0xf bank_mask:0xf\n\t"
    "v_mfma_f32_16x16x32_bf16 v[8:11], v[20:23], v[48:51], v[8:11]\n\t"
    "v_perm_b32 v44, v161, v145, s67\n\t"
    "v_perm_b32 v45, v165, v149, s67\n\t"
    "v_perm_b32 v46, v169, v153, s67\n\t"
    "v_perm_b32 v47, v173, v157, s67\n\t"
    "v_max_f32_dpp v37, v37, v37 row_ror:8 row_mask:0xf bank_mask:0xf\n\t"
    "v_mfma_f32_16x16x32_bf16 v[8:11], v[24:27], v[52:55], v[8:11]\n\t"
    "v_perm_b32 v48, v162, v146, s66\n\t"
    "v_perm_b32 v49, v166, v150, s66\n\t"
    "v_perm_b32 v50, v170, v154, s66\n\t"
    "v_perm_b32 v51, v174, v158, s66\n\t"
    "v_mov_b32 v38, v37\n\t"
    "v_mfma_f32_16x16x32_bf16 v[8:11], v[28:31], v[44:47], v[8:11]\n\t"
    "v_perm_b32 v52, v162, v146, s67\n\t"
    "v_perm_b32 v53, v166, v150, s67\n\t"
    "v_perm_b32 v54, v170, v154, s67\n\t"
    "v_perm_b32 v55, v174, v158, s67\n\t"
    "v_permlane16_swap_b32 v37, v38\n\t"
    "v_mfma_f32_16x16x32_bf16 v[12:15], v[16:19], v[48:51], v[12:15]\n\t"
    "v_perm_b32 v44, v163, v147, s66\n\t"
    "v_perm_b32 v45, v167, v151, s66\n\t"
    "v_perm_b32 v46, v171, v155, s66\n\t"
    "v_perm_b32 v47, v175, v159, s66\n\t"
    "v_max_f32 v37, v37, v38\n\t"
    "v_mfma_f32_16x16x32_bf16 v[12:15], v[20:23], v[52:55], v[12:15]\n\t"
    "v_perm_b32 v48, v163, v147, s67\n\t"
    "v_perm_b32 v49, v167, v151, s67\n\t"
    "v_perm_b32 v50, v171, v155, s67\n\t"
    "v_perm_b32 v51, v175, v159, s67\n\t"
    "v_mov_b32 v38, v37\n\t"
    "v_mfma_f32_16x16x32_bf16 v[12:15], v[24:27], v[44:47], v[12:15]\n\t"
    "s_nop 0\n\t"
    "v_permlane32_swap_b32 v37, v38\n\t"
    "v_mfma_f32_16x16x32_bf16 v[12:15], v[28:31], v[48:51], v[12:15]\n\t"
    "v_max_f32 v37, v37, v38\n\t"
    "v_cmp_gt_f32 vcc, v37, v32\n\t"
    "s_cbranch_vccz .Lr12_%=\n\t"
    "v_max_f32 v37, v37, v32\n\t"
    "v_sub_f32 v38, v32, v37\n\t"
    "v_mul_f32 v38, s64, v38\n\t"
    "v_exp_f32 v38, v38\n\t"
    "v_mov_b32 v32, v37\n\t"
    "v_mul_f32 v34, s65, v37\n\t"
    "v_mul_f32 v33, v33, v38\n\t"
    "v_mul_f32_dpp v8, v38, v8 quad_perm:[0,0,0,0] row_mask:0xf bank_mask:0xf\n\t"
    "v_mul_f32_dpp v9, v38, v9 quad_perm:[1,1,1,1] row_mask:0xf bank_mask:0xf\n\t"
    "v_mul_f32_dpp v10, v38, v10 quad_perm:[2,2,2,2] row_mask:0xf bank_mask:0xf\n\t"
    "v_mul_f32_dpp v11, v38, v11 quad_perm:[3,3,3,3] row_mask:0xf bank_mask:0xf\n\t"
    "v_mul_f32_dpp v12, v38, v12 quad_perm:[0,0,0,0] row_mask:0xf bank_mask:0xf\n\t"
    "v_mul_f32_dpp v13, v38, v13 quad_perm:[1,1,1,1] row_mask:0xf bank_mask:0xf\n\t"
    "v_mul_f32_dpp v14, v38, v14 quad_perm:[2,2,2,2] row_mask:0xf bank_mask:0xf\n\t"
    "v_mul_f32_dpp v15, v38, v15 quad_perm:[3,3,3,3] row_mask:0xf bank_mask:0xf\n\t"
    ".Lr12_%=:\n\t"
    "v_fma_f32 v35, v35, s64, v34\n\t"
    "v_fma_f32 v36, v36, s64, v34\n\t"
    "v_exp_f32 v35, v35\n\t"
    "v_exp_f32 v36, v36\n\t"
    "s_nop 0\n\t"
    "v_cvt_pk_bf16_f32 v39, v35, v36\n\t"
    "v_dot2_f32_bf16 v33, v39, s68, v33\n\t"
    "s_nop 0\n\t"
    "v_mov_b32_dpp v16, v39 quad_perm:[0,1,2,3] row_mask:0xf bank_mask:0x1\n\t"
    "v_mov_b32_dpp v17, v39 row_ror:12 row_mask:0xf bank_mask:0x1\n\t"
    "v_mov_b32_dpp v18, v39 row_ror:8 row_mask:0xf bank_mask:0x1\n\t"
    "v_mov_b32_dpp v19, v39 row_ror:4 row_mask:0xf bank_mask:0x1\n\t"
    "v_mov_b32_dpp v20, v39 row_ror:4 row_mask:0xf bank_mask:0x2\n\t"
    "v_mov_b32_dpp v21, v39 quad_perm:[0,1,2,3] row_mask:0xf bank_mask:0x2\n\t"
    "v_mov_b32_dpp v22, v39 row_ror:12 row_mask:0xf bank_mask:0x2\n\t"
    "v_mov_b32_dpp v23, v39 row_ror:8 row_mask:0xf bank_mask:0x2\n\t"
    "v_mov_b32_dpp v24, v39 row_ror:8 row_mask:0xf bank_mask:0x4\n\t"
    "v_mov_b32_dpp v25, v39 row_ror:4 row_mask:0xf bank_mask:0x4\n\t"
    "v_mov_b32_dpp v26, v39 quad_perm:[0,1,2,3] row_mask:0xf bank_mask:0x4\n\t"
    "v_mov_b32_dpp v27, v39 row_ror:12 row_mask:0xf bank_mask:0x4\n\t"
    "v_mov_b32_dpp v28, v39 row_ror:12 row_mask:0xf bank_mask:0x8\n\t"
    "v_mov_b32_dpp v29, v39 row_ror:8 row_mask:0xf bank_mask:0x8\n\t"
    "v_mov_b32_dpp v30, v39 row_ror:4 row_mask:0xf bank_mask:0x8\n\t"
    "v_mov_b32_dpp v31, v39 quad_perm:[0,1,2,3] row_mask:0xf bank_mask:0x8\n\t"
    "s_waitcnt vmcnt(9)\n\t"
    "s_add_u32 s57, s33, 0x3000\n\t"
    "s_xor_b32 s57, s57, s80\n\t"
    "buffer_load_dwordx4 v[188:191], v1, s[44:47], s57 offen offset:0 nt\n\t"
    "v_mfma_f32_16x16x32_bf16 v[44:47], a[88:91], v[4:7], 0\n\t"
    "v_mfma_f32_16x16x32_bf16 v[48:51], a[92:95], v[4:7], 0\n\t"
    "s_waitcnt vmcnt(9)\n\t"
    "s_add_u32 s57, s33, 0x4000\n\t"
    "s_xor_b32 s57, s57, s80\n\t"
    "buffer_load_dwordx4 v[192:195], v1, s[44:47], s57 offen offset:0 nt\n\t"
    "v_mfma_f32_16x16x32_bf16 v[52:55], a[96:99], v[4:7], 0\n\t"
    "s_nop 1\n\t"
    "v_add_f32_dpp v40, v45, v44 row_ror:12 row_mask:0xf bank_mask:0xf\n\t"
    "v_add_f32_dpp v42, v47, v46 row_ror:12 row_mask:0xf bank_mask:0xf\n\t"
    "s_waitcnt vmcnt(9)\n\t"
    "s_add_u32 s57, s33, 0x5000\n\t"
    "s_xor_b32 s57, s57, s80\n\t"
    "buffer_load_dwordx4 v[196:199], v1, s[44:47], s57 offen offset:0 nt\n\t"
    "v_mfma_f32_16x16x32_bf16 v[44:47], a[100:103], v[4:7], 0\n\t"
    "v_add_f32_dpp v41, v50, v49 row_ror:12 row_mask:0xf bank_mask:0xf\n\t"
    "v_add_f32_dpp v43, v48, v51 row_ror:12 row_mask:0xf bank_mask:0xf\n\t"
    "v_add_f32_dpp v35, v42, v40 row_ror:8 row_mask:0xf bank_mask:0x1\n\t"
    "s_waitcnt vmcnt(9)\n\t"
    "s_add_u32 s57, s33, 0x6000\n\t"
    "s_xor_b32 s57, s57, s80\n\t"
    "buffer_load_dwordx4 v[200:203], v1, s[44:47], s57 offen offset:0 nt\n\t"
    "v_mfma_f32_16x16x32_bf16 v[48:51], a[104:107], v[4:7], 0\n\t"
    "v_add_f32_dpp v40, v55, v54 row_ror:12 row_mask:0xf bank_mask:0xf\n\t"
    "v_add_f32_dpp v42, v53, v52 row_ror:12 row_mask:0xf bank_mask:0xf\n\t"
    "v_add_f32_dpp v35, v43, v41 row_ror:8 row_mask:0xf bank_mask:0x2\n\t"
    "s_waitcnt vmcnt(9)\n\t"
    "s_add_u32 s57, s33, 0x7000\n\t"
    "s_xor_b32 s57, s57, s80\n\t"
    "buffer_load_dwordx4 v[204:207], v1, s[44:47], s57 offen offset:0 nt\n\t"
    "v_mfma_f32_16x16x32_bf16 v[52:55], a[108:111], v[4:7], 0\n\t"
    "v_add_f32_dpp v41, v44, v47 row_ror:12 row_mask:0xf bank_mask:0xf\n\t"
    "v_add_f32_dpp v43, v46, v45 row_ror:12 row_mask:0xf bank_mask:0xf\n\t"
    "v_add_f32_dpp v35, v42, v40 row_ror:8 row_mask:0xf bank_mask:0x4\n\t"
    "s_waitcnt vmcnt(9)\n\t"
    "s_add_u32 s32, s34, 0x50000\n\t"
    "s_and_b32 s32, s32, 0x7ffff\n\t"
    "s_add_u32 s32, s32, s56\n\t"
    "s_add_u32 s57, s32, 0\n\t"
    "s_xor_b32 s57, s57, s80\n\t"
    "buffer_load_dwordx4 a[120:123], v0, s[40:43], s57 offen offset:0 nt\n\t"
    "v_mfma_f32_16x16x32_bf16 v[44:47], a[112:115], v[4:7], 0\n\t"
    "v_add_f32_dpp v40, v49, v48 row_ror:12 row_mask:0xf bank_mask:0xf\n\t"
    "v_add_f32_dpp v42, v51, v50 row_ror:12 row_mask:0xf bank_mask:0xf\n\t"
    "v_add_f32_dpp v35, v43, v41 row_ror:8 row_mask:0xf bank_mask:0x8\n\t"
    "s_waitcnt vmcnt(9)\n\t"
    "s_add_u32 s57, s32, 0x1000\n\t"
    "s_xor_b32 s57, s57, s80\n\t"
    "buffer_load_dwordx4 a[124:127], v0, s[40:43], s57 offen offset:0 nt\n\t"
    "v_mfma_f32_16x16x32_bf16 v[48:51], a[116:119], v[4:7], 0\n\t"
    "v_add_f32_dpp v41, v54, v53 row_ror:12 row_mask:0xf bank_mask:0xf\n\t"
    "v_add_f32_dpp v43, v52, v55 row_ror:12 row_mask:0xf bank_mask:0xf\n\t"
    "v_add_f32_dpp v36, v42, v40 row_ror:8 row_mask:0xf bank_mask:0x1\n\t"
    "v_add_f32_dpp v40, v47, v46 row_ror:12 row_mask:0xf bank_mask:0xf\n\t"
    "v_add_f32_dpp v42, v45, v44 row_ror:12 row_mask:0xf bank_mask:0xf\n\t"
    "v_add_f32_dpp v36, v43, v41 row_ror:8 row_mask:0xf bank_mask:0x2\n\t"
    "s_nop 1\n\t"
    "v_add_f32_dpp v41, v48, v51 row_ror:12 row_mask:0xf bank_mask:0xf\n\t"
    "v_add_f32_dpp v43, v50, v49 row_ror:12 row_mask:0xf bank_mask:0xf\n\t"
    "v_add_f32_dpp v36, v42, v40 row_ror:8 row_mask:0xf bank_mask:0x4\n\t"
    "s_nop 0\n\t"
    "v_add_f32_dpp v36, v43, v41 row_ror:8 row_mask:0xf bank_mask:0x8\n\t"
    "s_waitcnt vmcnt(9)\n\t"
    "s_add_u32 s57, s32, 0x2000\n\t"
    "s_xor_b32 s57, s57, s80\n\t"
    "buffer_load_dwordx4 a[128:131], v0, s[40:43], s57 offen offset:0 nt\n\t"
    "s_waitcnt vmcnt(9)\n\t"
    "s_add_u32 s57, s32, 0x3000\n\t"
    "s_xor_b32 s57, s57, s80\n\t"
    "buffer_load_dwordx4 a[132:135], v0, s[40:43], s57 offen offset:0 nt\n\t"
    "s_waitcnt vmcnt(9)\n\t"
    "s_add_u32 s57, s32, 0x4000\n\t"
    "s_xor_b32 s57, s57, s80\n\t"
    "buffer_load_dwordx4 a[136:139], v0, s[40:43], s57 offen offset:0 nt\n\t"
    "s_waitcnt vmcnt(9)\n\t"
    "s_add_u32 s57, s32, 0x5000\n\t"
    "s_xor_b32 s57, s57, s80\n\t"
    "buffer_load_dwordx4 a[140:143], v0, s[40:43], s57 offen offset:0 nt\n\t"
    "s_waitcnt vmcnt(9)\n\t"
    "s_add_u32 s57, s32, 0x6000\n\t"
    "s_xor_b32 s57, s57, s80\n\t"
    "buffer_load_dwordx4 a[144:147], v0, s[40:43], s57 offen offset:0 nt\n\t"
    "s_waitcnt vmcnt(9)\n\t"
    "s_add_u32 s57, s32, 0x7000\n\t"
    "s_xor_b32 s57, s57, s80\n\t"
    "buffer_load_dwordx4 a[148:151], v0, s[40:43], s57 offen offset:0 nt\n\t"
    "s_waitcnt vmcnt(9)\n\t"
    "s_add_u32 s33, s34, 0x48000\n\t"
    "s_and_b32 s33, s33, 0x7ffff\n\t"
    "s_add_u32 s33, s33, s56\n\t"
    "s_add_u32 s57, s33, 0\n\t"
    "s_xor_b32 s57, s57, s80\n\t"
    "buffer_load_dwordx4 v[56:59], v1, s[44:47], s57 offen offset:0 nt\n\t"
    "s_waitcnt vmcnt(9)\n\t"
    "s_add_u32 s57, s33, 0x1000\n\t"
    "s_xor_b32 s57, s57, s80\n\t"
    "buffer_load_dwordx4 v[60:63], v1, s[44:47], s57 offen offset:0 nt\n\t"
    "v_perm_b32 v44, v192, v176, s66\n\t"
    "v_perm_b32 v45, v196, v180, s66\n\t"
    "v_perm_b32 v46, v200, v184, s66\n\t"
    "v_perm_b32 v47, v204, v188, s66\n\t"
    "v_perm_b32 v48, v192, v176, s67\n\t"
    "v_perm_b32 v49, v196, v180, s67\n\t"
    "v_perm_b32 v50, v200, v184, s67\n\t"
    "v_perm_b32 v51, v204, v188, s67\n\t"
    "v_max_f32 v37, v35, v36\n\t"
    "v_mfma_f32_16x16x32_bf16 v[8:11], v[16:19], v[44:47], v[8:11]\n\t"
    "v_perm_b32 v52, v193, v177, s66\n\t"
    "v_perm_b32 v53, v197, v181, s66\n\t"
    "v_perm_b32 v54, v201, v185, s66\n\t"
    "v_perm_b32 v55, v205, v189, s66\n\t"
    "v_max_f32_dpp v37, v37, v37 row_ror:4 row_mask:0xf bank_mask:0xf\n\t"
    "v_mfma_f32_16x16x32_bf16 v[8:11], v[20:23], v[48:51], v[8:11]\n\t"
    "v_perm_b32 v44, v193, v177, s67\n\t"
    "v_perm_b32 v45, v197, v181, s67\n\t"
    "v_perm_b32 v46, v201, v185, s67\n\t"
    "v_perm_b32 v47, v205, v189, s67\n\t"
    "v_max_f32_dpp v37, v37, v37 row_ror:8 row_mask:0xf bank_mask:0xf\n\t"
    "v_mfma_f32_16x16x32_bf16 v[8:11], v[24:27], v[52:55], v[8:11]\n\t"
    "v_perm_b32 v48, v194, v178, s66\n\t"
    "v_perm_b32 v49, v198, v182, s66\n\t"
    "v_perm_b32 v50, v202, v186, s66\n\t"
    "v_perm_b32 v51, v206, v190, s66\n\t"
    "v_mov_b32 v38, v37\n\t"
    "v_mfma_f32_16x16x32_bf16 v[8:11], v[28:31], v[44:47], v[8:11]\n\t"
    "v_perm_b32 v52, v194, v178, s67\n\t"
    "v_perm_b32 v53, v198, v182, s67\n\t"
    "v_perm_b32 v54, v202, v186, s67\n\t"
    "v_perm_b32 v55, v206, v190, s67\n\t"
    "v_permlane16_swap_b32 v37, v38\n\t"
    "v_mfma_f32_16x16x32_bf16 v[12:15], v[16:19], v[48:51], v[12:15]\n\t"
    "v_perm_b32 v44, v195, v179, s66\n\t"
    "v_perm_b32 v45, v199, v183, s66\n\t"
    "v_perm_b32 v46, v203, v187, s66\n\t"
    "v_perm_b32 v47, v207, v191, s66\n\t"
    "v_max_f32 v37, v37, v38\n\t"
    "v_mfma_f32_16x16x32_bf16 v[12:15], v[20:23], v[52:55], v[12:15]\n\t"
    "v_perm_b32 v48, v195, v179, s67\n\t"
    "v_perm_b32 v49, v199, v183, s67\n\t"
    "v_perm_b32 v50, v203, v187, s67\n\t"
    "v_perm_b32 v51, v207, v191, s67\n\t"
    "v_mov_b32 v38, v37\n\t"
    "v_mfma_f32_16x16x32_bf16 v[12:15], v[24:27], v[44:47], v[12:15]\n\t"
    "s_nop 0\n\t"
    "v_permlane32_swap_b32 v37, v38\n\t"
    "v_mfma_f32_16x16x32_bf16 v[12:15], v[28:31], v[48:51], v[12:15]\n\t"
    "v_max_f32 v37, v37, v38\n\t"
    "v_cmp_gt_f32 vcc, v37, v32\n\t"
    "s_cbranch_vccz .Lr13_%=\n\t"
    "v_max_f32 v37, v37, v32\n\t"
    "v_sub_f32 v38, v32, v37\n\t"
    "v_mul_f32 v38, s64, v38\n\t"
    "v_exp_f32 v38, v38\n\t"
    "v_mov_b32 v32, v37\n\t"
    "v_mul_f32 v34, s65, v37\n\t"
    "v_mul_f32 v33, v33, v38\n\t"
    "v_mul_f32_dpp v8, v38, v8 quad_perm:[0,0,0,0] row_mask:0xf bank_mask:0xf\n\t"
    "v_mul_f32_dpp v9, v38, v9 quad_perm:[1,1,1,1] row_mask:0xf bank_mask:0xf\n\t"
    "v_mul_f32_dpp v10, v38, v10 quad_perm:[2,2,2,2] row_mask:0xf bank_mask:0xf\n\t"
    "v_mul_f32_dpp v11, v38, v11 quad_perm:[3,3,3,3] row_mask:0xf bank_mask:0xf\n\t"
    "v_mul_f32_dpp v12, v38, v12 quad_perm:[0,0,0,0] row_mask:0xf bank_mask:0xf\n\t"
    "v_mul_f32_dpp v13, v38, v13 quad_perm:[1,1,1,1] row_mask:0xf bank_mask:0xf\n\t"
    "v_mul_f32_dpp v14, v38, v14 quad_perm:[2,2,2,2] row_mask:0xf bank_mask:0xf\n\t"
    "v_mul_f32_dpp v15, v38, v15 quad_perm:[3,3,3,3] row_mask:0xf bank_mask:0xf\n\t"
    ".Lr13_%=:\n\t"
    "v_fma_f32 v35, v35, s64, v34\n\t"
    "v_fma_f32 v36, v36, s64, v34\n\t"
    "v_exp_f32 v35, v35\n\t"
    "v_exp_f32 v36, v36\n\t"
    "s_nop 0\n\t"
    "v_cvt_pk_bf16_f32 v39, v35, v36\n\t"
    "v_dot2_f32_bf16 v33, v39, s68, v33\n\t"
    "s_nop 0\n\t"
    "v_mov_b32_dpp v16, v39 quad_perm:[0,1,2,3] row_mask:0xf bank_mask:0x1\n\t"
    "v_mov_b32_dpp v17, v39 row_ror:12 row_mask:0xf bank_mask:0x1\n\t"
    "v_mov_b32_dpp v18, v39 row_ror:8 row_mask:0xf bank_mask:0x1\n\t"
    "v_mov_b32_dpp v19, v39 row_ror:4 row_mask:0xf bank_mask:0x1\n\t"
    "v_mov_b32_dpp v20, v39 row_ror:4 row_mask:0xf bank_mask:0x2\n\t"
    "v_mov_b32_dpp v21, v39 quad_perm:[0,1,2,3] row_mask:0xf bank_mask:0x2\n\t"
    "v_mov_b32_dpp v22, v39 row_ror:12 row_mask:0xf bank_mask:0x2\n\t"
    "v_mov_b32_dpp v23, v39 row_ror:8 row_mask:0xf bank_mask:0x2\n\t"
    "s_waitcnt vmcnt(9)\n\t"
    "s_add_u32 s57, s33, 0x2000\n\t"
    "s_xor_b32 s57, s57, s80\n\t"
    "buffer_load_dwordx4 v[64:67], v1, s[44:47], s57 offen offset:0 nt\n\t"
    "v_mov_b32_dpp v24, v39 row_ror:8 row_mask:0xf bank_mask:0x4\n\t"
    "v_mov_b32_dpp v25, v39 row_ror:4 row_mask:0xf bank_mask:0x4\n\t"
    "v_mov_b32_dpp v26, v39 quad_perm:[0,1,2,3] row_mask:0xf bank_mask:0x4\n\t"
    "v_mov_b32_dpp v27, v39 row_ror:12 row_mask:0xf bank_mask:0x4\n\t"
    "v_mov_b32_dpp v28, v39 row_ror:12 row_mask:0xf bank_mask:0x8\n\t"
    "v_mov_b32_dpp v29, v39 row_ror:8 row_mask:0xf bank_mask:0x8\n\t"
    "v_mov_b32_dpp v30, v39 row_ror:4 row_mask:0xf bank_mask:0x8\n\t"
    "v_mov_b32_dpp v31, v39 quad_perm:[0,1,2,3] row_mask:0xf bank_mask:0x8\n\t"
    "v_mfma_f32_16x16x32_bf16 v[44:47], a[120:123], v[4:7], 0\n\t"
    "s_waitcnt vmcnt(9)\n\t"
    "s_add_u32 s57, s33, 0x3000\n\t"
    "s_xor_b32 s57, s57, s80\n\t"
    "buffer_load_dwordx4 v[68:71], v1, s[44:47], s57 offen offset:0 nt\n\t"
    "v_mfma_f32_16x16x32_bf16 v[48:51], a[124:127], v[4:7], 0\n\t"
    "s_waitcnt vmcnt(9)\n\t"
    "s_add_u32 s57, s33, 0x4000\n\t"
    "s_xor_b32 s57, s57, s80\n\t"
    "buffer_load_dwordx4 v[72:75], v1, s[44:47], s57 offen offset:0 nt\n\t"
    "v_mfma_f32_16x16x32_bf16 v[52:55], a[128:131], v[4:7], 0\n\t"
    "v_add_f32_dpp v40, v45, v44 row_ror:12 row_mask:0xf bank_mask:0xf\n\t"
    "v_add_f32_dpp v42, v47, v46 row_ror:12 row_mask:0xf bank_mask:0xf\n\t"
    "s_waitcnt vmcnt(9)\n\t"
    "s_add_u32 s57, s33, 0x5000\n\t"
    "s_xor_b32 s57, s57, s80\n\t"
    "buffer_load_dwordx4 v[76:79], v1, s[44:47], s57 offen offset:0 nt\n\t"
    "v_mfma_f32_16x16x32_bf16 v[44:47], a[132:135], v[4:7], 0\n\t"
    "v_add_f32_dpp v41, v50, v49 row_ror:12 row_mask:0xf bank_mask:0xf\n\t"
    "v_add_f32_dpp v43, v48, v51 row_ror:12 row_mask:0xf bank_mask:0xf\n\t"
    "v_add_f32_dpp v35, v42, v40 row_ror:8 row_mask:0xf bank_mask:0x1\n\t"
    "s_waitcnt vmcnt(9)\n\t"
    "s_add_u32 s57, s33, 0x6000\n\t"
    "s_xor_b32 s57, s57, s80\n\t"
    "buffer_load_dwordx4 v[80:83], v1, s[44:47], s57 offen offset:0 nt\n\t"
    "s_getpc_b64 s[52:53]\n\t"
    ".Lpc_%=:\n\t"
    "s_add_u32 s52, s52, .Lpass2_%=-.Lpc_%=\n\t"
    "s_addc_u32 s53, s53, 0\n\t"
    "s_and_b32 s53, s53, 0xffff\n\t"
    "s_mov_b32 s54, .Lend_%=-.Lpass2_%=\n\t"
    "s_mov_b32 s55, 0x20000\n\t"
    "s_lshr_b32 s72, s59, 3\n\t"
    "s_and_b32 s72, s72, 15\n\t"
    "s_lshl_b32 s72, s72, 12\n\t"
    "buffer_load_dwordx4 v[220:223], v227, s[52:55], s72 offen\n\t"
    "v_mfma_f32_16x16x32_bf16 v[48:51], a[136:139], v[4:7], 0\n\t"
    "v_add_f32_dpp v40, v55, v54 row_ror:12 row_mask:0xf bank_mask:0xf\n\t"
    "v_add_f32_dpp v42, v53, v52 row_ror:12 row_mask:0xf bank_mask:0xf\n\t"
    "v_add_f32_dpp v35, v43, v41 row_ror:8 row_mask:0xf bank_mask:0x2\n\t"
    "s_waitcnt vmcnt(10)\n\t"
    "s_add_u32 s57, s33, 0x7000\n\t"
    "s_xor_b32 s57, s57, s80\n\t"
    "buffer_load_dwordx4 v[84:87], v1, s[44:47], s57 offen offset:0 nt\n\t"
    "v_mfma_f32_16x16x32_bf16 v[52:55], a[140:143], v[4:7], 0\n\t"
    "v_add_f32_dpp v41, v44, v47 row_ror:12 row_mask:0xf bank_mask:0xf\n\t"
    "v_add_f32_dpp v43, v46, v45 row_ror:12 row_mask:0xf bank_mask:0xf\n\t"
    "v_add_f32_dpp v35, v42, v40 row_ror:8 row_mask:0xf bank_mask:0x4\n\t"
    "s_waitcnt vmcnt(10)\n\t"
    "s_add_u32 s32, s34, 0x58000\n\t"
    "s_and_b32 s32, s32, 0x7ffff\n\t"
    "s_add_u32 s32, s32, s56\n\t"
    "s_add_u32 s57, s32, 0\n\t"
    "s_xor_b32 s57, s57, s80\n\t"
    "buffer_load_dwordx4 a[152:155], v0, s[40:43], s57 offen offset:0 nt\n\t"
    "v_mfma_f32_16x16x32_bf16 v[44:47], a[144:147], v[4:7], 0\n\t"
    "v_add_f32_dpp v40, v49, v48 row_ror:12 row_mask:0xf bank_mask:0xf\n\t"
    "v_add_f32_dpp v42, v51, v50 row_ror:12 row_mask:0xf bank_mask:0xf\n\t"
    "v_add_f32_dpp v35, v43, v41 row_ror:8 row_mask:0xf bank_mask:0x8\n\t"
    "s_waitcnt vmcnt(10)\n\t"
    "s_add_u32 s57, s32, 0x1000\n\t"
    "s_xor_b32 s57, s57, s80\n\t"
    "buffer_load_dwordx4 a[156:159], v0, s[40:43], s57 offen offset:0 nt\n\t"
    "v_mfma_f32_16x16x32_bf16 v[48:51], a[148:151], v[4:7], 0\n\t"
    "v_add_f32_dpp v41, v54, v53 row_ror:12 row_mask:0xf bank_mask:0xf\n\t"
    "v_add_f32_dpp v43, v52, v55 row_ror:12 row_mask:0xf bank_mask:0xf\n\t"
    "v_add_f32_dpp v36, v42, v40 row_ror:8 row_mask:0xf bank_mask:0x1\n\t"
    "v_add_f32_dpp v40, v47, v46 row_ror:12 row_mask:0xf bank_mask:0xf\n\t"
    "v_add_f32_dpp v42, v45, v44 row_ror:12 row_mask:0xf bank_mask:0xf\n\t"
    "v_add_f32_dpp v36, v43, v41 row_ror:8 row_mask:0xf bank_mask:0x2\n\t"
    "s_nop 1\n\t"
    "v_add_f32_dpp v41, v48, v51 row_ror:12 row_mask:0xf bank_mask:0xf\n\t"
    "v_add_f32_dpp v43, v50, v49 row_ror:12 row_mask:0xf bank_mask:0xf\n\t"
    "v_add_f32_dpp v36, v42, v40 row_ror:8 row_mask:0xf bank_mask:0x4\n\t"
    "s_nop 0\n\t"
    "v_add_f32_dpp v36, v43, v41 row_ror:8 row_mask:0xf bank_mask:0x8\n\t"
    "s_waitcnt vmcnt(10)\n\t"
    "s_add_u32 s57, s32, 0x2000\n\t"
    "s_xor_b32 s57, s57, s80\n\t"
    "buffer_load_dwordx4 a[160:163], v0, s[40:43], s57 offen offset:0 nt\n\t"
    "s_waitcnt vmcnt(10)\n\t"
    "s_add_u32 s57, s32, 0x3000\n\t"
    "s_xor_b32 s57, s57, s80\n\t"
    "buffer_load_dwordx4 a[164:167], v0, s[40:43], s57 offen offset:0 nt\n\t"
    "s_waitcnt vmcnt(10)\n\t"
    "s_add_u32 s57, s32, 0x4000\n\t"
    "s_xor_b32 s57, s57, s80\n\t"
    "buffer_load_dwordx4 a[168:171], v0, s[40:43], s57 offen offset:0 nt\n\t"
    "s_waitcnt vmcnt(10)\n\t"
    "s_add_u32 s57, s32, 0x5000\n\t"
    "s_xor_b32 s57, s57, s80\n\t"
    "buffer_load_dwordx4 a[172:175], v0, s[40:43], s57 offen offset:0 nt\n\t"
    "s_waitcnt vmcnt(10)\n\t"
    "s_add_u32 s57, s32, 0x6000\n\t"
    "s_xor_b32 s57, s57, s80\n\t"
    "buffer_load_dwordx4 a[176:179], v0, s[40:43], s57 offen offset:0 nt\n\t"
    "s_waitcnt vmcnt(10)\n\t"
    "s_add_u32 s57, s32, 0x7000\n\t"
    "s_xor_b32 s57, s57, s80\n\t"
    "buffer_load_dwordx4 a[180:183], v0, s[40:43], s57 offen offset:0 nt\n\t"
    "s_waitcnt vmcnt(10)\n\t"
    "s_add_u32 s33, s34, 0x50000\n\t"
    "s_and_b32 s33, s33, 0x7ffff\n\t"
    "s_add_u32 s33, s33, s56\n\t"
    "s_add_u32 s57, s33, 0\n\t"
    "s_xor_b32 s57, s57, s80\n\t"
    "buffer_load_dwordx4 v[88:91], v1, s[44:47], s57 offen offset:0 nt\n\t"
    "s_waitcnt vmcnt(9)\n\t"
    "s_add_u32 s57, s33, 0x1000\n\t"
    "s_xor_b32 s57, s57, s80\n\t"
    "buffer_load_dwordx4 v[92:95], v1, s[44:47], s57 offen offset:0 nt\n\t"
    "v_perm_b32 v44, v72, v56, s66\n\t"
    "v_perm_b32 v45, v76, v60, s66\n\t"
    "v_perm_b32 v46, v80, v64, s66\n\t"
    "v_perm_b32 v47, v84, v68, s66\n\t"
    "v_perm_b32 v48, v72, v56, s67\n\t"
    "v_perm_b32 v49, v76, v60, s67\n\t"
    "v_perm_b32 v50, v80, v64, s67\n\t"
    "v_perm_b32 v51, v84, v68, s67\n\t"
    "v_max_f32 v37, v35, v36\n\t"
    "v_mfma_f32_16x16x32_bf16 v[8:11], v[16:19], v[44:47], v[8:11]\n\t"
    "v_perm_b32 v52, v73, v57, s66\n\t"
    "v_perm_b32 v53, v77, v61, s66\n\t"
    "v_perm_b32 v54, v81, v65, s66\n\t"
    "v_perm_b32 v55, v85, v69, s66\n\t"
    "v_max_f32_dpp v37, v37, v37 row_ror:4 row_mask:0xf bank_mask:0xf\n\t"
    "v_mfma_f32_16x16x32_bf16 v[8:11], v[20:23], v[48:51], v[8:11]\n\t"
    "v_perm_b32 v44, v73, v57, s67\n\t"
    "v_perm_b32 v45, v77, v61, s67\n\t"
    "v_perm_b32 v46, v81, v65, s67\n\t"
    "v_perm_b32 v47, v85, v69, s67\n\t"
    "v_max_f32_dpp v37, v37, v37 row_ror:8 row_mask:0xf bank_mask:0xf\n\t"
    "v_mfma_f32_16x16x32_bf16 v[8:11], v[24:27], v[52:55], v[8:11]\n\t"
    "v_perm_b32 v48, v74, v58, s66\n\t"
    "v_perm_b32 v49, v78, v62, s66\n\t"
    "v_perm_b32 v50, v82, v66, s66\n\t"
    "v_perm_b32 v51, v86, v70, s66\n\t"
    "v_mov_b32 v38, v37\n\t"
    "v_mfma_f32_16x16x32_bf16 v[8:11], v[28:31], v[44:47], v[8:11]\n\t"
    "v_perm_b32 v52, v74, v58, s67\n\t"
    "v_perm_b32 v53, v78, v62, s67\n\t"
    "v_perm_b32 v54, v82, v66, s67\n\t"
    "v_perm_b32 v55, v86, v70, s67\n\t"
    "v_permlane16_swap_b32 v37, v38\n\t"
    "v_mfma_f32_16x16x32_bf16 v[12:15], v[16:19], v[48:51], v[12:15]\n\t"
    "v_perm_b32 v44, v75, v59, s66\n\t"
    "v_perm_b32 v45, v79, v63, s66\n\t"
    "v_perm_b32 v46, v83, v67, s66\n\t"
    "v_perm_b32 v47, v87, v71, s66\n\t"
    "v_max_f32 v37, v37, v38\n\t"
    "v_mfma_f32_16x16x32_bf16 v[12:15], v[20:23], v[52:55], v[12:15]\n\t"
    "v_perm_b32 v48, v75, v59, s67\n\t"
    "v_perm_b32 v49, v79, v63, s67\n\t"
    "v_perm_b32 v50, v83, v67, s67\n\t"
    "v_perm_b32 v51, v87, v71, s67\n\t"
    "v_mov_b32 v38, v37\n\t"
    "v_mfma_f32_16x16x32_bf16 v[12:15], v[24:27], v[44:47], v[12:15]\n\t"
    "s_nop 0\n\t"
    "v_permlane32_swap_b32 v37, v38\n\t"
    "v_mfma_f32_16x16x32_bf16 v[12:15], v[28:31], v[48:51], v[12:15]\n\t"
    "v_max_f32 v37, v37, v38\n\t"
    "v_cmp_gt_f32 vcc, v37, v32\n\t"
    "s_cbranch_vccz .Lr14_%=\n\t"
    "v_max_f32 v37, v37, v32\n\t"
    "v_sub_f32 v38, v32, v37\n\t"
    "v_mul_f32 v38, s64, v38\n\t"
    "v_exp_f32 v38, v38\n\t"
    "v_mov_b32 v32, v37\n\t"
    "v_mul_f32 v34, s65, v37\n\t"
    "v_mul_f32 v33, v33, v38\n\t"
    "v_mul_f32_dpp v8, v38, v8 quad_perm:[0,0,0,0] row_mask:0xf bank_mask:0xf\n\t"
    "v_mul_f32_dpp v9, v38, v9 quad_perm:[1,1,1,1] row_mask:0xf bank_mask:0xf\n\t"
    "v_mul_f32_dpp v10, v38, v10 quad_perm:[2,2,2,2] row_mask:0xf bank_mask:0xf\n\t"
    "v_mul_f32_dpp v11, v38, v11 quad_perm:[3,3,3,3] row_mask:0xf bank_mask:0xf\n\t"
    "v_mul_f32_dpp v12, v38, v12 quad_perm:[0,0,0,0] row_mask:0xf bank_mask:0xf\n\t"
    "v_mul_f32_dpp v13, v38, v13 quad_perm:[1,1,1,1] row_mask:0xf bank_mask:0xf\n\t"
    "v_mul_f32_dpp v14, v38, v14 quad_perm:[2,2,2,2] row_mask:0xf bank_mask:0xf\n\t"
    "v_mul_f32_dpp v15, v38, v15 quad_perm:[3,3,3,3] row_mask:0xf bank_mask:0xf\n\t"
    ".Lr14_%=:\n\t"
    "v_fma_f32 v35, v35, s64, v34\n\t"
    "v_fma_f32 v36, v36, s64, v34\n\t"
    "v_exp_f32 v35, v35\n\t"
    "v_exp_f32 v36, v36\n\t"
    "s_nop 0\n\t"
    "v_cvt_pk_bf16_f32 v39, v35, v36\n\t"
    "v_dot2_f32_bf16 v33, v39, s68, v33\n\t"
    "s_nop 0\n\t"
    "v_mov_b32_dpp v16, v39 quad_perm:[0,1,2,3] row_mask:0xf bank_mask:0x1\n\t"
    "v_mov_b32_dpp v17, v39 row_ror:12 row_mask:0xf bank_mask:0x1\n\t"
    "v_mov_b32_dpp v18, v39 row_ror:8 row_mask:0xf bank_mask:0x1\n\t"
    "v_mov_b32_dpp v19, v39 row_ror:4 row_mask:0xf bank_mask:0x1\n\t"
    "v_mov_b32_dpp v20, v39 row_ror:4 row_mask:0xf bank_mask:0x2\n\t"
    "v_mov_b32_dpp v21, v39 quad_perm:[0,1,2,3] row_mask:0xf bank_mask:0x2\n\t"
    "v_mov_b32_dpp v22, v39 row_ror:12 row_mask:0xf bank_mask:0x2\n\t"
    "v_mov_b32_dpp v23, v39 row_ror:8 row_mask:0xf bank_mask:0x2\n\t"
    "s_waitcnt vmcnt(9)\n\t"
    "s_add_u32 s57, s33, 0x2000\n\t"
    "s_xor_b32 s57, s57, s80\n\t"
    "buffer_load_dwordx4 v[96:99], v1, s[44:47], s57 offen offset:0 nt\n\t"
    "v_mov_b32_dpp v24, v39 row_ror:8 row_mask:0xf bank_mask:0x4\n\t"
    "v_mov_b32_dpp v25, v39 row_ror:4 row_mask:0xf bank_mask:0x4\n\t"
    "v_mov_b32_dpp v26, v39 quad_perm:[0,1,2,3] row_mask:0xf bank_mask:0x4\n\t"
    "v_mov_b32_dpp v27, v39 row_ror:12 row_mask:0xf bank_mask:0x4\n\t"
    "v_mov_b32_dpp v28, v39 row_ror:12 row_mask:0xf bank_mask:0x8\n\t"
    "v_mov_b32_dpp v29, v39 row_ror:8 row_mask:0xf bank_mask:0x8\n\t"
    "v_mov_b32_dpp v30, v39 row_ror:4 row_mask:0xf bank_mask:0x8\n\t"
    "v_mov_b32_dpp v31, v39 quad_perm:[0,1,2,3] row_mask:0xf bank_mask:0x8\n\t"
    "v_mfma_f32_16x16x32_bf16 v[44:47], a[152:155], v[4:7], 0\n\t"
    "s_waitcnt vmcnt(9)\n\t"
    "s_add_u32 s57, s33, 0x3000\n\t"
    "s_xor_b32 s57, s57, s80\n\t"
    "buffer_load_dwordx4 v[100:103], v1, s[44:47], s57 offen offset:0 nt\n\t"
    "v_mfma_f32_16x16x32_bf16 v[48:51], a[156:159], v[4:7], 0\n\t"
    "s_waitcnt vmcnt(9)\n\t"
    "s_add_u32 s57, s33, 0x4000\n\t"
    "s_xor_b32 s57, s57, s80\n\t"
    "buffer_load_dwordx4 v[104:107], v1, s[44:47], s57 offen offset:0 nt\n\t"
    "v_mfma_f32_16x16x32_bf16 v[52:55], a[160:163], v[4:7], 0\n\t"
    "v_add_f32_dpp v40, v45, v44 row_ror:12 row_mask:0xf bank_mask:0xf\n\t"
    "v_add_f32_dpp v42, v47, v46 row_ror:12 row_mask:0xf bank_mask:0xf\n\t"
    "s_waitcnt vmcnt(9)\n\t"
    "s_add_u32 s57, s33, 0x5000\n\t"
    "s_xor_b32 s57, s57, s80\n\t"
    "buffer_load_dwordx4 v[108:111], v1, s[44:47], s57 offen offset:0 nt\n\t"
    "v_mfma_f32_16x16x32_bf16 v[44:47], a[164:167], v[4:7], 0\n\t"
    "v_add_f32_dpp v41, v50, v49 row_ror:12 row_mask:0xf bank_mask:0xf\n\t"
    "v_add_f32_dpp v43, v48, v51 row_ror:12 row_mask:0xf bank_mask:0xf\n\t"
    "v_add_f32_dpp v35, v42, v40 row_ror:8 row_mask:0xf bank_mask:0x1\n\t"
    "s_waitcnt vmcnt(9)\n\t"
    "s_add_u32 s57, s33, 0x6000\n\t"
    "s_xor_b32 s57, s57, s80\n\t"
    "buffer_load_dwordx4 v[112:115], v1, s[44:47], s57 offen offset:0 nt\n\t"
    "v_mfma_f32_16x16x32_bf16 v[48:51], a[168:171], v[4:7], 0\n\t"
    "v_add_f32_dpp v40, v55, v54 row_ror:12 row_mask:0xf bank_mask:0xf\n\t"
    "v_add_f32_dpp v42, v53, v52 row_ror:12 row_mask:0xf bank_mask:0xf\n\t"
    "v_add_f32_dpp v35, v43, v41 row_ror:8 row_mask:0xf bank_mask:0x2\n\t"
    "s_waitcnt vmcnt(9)\n\t"
    "s_add_u32 s57, s33, 0x7000\n\t"
    "s_xor_b32 s57, s57, s80\n\t"
    "buffer_load_dwordx4 v[116:119], v1, s[44:47], s57 offen offset:0 nt\n\t"
    "v_mfma_f32_16x16x32_bf16 v[52:55], a[172:175], v[4:7], 0\n\t"
    "v_add_f32_dpp v41, v44, v47 row_ror:12 row_mask:0xf bank_mask:0xf\n\t"
    "v_add_f32_dpp v43, v46, v45 row_ror:12 row_mask:0xf bank_mask:0xf\n\t"
    "v_add_f32_dpp v35, v42, v40 row_ror:8 row_mask:0xf bank_mask:0x4\n\t"
    "s_waitcnt vmcnt(9)\n\t"
    "s_add_u32 s32, s34, 0x60000\n\t"
    "s_and_b32 s32, s32, 0x7ffff\n\t"
    "s_add_u32 s32, s32, s56\n\t"
    "s_add_u32 s57, s32, 0\n\t"
    "s_xor_b32 s57, s57, s80\n\t"
    "buffer_load_dwordx4 a[184:187], v0, s[40:43], s57 offen offset:0\n\t"
    "v_mfma_f32_16x16x32_bf16 v[44:47], a[176:179], v[4:7], 0\n\t"
    "v_add_f32_dpp v40, v49, v48 row_ror:12 row_mask:0xf bank_mask:0xf\n\t"
    "v_add_f32_dpp v42, v51, v50 row_ror:12 row_mask:0xf bank_mask:0xf\n\t"
    "v_add_f32_dpp v35, v43, v41 row_ror:8 row_mask:0xf bank_mask:0x8\n\t"
    "s_waitcnt vmcnt(9)\n\t"
    "s_add_u32 s57, s32, 0x1000\n\t"
    "s_xor_b32 s57, s57, s80\n\t"
    "buffer_load_dwordx4 a[188:191], v0, s[40:43], s57 offen offset:0\n\t"
    "v_mfma_f32_16x16x32_bf16 v[48:51], a[180:183], v[4:7], 0\n\t"
    "v_add_f32_dpp v41, v54, v53 row_ror:12 row_mask:0xf bank_mask:0xf\n\t"
    "v_add_f32_dpp v43, v52, v55 row_ror:12 row_mask:0xf bank_mask:0xf\n\t"
    "v_add_f32_dpp v36, v42, v40 row_ror:8 row_mask:0xf bank_mask:0x1\n\t"
    "v_add_f32_dpp v40, v47, v46 row_ror:12 row_mask:0xf bank_mask:0xf\n\t"
    "v_add_f32_dpp v42, v45, v44 row_ror:12 row_mask:0xf bank_mask:0xf\n\t"
    "v_add_f32_dpp v36, v43, v41 row_ror:8 row_mask:0xf bank_mask:0x2\n\t"
    "s_nop 1\n\t"
    "v_add_f32_dpp v41, v48, v51 row_ror:12 row_mask:0xf bank_mask:0xf\n\t"
    "v_add_f32_dpp v43, v50, v49 row_ror:12 row_mask:0xf bank_mask:0xf\n\t"
    "v_add_f32_dpp v36, v42, v40 row_ror:8 row_mask:0xf bank_mask:0x4\n\t"
    "s_nop 0\n\t"
    "v_add_f32_dpp v36, v43, v41 row_ror:8 row_mask:0xf bank_mask:0x8\n\t"
    "s_waitcnt vmcnt(9)\n\t"
    "s_add_u32 s57, s32, 0x2000\n\t"
    "s_xor_b32 s57, s57, s80\n\t"
    "buffer_load_dwordx4 a[192:195], v0, s[40:43], s57 offen offset:0\n\t"
    "s_waitcnt vmcnt(9)\n\t"
    "s_add_u32 s57, s32, 0x3000\n\t"
    "s_xor_b32 s57, s57, s80\n\t"
    "buffer_load_dwordx4 a[196:199], v0, s[40:43], s57 offen offset:0\n\t"
    "s_waitcnt vmcnt(9)\n\t"
    "s_add_u32 s57, s32, 0x4000\n\t"
    "s_xor_b32 s57, s57, s80\n\t"
    "buffer_load_dwordx4 a[200:203], v0, s[40:43], s57 offen offset:0\n\t"
    "s_waitcnt vmcnt(9)\n\t"
    "s_add_u32 s57, s32, 0x5000\n\t"
    "s_xor_b32 s57, s57, s80\n\t"
    "buffer_load_dwordx4 a[204:207], v0, s[40:43], s57 offen offset:0\n\t"
    "s_waitcnt vmcnt(9)\n\t"
    "s_add_u32 s57, s32, 0x6000\n\t"
    "s_xor_b32 s57, s57, s80\n\t"
    "buffer_load_dwordx4 a[208:211], v0, s[40:43], s57 offen offset:0\n\t"
    "s_waitcnt vmcnt(9)\n\t"
    "s_add_u32 s57, s32, 0x7000\n\t"
    "s_xor_b32 s57, s57, s80\n\t"
    "buffer_load_dwordx4 a[212:215], v0, s[40:43], s57 offen offset:0\n\t"
    "s_waitcnt vmcnt(9)\n\t"
    "s_add_u32 s33, s34, 0x58000\n\t"
    "s_and_b32 s33, s33, 0x7ffff\n\t"
    "s_add_u32 s33, s33, s56\n\t"
    "s_add_u32 s57, s33, 0\n\t"
    "s_xor_b32 s57, s57, s80\n\t"
    "buffer_load_dwordx4 v[128:131], v1, s[44:47], s57 offen offset:0 nt\n\t"
    "s_waitcnt vmcnt(9)\n\t"
    "s_add_u32 s57, s33, 0x1000\n\t"
    "s_xor_b32 s57, s57, s80\n\t"
    "buffer_load_dwordx4 v[132:135], v1, s[44:47], s57 offen offset:0 nt\n\t"
    "v_perm_b32 v44, v104, v88, s66\n\t"
    "v_perm_b32 v45, v108, v92, s66\n\t"
    "v_perm_b32 v46, v112, v96, s66\n\t"
    "v_perm_b32 v47, v116, v100, s66\n\t"
    "v_perm_b32 v48, v104, v88, s67\n\t"
    "v_perm_b32 v49, v108, v92, s67\n\t"
    "v_perm_b32 v50, v112, v96, s67\n\t"
    "v_perm_b32 v51, v116, v100, s67\n\t"
    "v_max_f32 v37, v35, v36\n\t"
    "v_mfma_f32_16x16x32_bf16 v[8:11], v[16:19], v[44:47], v[8:11]\n\t"
    "v_perm_b32 v52, v105, v89, s66\n\t"
    "v_perm_b32 v53, v109, v93, s66\n\t"
    "v_perm_b32 v54, v113, v97, s66\n\t"
    "v_perm_b32 v55, v117, v101, s66\n\t"
    "v_max_f32_dpp v37, v37, v37 row_ror:4 row_mask:0xf bank_mask:0xf\n\t"
    "v_mfma_f32_16x16x32_bf16 v[8:11], v[20:23], v[48:51], v[8:11]\n\t"
    "v_perm_b32 v44, v105, v89, s67\n\t"
    "v_perm_b32 v45, v109, v93, s67\n\t"
    "v_perm_b32 v46, v113, v97, s67\n\t"
    "v_perm_b32 v47, v117, v101, s67\n\t"
    "v_max_f32_dpp v37, v37, v37 row_ror:8 row_mask:0xf bank_mask:0xf\n\t"
    "v_mfma_f32_16x16x32_bf16 v[8:11], v[24:27], v[52:55], v[8:11]\n\t"
    "v_perm_b32 v48, v106, v90, s66\n\t"
    "v_perm_b32 v49, v110, v94, s66\n\t"
    "v_perm_b32 v50, v114, v98, s66\n\t"
    "v_perm_b32 v51, v118, v102, s66\n\t"
    "v_mov_b32 v38, v37\n\t"
    "v_mfma_f32_16x16x32_bf16 v[8:11], v[28:31], v[44:47], v[8:11]\n\t"
    "v_perm_b32 v52, v106, v90, s67\n\t"
    "v_perm_b32 v53, v110, v94, s67\n\t"
    "v_perm_b32 v54, v114, v98, s67\n\t"
    "v_perm_b32 v55, v118, v102, s67\n\t"
    "v_permlane16_swap_b32 v37, v38\n\t"
    "v_mfma_f32_16x16x32_bf16 v[12:15], v[16:19], v[48:51], v[12:15]\n\t"
    "v_perm_b32 v44, v107, v91, s66\n\t"
    "v_perm_b32 v45, v111, v95, s66\n\t"
    "v_perm_b32 v46, v115, v99, s66\n\t"
    "v_perm_b32 v47, v119, v103, s66\n\t"
    "v_max_f32 v37, v37, v38\n\t"
    "v_mfma_f32_16x16x32_bf16 v[12:15], v[20:23], v[52:55], v[12:15]\n\t"
    "v_perm_b32 v48, v107, v91, s67\n\t"
    "v_perm_b32 v49, v111, v95, s67\n\t"
    "v_perm_b32 v50, v115, v99, s67\n\t"
    "v_perm_b32 v51, v119, v103, s67\n\t"
    "v_mov_b32 v38, v37\n\t"
    "v_mfma_f32_16x16x32_bf16 v[12:15], v[24:27], v[44:47], v[12:15]\n\t"
    "s_nop 0\n\t"
    "v_permlane32_swap_b32 v37, v38\n\t"
    "v_mfma_f32_16x16x32_bf16 v[12:15], v[28:31], v[48:51], v[12:15]\n\t"
    "v_max_f32 v37, v37, v38\n\t"
    "v_cmp_gt_f32 vcc, v37, v32\n\t"
    "s_cbranch_vccz .Lr15_%=\n\t"
    "v_max_f32 v37, v37, v32\n\t"
    "v_sub_f32 v38, v32, v37\n\t"
    "v_mul_f32 v38, s64, v38\n\t"
    "v_exp_f32 v38, v38\n\t"
    "v_mov_b32 v32, v37\n\t"
    "v_mul_f32 v34, s65, v37\n\t"
    "v_mul_f32 v33, v33, v38\n\t"
    "v_mul_f32_dpp v8, v38, v8 quad_perm:[0,0,0,0] row_mask:0xf bank_mask:0xf\n\t"
    "v_mul_f32_dpp v9, v38, v9 quad_perm:[1,1,1,1] row_mask:0xf bank_mask:0xf\n\t"
    "v_mul_f32_dpp v10, v38, v10 quad_perm:[2,2,2,2] row_mask:0xf bank_mask:0xf\n\t"
    "v_mul_f32_dpp v11, v38, v11 quad_perm:[3,3,3,3] row_mask:0xf bank_mask:0xf\n\t"
    "v_mul_f32_dpp v12, v38, v12 quad_perm:[0,0,0,0] row_mask:0xf bank_mask:0xf\n\t"
    "v_mul_f32_dpp v13, v38, v13 quad_perm:[1,1,1,1] row_mask:0xf bank_mask:0xf\n\t"
    "v_mul_f32_dpp v14, v38, v14 quad_perm:[2,2,2,2] row_mask:0xf bank_mask:0xf\n\t"
    "v_mul_f32_dpp v15, v38, v15 quad_perm:[3,3,3,3] row_mask:0xf bank_mask:0xf\n\t"
    ".Lr15_%=:\n\t"
    "v_fma_f32 v35, v35, s64, v34\n\t"
    "v_fma_f32 v36, v36, s64, v34\n\t"
    "v_exp_f32 v35, v35\n\t"
    "v_exp_f32 v36, v36\n\t"
    "s_nop 0\n\t"
    "v_cvt_pk_bf16_f32 v39, v35, v36\n\t"
    "v_dot2_f32_bf16 v33, v39, s68, v33\n\t"
    "s_nop 0\n\t"
    "v_mov_b32_dpp v16, v39 quad_perm:[0,1,2,3] row_mask:0xf bank_mask:0x1\n\t"
    "v_mov_b32_dpp v17, v39 row_ror:12 row_mask:0xf bank_mask:0x1\n\t"
    "v_mov_b32_dpp v18, v39 row_ror:8 row_mask:0xf bank_mask:0x1\n\t"
    "v_mov_b32_dpp v19, v39 row_ror:4 row_mask:0xf bank_mask:0x1\n\t"
    "v_mov_b32_dpp v20, v39 row_ror:4 row_mask:0xf bank_mask:0x2\n\t"
    "v_mov_b32_dpp v21, v39 quad_perm:[0,1,2,3] row_mask:0xf bank_mask:0x2\n\t"
    "v_mov_b32_dpp v22, v39 row_ror:12 row_mask:0xf bank_mask:0x2\n\t"
    "v_mov_b32_dpp v23, v39 row_ror:8 row_mask:0xf bank_mask:0x2\n\t"
    "s_waitcnt vmcnt(9)\n\t"
    "s_add_u32 s57, s33, 0x2000\n\t"
    "s_xor_b32 s57, s57, s80\n\t"
    "buffer_load_dwordx4 v[136:139], v1, s[44:47], s57 offen offset:0 nt\n\t"
    "v_mov_b32_dpp v24, v39 row_ror:8 row_mask:0xf bank_mask:0x4\n\t"
    "v_mov_b32_dpp v25, v39 row_ror:4 row_mask:0xf bank_mask:0x4\n\t"
    "v_mov_b32_dpp v26, v39 quad_perm:[0,1,2,3] row_mask:0xf bank_mask:0x4\n\t"
    "v_mov_b32_dpp v27, v39 row_ror:12 row_mask:0xf bank_mask:0x4\n\t"
    "v_mov_b32_dpp v28, v39 row_ror:12 row_mask:0xf bank_mask:0x8\n\t"
    "v_mov_b32_dpp v29, v39 row_ror:8 row_mask:0xf bank_mask:0x8\n\t"
    "v_mov_b32_dpp v30, v39 row_ror:4 row_mask:0xf bank_mask:0x8\n\t"
    "v_mov_b32_dpp v31, v39 quad_perm:[0,1,2,3] row_mask:0xf bank_mask:0x8\n\t"
    "v_mfma_f32_16x16x32_bf16 v[44:47], a[184:187], v[4:7], 0\n\t"
    "s_waitcnt vmcnt(9)\n\t"
    "s_add_u32 s57, s33, 0x3000\n\t"
    "s_xor_b32 s57, s57, s80\n\t"
    "buffer_load_dwordx4 v[140:143], v1, s[44:47], s57 offen offset:0 nt\n\t"
    "v_mfma_f32_16x16x32_bf16 v[48:51], a[188:191], v[4:7], 0\n\t"
    "s_waitcnt vmcnt(9)\n\t"
    "s_add_u32 s57, s33, 0x4000\n\t"
    "s_xor_b32 s57, s57, s80\n\t"
    "buffer_load_dwordx4 v[144:147], v1, s[44:47], s57 offen offset:0 nt\n\t"
    "v_mfma_f32_16x16x32_bf16 v[52:55], a[192:195], v[4:7], 0\n\t"
    "v_add_f32_dpp v40, v45, v44 row_ror:12 row_mask:0xf bank_mask:0xf\n\t"
    "v_add_f32_dpp v42, v47, v46 row_ror:12 row_mask:0xf bank_mask:0xf\n\t"
    "s_waitcnt vmcnt(9)\n\t"
    "s_add_u32 s57, s33, 0x5000\n\t"
    "s_xor_b32 s57, s57, s80\n\t"
    "buffer_load_dwordx4 v[148:151], v1, s[44:47], s57 offen offset:0 nt\n\t"
    "v_mfma_f32_16x16x32_bf16 v[44:47], a[196:199], v[4:7], 0\n\t"
    "v_add_f32_dpp v41, v50, v49 row_ror:12 row_mask:0xf bank_mask:0xf\n\t"
    "v_add_f32_dpp v43, v48, v51 row_ror:12 row_mask:0xf bank_mask:0xf\n\t"
    "v_add_f32_dpp v35, v42, v40 row_ror:8 row_mask:0xf bank_mask:0x1\n\t"
    "s_waitcnt vmcnt(9)\n\t"
    "s_add_u32 s57, s33, 0x6000\n\t"
    "s_xor_b32 s57, s57, s80\n\t"
    "buffer_load_dwordx4 v[152:155], v1, s[44:47], s57 offen offset:0 nt\n\t"
    "v_mfma_f32_16x16x32_bf16 v[48:51], a[200:203], v[4:7], 0\n\t"
    "v_add_f32_dpp v40, v55, v54 row_ror:12 row_mask:0xf bank_mask:0xf\n\t"
    "v_add_f32_dpp v42, v53, v52 row_ror:12 row_mask:0xf bank_mask:0xf\n\t"
    "v_add_f32_dpp v35, v43, v41 row_ror:8 row_mask:0xf bank_mask:0x2\n\t"
    "s_waitcnt vmcnt(9)\n\t"
    "s_add_u32 s57, s33, 0x7000\n\t"
    "s_xor_b32 s57, s57, s80\n\t"
    "buffer_load_dwordx4 v[156:159], v1, s[44:47], s57 offen offset:0 nt\n\t"
    "v_mfma_f32_16x16x32_bf16 v[52:55], a[204:207], v[4:7], 0\n\t"
    "v_add_f32_dpp v41, v44, v47 row_ror:12 row_mask:0xf bank_mask:0xf\n\t"
    "v_add_f32_dpp v43, v46, v45 row_ror:12 row_mask:0xf bank_mask:0xf\n\t"
    "v_add_f32_dpp v35, v42, v40 row_ror:8 row_mask:0xf bank_mask:0x4\n\t"
    "s_waitcnt vmcnt(9)\n\t"
    "s_add_u32 s32, s34, 0x68000\n\t"
    "s_and_b32 s32, s32, 0x7ffff\n\t"
    "s_add_u32 s32, s32, s56\n\t"
    "s_add_u32 s57, s32, 0\n\t"
    "s_xor_b32 s57, s57, s80\n\t"
    "buffer_load_dwordx4 a[216:219], v0, s[40:43], s57 offen offset:0\n\t"
    "v_mfma_f32_16x16x32_bf16 v[44:47], a[208:211], v[4:7], 0\n\t"
    "v_add_f32_dpp v40, v49, v48 row_ror:12 row_mask:0xf bank_mask:0xf\n\t"
    "v_add_f32_dpp v42, v51, v50 row_ror:12 row_mask:0xf bank_mask:0xf\n\t"
    "v_add_f32_dpp v35, v43, v41 row_ror:8 row_mask:0xf bank_mask:0x8\n\t"
    "s_waitcnt vmcnt(9)\n\t"
    "s_add_u32 s57, s32, 0x1000\n\t"
    "s_xor_b32 s57, s57, s80\n\t"
    "buffer_load_dwordx4 a[220:223], v0, s[40:43], s57 offen offset:0\n\t"
    "v_mfma_f32_16x16x32_bf16 v[48:51], a[212:215], v[4:7], 0\n\t"
    "v_add_f32_dpp v41, v54, v53 row_ror:12 row_mask:0xf bank_mask:0xf\n\t"
    "v_add_f32_dpp v43, v52, v55 row_ror:12 row_mask:0xf bank_mask:0xf\n\t"
    "v_add_f32_dpp v36, v42, v40 row_ror:8 row_mask:0xf bank_mask:0x1\n\t"
    "v_add_f32_dpp v40, v47, v46 row_ror:12 row_mask:0xf bank_mask:0xf\n\t"
    "v_add_f32_dpp v42, v45, v44 row_ror:12 row_mask:0xf bank_mask:0xf\n\t"
    "v_add_f32_dpp v36, v43, v41 row_ror:8 row_mask:0xf bank_mask:0x2\n\t"
    "s_nop 1\n\t"
    "v_add_f32_dpp v41, v48, v51 row_ror:12 row_mask:0xf bank_mask:0xf\n\t"
    "v_add_f32_dpp v43, v50, v49 row_ror:12 row_mask:0xf bank_mask:0xf\n\t"
    "v_add_f32_dpp v36, v42, v40 row_ror:8 row_mask:0xf bank_mask:0x4\n\t"
    "s_nop 0\n\t"
    "v_add_f32_dpp v36, v43, v41 row_ror:8 row_mask:0xf bank_mask:0x8\n\t"
    "s_waitcnt vmcnt(9)\n\t"
    "s_add_u32 s57, s32, 0x2000\n\t"
    "s_xor_b32 s57, s57, s80\n\t"
    "buffer_load_dwordx4 a[224:227], v0, s[40:43], s57 offen offset:0\n\t"
    "s_waitcnt vmcnt(9)\n\t"
    "s_add_u32 s57, s32, 0x3000\n\t"
    "s_xor_b32 s57, s57, s80\n\t"
    "buffer_load_dwordx4 a[228:231], v0, s[40:43], s57 offen offset:0\n\t"
    "s_waitcnt vmcnt(9)\n\t"
    "s_add_u32 s57, s32, 0x4000\n\t"
    "s_xor_b32 s57, s57, s80\n\t"
    "buffer_load_dwordx4 a[232:235], v0, s[40:43], s57 offen offset:0\n\t"
    "s_waitcnt vmcnt(9)\n\t"
    "s_add_u32 s57, s32, 0x5000\n\t"
    "s_xor_b32 s57, s57, s80\n\t"
    "buffer_load_dwordx4 a[236:239], v0, s[40:43], s57 offen offset:0\n\t"
    "s_waitcnt vmcnt(9)\n\t"
    "s_add_u32 s57, s32, 0x6000\n\t"
    "s_xor_b32 s57, s57, s80\n\t"
    "buffer_load_dwordx4 a[240:243], v0, s[40:43], s57 offen offset:0\n\t"
    "s_waitcnt vmcnt(9)\n\t"
    "s_add_u32 s57, s32, 0x7000\n\t"
    "s_xor_b32 s57, s57, s80\n\t"
    "buffer_load_dwordx4 a[244:247], v0, s[40:43], s57 offen offset:0\n\t"
    "s_waitcnt vmcnt(9)\n\t"
    "s_add_u32 s33, s34, 0x60000\n\t"
    "s_and_b32 s33, s33, 0x7ffff\n\t"
    "s_add_u32 s33, s33, s56\n\t"
    "s_add_u32 s57, s33, 0\n\t"
    "s_xor_b32 s57, s57, s80\n\t"
    "buffer_load_dwordx4 v[160:163], v1, s[44:47], s57 offen offset:0\n\t"
    "s_waitcnt vmcnt(9)\n\t"
    "s_add_u32 s57, s33, 0x1000\n\t"
    "s_xor_b32 s57, s57, s80\n\t"
    "buffer_load_dwordx4 v[164:167], v1, s[44:47], s57 offen offset:0\n\t"
    "v_perm_b32 v44, v144, v128, s66\n\t"
    "v_perm_b32 v45, v148, v132, s66\n\t"
    "v_perm_b32 v46, v152, v136, s66\n\t"
    "v_perm_b32 v47, v156, v140, s66\n\t"
    "v_perm_b32 v48, v144, v128, s67\n\t"
    "v_perm_b32 v49, v148, v132, s67\n\t"
    "v_perm_b32 v50, v152, v136, s67\n\t"
    "v_perm_b32 v51, v156, v140, s67\n\t"
    "v_max_f32 v37, v35, v36\n\t"
    "v_mfma_f32_16x16x32_bf16 v[8:11], v[16:19], v[44:47], v[8:11]\n\t"
    "v_perm_b32 v52, v145, v129, s66\n\t"
    "v_perm_b32 v53, v149, v133, s66\n\t"
    "v_perm_b32 v54, v153, v137, s66\n\t"
    "v_perm_b32 v55, v157, v141, s66\n\t"
    "v_max_f32_dpp v37, v37, v37 row_ror:4 row_mask:0xf bank_mask:0xf\n\t"
    "v_mfma_f32_16x16x32_bf16 v[8:11], v[20:23], v[48:51], v[8:11]\n\t"
    "v_perm_b32 v44, v145, v129, s67\n\t"
    "v_perm_b32 v45, v149, v133, s67\n\t"
    "v_perm_b32 v46, v153, v137, s67\n\t"
    "v_perm_b32 v47, v157, v141, s67\n\t"
    "v_max_f32_dpp v37, v37, v37 row_ror:8 row_mask:0xf bank_mask:0xf\n\t"
    "v_mfma_f32_16x16x32_bf16 v[8:11], v[24:27], v[52:55], v[8:11]\n\t"
    "v_perm_b32 v48, v146, v130, s66\n\t"
    "v_perm_b32 v49, v150, v134, s66\n\t"
    "v_perm_b32 v50, v154, v138, s66\n\t"
    "v_perm_b32 v51, v158, v142, s66\n\t"
    "v_mov_b32 v38, v37\n\t"
    "v_mfma_f32_16x16x32_bf16 v[8:11], v[28:31], v[44:47], v[8:11]\n\t"
    "v_perm_b32 v52, v146, v130, s67\n\t"
    "v_perm_b32 v53, v150, v134, s67\n\t"
    "v_perm_b32 v54, v154, v138, s67\n\t"
    "v_perm_b32 v55, v158, v142, s67\n\t"
    "v_permlane16_swap_b32 v37, v38\n\t"
    "v_mfma_f32_16x16x32_bf16 v[12:15], v[16:19], v[48:51], v[12:15]\n\t"
    "v_perm_b32 v44, v147, v131, s66\n\t"
    "v_perm_b32 v45, v151, v135, s66\n\t"
    "v_perm_b32 v46, v155, v139, s66\n\t"
    "v_perm_b32 v47, v159, v143, s66\n\t"
    "v_max_f32 v37, v37, v38\n\t"
    "v_mfma_f32_16x16x32_bf16 v[12:15], v[20:23], v[52:55], v[12:15]\n\t"
    "v_perm_b32 v48, v147, v131, s67\n\t"
    "v_perm_b32 v49, v151, v135, s67\n\t"
    "v_perm_b32 v50, v155, v139, s67\n\t"
    "v_perm_b32 v51, v159, v143, s67\n\t"
    "v_mov_b32 v38, v37\n\t"
    "v_mfma_f32_16x16x32_bf16 v[12:15], v[24:27], v[44:47], v[12:15]\n\t"
    "s_nop 0\n\t"
    "v_permlane32_swap_b32 v37, v38\n\t"
    "v_mfma_f32_16x16x32_bf16 v[12:15], v[28:31], v[48:51], v[12:15]\n\t"
    "v_max_f32 v37, v37, v38\n\t"
    "v_cmp_gt_f32 vcc, v37, v32\n\t"
    "s_cbranch_vccz .Lr16_%=\n\t"
    "v_max_f32 v37, v37, v32\n\t"
    "v_sub_f32 v38, v32, v37\n\t"
    "v_mul_f32 v38, s64, v38\n\t"
    "v_exp_f32 v38, v38\n\t"
    "v_mov_b32 v32, v37\n\t"
    "v_mul_f32 v34, s65, v37\n\t"
    "v_mul_f32 v33, v33, v38\n\t"
    "v_mul_f32_dpp v8, v38, v8 quad_perm:[0,0,0,0] row_mask:0xf bank_mask:0xf\n\t"
    "v_mul_f32_dpp v9, v38, v9 quad_perm:[1,1,1,1] row_mask:0xf bank_mask:0xf\n\t"
    "v_mul_f32_dpp v10, v38, v10 quad_perm:[2,2,2,2] row_mask:0xf bank_mask:0xf\n\t"
    "v_mul_f32_dpp v11, v38, v11 quad_perm:[3,3,3,3] row_mask:0xf bank_mask:0xf\n\t"
    "v_mul_f32_dpp v12, v38, v12 quad_perm:[0,0,0,0] row_mask:0xf bank_mask:0xf\n\t"
    "v_mul_f32_dpp v13, v38, v13 quad_perm:[1,1,1,1] row_mask:0xf bank_mask:0xf\n\t"
    "v_mul_f32_dpp v14, v38, v14 quad_perm:[2,2,2,2] row_mask:0xf bank_mask:0xf\n\t"
    "v_mul_f32_dpp v15, v38, v15 quad_perm:[3,3,3,3] row_mask:0xf bank_mask:0xf\n\t"
    ".Lr16_%=:\n\t"
    "v_fma_f32 v35, v35, s64, v34\n\t"
    "v_fma_f32 v36, v36, s64, v34\n\t"
    "v_exp_f32 v35, v35\n\t"
    "v_exp_f32 v36, v36\n\t"
    "s_nop 0\n\t"
    "v_cvt_pk_bf16_f32 v39, v35, v36\n\t"
    "v_dot2_f32_bf16 v33, v39, s68, v33\n\t"
    "s_nop 0\n\t"
    "v_mov_b32_dpp v16, v39 quad_perm:[0,1,2,3] row_mask:0xf bank_mask:0x1\n\t"
    "v_mov_b32_dpp v17, v39 row_ror:12 row_mask:0xf bank_mask:0x1\n\t"
    "v_mov_b32_dpp v18, v39 row_ror:8 row_mask:0xf bank_mask:0x1\n\t"
    "v_mov_b32_dpp v19, v39 row_ror:4 row_mask:0xf bank_mask:0x1\n\t"
    "v_mov_b32_dpp v20, v39 row_ror:4 row_mask:0xf bank_mask:0x2\n\t"
    "v_mov_b32_dpp v21, v39 quad_perm:[0,1,2,3] row_mask:0xf bank_mask:0x2\n\t"
    "v_mov_b32_dpp v22, v39 row_ror:12 row_mask:0xf bank_mask:0x2\n\t"
    "v_mov_b32_dpp v23, v39 row_ror:8 row_mask:0xf bank_mask:0x2\n\t"
    "s_waitcnt vmcnt(9)\n\t"
    "s_add_u32 s57, s33, 0x2000\n\t"
    "s_xor_b32 s57, s57, s80\n\t"
    "buffer_load_dwordx4 v[168:171], v1, s[44:47], s57 offen offset:0\n\t"
    "v_mov_b32_dpp v24, v39 row_ror:8 row_mask:0xf bank_mask:0x4\n\t"
    "v_mov_b32_dpp v25, v39 row_ror:4 row_mask:0xf bank_mask:0x4\n\t"
    "v_mov_b32_dpp v26, v39 quad_perm:[0,1,2,3] row_mask:0xf bank_mask:0x4\n\t"
    "v_mov_b32_dpp v27, v39 row_ror:12 row_mask:0xf bank_mask:0x4\n\t"
    "v_mov_b32_dpp v28, v39 row_ror:12 row_mask:0xf bank_mask:0x8\n\t"
    "v_mov_b32_dpp v29, v39 row_ror:8 row_mask:0xf bank_mask:0x8\n\t"
    "v_mov_b32_dpp v30, v39 row_ror:4 row_mask:0xf bank_mask:0x8\n\t"
    "v_mov_b32_dpp v31, v39 quad_perm:[0,1,2,3] row_mask:0xf bank_mask:0x8\n\t"
    "v_mfma_f32_16x16x32_bf16 v[44:47], a[216:219], v[4:7], 0\n\t"
    "s_waitcnt vmcnt(9)\n\t"
    "s_add_u32 s57, s33, 0x3000\n\t"
    "s_xor_b32 s57, s57, s80\n\t"
    "buffer_load_dwordx4 v[172:175], v1, s[44:47], s57 offen offset:0\n\t"
    "v_mfma_f32_16x16x32_bf16 v[48:51], a[220:223], v[4:7], 0\n\t"
    "s_waitcnt vmcnt(9)\n\t"
    "s_add_u32 s57, s33, 0x4000\n\t"
    "s_xor_b32 s57, s57, s80\n\t"
    "buffer_load_dwordx4 v[220:223], v1, s[44:47], s57 offen offset:0\n\t"
    "v_mfma_f32_16x16x32_bf16 v[52:55], a[224:227], v[4:7], 0\n\t"
    "v_add_f32_dpp v40, v45, v44 row_ror:12 row_mask:0xf bank_mask:0xf\n\t"
    "v_add_f32_dpp v42, v47, v46 row_ror:12 row_mask:0xf bank_mask:0xf\n\t"
    "s_waitcnt vmcnt(9)\n\t"
    "s_add_u32 s57, s33, 0x5000\n\t"
    "s_xor_b32 s57, s57, s80\n\t"
    "buffer_load_dwordx4 v[216:219], v1, s[44:47], s57 offen offset:0\n\t"
    "v_mfma_f32_16x16x32_bf16 v[44:47], a[228:231], v[4:7], 0\n\t"
    "v_add_f32_dpp v41, v50, v49 row_ror:12 row_mask:0xf bank_mask:0xf\n\t"
    "v_add_f32_dpp v43, v48, v51 row_ror:12 row_mask:0xf bank_mask:0xf\n\t"
    "v_add_f32_dpp v35, v42, v40 row_ror:8 row_mask:0xf bank_mask:0x1\n\t"
    "s_waitcnt vmcnt(9)\n\t"
    "s_add_u32 s57, s33, 0x6000\n\t"
    "s_xor_b32 s57, s57, s80\n\t"
    "buffer_load_dwordx4 v[212:215], v1, s[44:47], s57 offen offset:0\n\t"
    "v_mfma_f32_16x16x32_bf16 v[48:51], a[232:235], v[4:7], 0\n\t"
    "v_add_f32_dpp v40, v55, v54 row_ror:12 row_mask:0xf bank_mask:0xf\n\t"
    "v_add_f32_dpp v42, v53, v52 row_ror:12 row_mask:0xf bank_mask:0xf\n\t"
    "v_add_f32_dpp v35, v43, v41 row_ror:8 row_mask:0xf bank_mask:0x2\n\t"
    "s_waitcnt vmcnt(9)\n\t"
    "s_add_u32 s57, s33, 0x7000\n\t"
    "s_xor_b32 s57, s57, s80\n\t"
    "buffer_load_dwordx4 v[208:211], v1, s[44:47], s57 offen offset:0\n\t"
    "v_mfma_f32_16x16x32_bf16 v[52:55], a[236:239], v[4:7], 0\n\t"
    "v_add_f32_dpp v41, v44, v47 row_ror:12 row_mask:0xf bank_mask:0xf\n\t"
    "v_add_f32_dpp v43, v46, v45 row_ror:12 row_mask:0xf bank_mask:0xf\n\t"
    "v_add_f32_dpp v35, v42, v40 row_ror:8 row_mask:0xf bank_mask:0x4\n\t"
    "s_waitcnt vmcnt(9)\n\t"
    "s_add_u32 s32, s34, 0x70000\n\t"
    "s_and_b32 s32, s32, 0x7ffff\n\t"
    "s_add_u32 s32, s32, s56\n\t"
    "s_add_u32 s57, s32, 0\n\t"
    "s_xor_b32 s57, s57, s80\n\t"
    "buffer_load_dwordx4 a[184:187], v0, s[40:43], s57 offen offset:0 nt\n\t"
    "v_mfma_f32_16x16x32_bf16 v[44:47], a[240:243], v[4:7], 0\n\t"
    "v_add_f32_dpp v40, v49, v48 row_ror:12 row_mask:0xf bank_mask:0xf\n\t"
    "v_add_f32_dpp v42, v51, v50 row_ror:12 row_mask:0xf bank_mask:0xf\n\t"
    "v_add_f32_dpp v35, v43, v41 row_ror:8 row_mask:0xf bank_mask:0x8\n\t"
    "s_waitcnt vmcnt(9)\n\t"
    "s_add_u32 s57, s32, 0x1000\n\t"
    "s_xor_b32 s57, s57, s80\n\t"
    "buffer_load_dwordx4 a[188:191], v0, s[40:43], s57 offen offset:0 nt\n\t"
    "v_mfma_f32_16x16x32_bf16 v[48:51], a[244:247], v[4:7], 0\n\t"
    "v_add_f32_dpp v41, v54, v53 row_ror:12 row_mask:0xf bank_mask:0xf\n\t"
    "v_add_f32_dpp v43, v52, v55 row_ror:12 row_mask:0xf bank_mask:0xf\n\t"
    "v_add_f32_dpp v36, v42, v40 row_ror:8 row_mask:0xf bank_mask:0x1\n\t"
    "v_add_f32_dpp v40, v47, v46 row_ror:12 row_mask:0xf bank_mask:0xf\n\t"
    "v_add_f32_dpp v42, v45, v44 row_ror:12 row_mask:0xf bank_mask:0xf\n\t"
    "v_add_f32_dpp v36, v43, v41 row_ror:8 row_mask:0xf bank_mask:0x2\n\t"
    "s_nop 1\n\t"
    "v_add_f32_dpp v41, v48, v51 row_ror:12 row_mask:0xf bank_mask:0xf\n\t"
    "v_add_f32_dpp v43, v50, v49 row_ror:12 row_mask:0xf bank_mask:0xf\n\t"
    "v_add_f32_dpp v36, v42, v40 row_ror:8 row_mask:0xf bank_mask:0x4\n\t"
    "s_nop 0\n\t"
    "v_add_f32_dpp v36, v43, v41 row_ror:8 row_mask:0xf bank_mask:0x8\n\t"
    "s_waitcnt vmcnt(9)\n\t"
    "s_add_u32 s57, s32, 0x2000\n\t"
    "s_xor_b32 s57, s57, s80\n\t"
    "buffer_load_dwordx4 a[192:195], v0, s[40:43], s57 offen offset:0 nt\n\t"
    "s_waitcnt vmcnt(9)\n\t"
    "s_add_u32 s57, s32, 0x3000\n\t"
    "s_xor_b32 s57, s57, s80\n\t"
    "buffer_load_dwordx4 a[196:199], v0, s[40:43], s57 offen offset:0 nt\n\t"
    "s_waitcnt vmcnt(9)\n\t"
    "s_add_u32 s57, s32, 0x4000\n\t"
    "s_xor_b32 s57, s57, s80\n\t"
    "buffer_load_dwordx4 a[200:203], v0, s[40:43], s57 offen offset:0 nt\n\t"
    "s_waitcnt vmcnt(9)\n\t"
    "s_add_u32 s57, s32, 0x5000\n\t"
    "s_xor_b32 s57, s57, s80\n\t"
    "buffer_load_dwordx4 a[204:207], v0, s[40:43], s57 offen offset:0 nt\n\t"
    "s_waitcnt vmcnt(9)\n\t"
    "s_add_u32 s57, s32, 0x6000\n\t"
    "s_xor_b32 s57, s57, s80\n\t"
    "buffer_load_dwordx4 a[208:211], v0, s[40:43], s57 offen offset:0 nt\n\t"
    "s_waitcnt vmcnt(9)\n\t"
    "s_add_u32 s57, s32, 0x7000\n\t"
    "s_xor_b32 s57, s57, s80\n\t"
    "buffer_load_dwordx4 a[212:215], v0, s[40:43], s57 offen offset:0 nt\n\t"
    "s_waitcnt vmcnt(9)\n\t"
    "s_add_u32 s33, s34, 0x68000\n\t"
    "s_and_b32 s33, s33, 0x7ffff\n\t"
    "s_add_u32 s33, s33, s56\n\t"
    "s_add_u32 s57, s33, 0\n\t"
    "s_xor_b32 s57, s57, s80\n\t"
    "buffer_load_dwordx4 v[252:255], v1, s[44:47], s57 offen offset:0\n\t"
    "s_waitcnt vmcnt(9)\n\t"
    "s_add_u32 s57, s33, 0x1000\n\t"
    "s_xor_b32 s57, s57, s80\n\t"
    "buffer_load_dwordx4 v[248:251], v1, s[44:47], s57 offen offset:0\n\t"
    "v_perm_b32 v44, v220, v160, s66\n\t"
    "v_perm_b32 v45, v216, v164, s66\n\t"
    "v_perm_b32 v46, v212, v168, s66\n\t"
    "v_perm_b32 v47, v208, v172, s66\n\t"
    "v_perm_b32 v48, v220, v160, s67\n\t"
    "v_perm_b32 v49, v216, v164, s67\n\t"
    "v_perm_b32 v50, v212, v168, s67\n\t"
    "v_perm_b32 v51, v208, v172, s67\n\t"
    "v_max_f32 v37, v35, v36\n\t"
    "v_mfma_f32_16x16x32_bf16 v[8:11], v[16:19], v[44:47], v[8:11]\n\t"
    "v_perm_b32 v52, v221, v161, s66\n\t"
    "v_perm_b32 v53, v217, v165, s66\n\t"
    "v_perm_b32 v54, v213, v169, s66\n\t"
    "v_perm_b32 v55, v209, v173, s66\n\t"
    "v_max_f32_dpp v37, v37, v37 row_ror:4 row_mask:0xf bank_mask:0xf\n\t"
    "v_mfma_f32_16x16x32_bf16 v[8:11], v[20:23], v[48:51], v[8:11]\n\t"
    "v_perm_b32 v44, v221, v161, s67\n\t"
    "v_perm_b32 v45, v217, v165, s67\n\t"
    "v_perm_b32 v46, v213, v169, s67\n\t"
    "v_perm_b32 v47, v209, v173, s67\n\t"
    "v_max_f32_dpp v37, v37, v37 row_ror:8 row_mask:0xf bank_mask:0xf\n\t"
    "v_mfma_f32_16x16x32_bf16 v[8:11], v[24:27], v[52:55], v[8:11]\n\t"
    "v_perm_b32 v48, v222, v162, s66\n\t"
    "v_perm_b32 v49, v218, v166, s66\n\t"
    "v_perm_b32 v50, v214, v170, s66\n\t"
    "v_perm_b32 v51, v210, v174, s66\n\t"
    "v_mov_b32 v38, v37\n\t"
    "v_mfma_f32_16x16x32_bf16 v[8:11], v[28:31], v[44:47], v[8:11]\n\t"
    "v_perm_b32 v52, v222, v162, s67\n\t"
    "v_perm_b32 v53, v218, v166, s67\n\t"
    "v_perm_b32 v54, v214, v170, s67\n\t"
    "v_perm_b32 v55, v210, v174, s67\n\t"
    "v_permlane16_swap_b32 v37, v38\n\t"
    "v_mfma_f32_16x16x32_bf16 v[12:15], v[16:19], v[48:51], v[12:15]\n\t"
    "v_perm_b32 v44, v223, v163, s66\n\t"
    "v_perm_b32 v45, v219, v167, s66\n\t"
    "v_perm_b32 v46, v215, v171, s66\n\t"
    "v_perm_b32 v47, v211, v175, s66\n\t"
    "v_max_f32 v37, v37, v38\n\t"
    "v_mfma_f32_16x16x32_bf16 v[12:15], v[20:23], v[52:55], v[12:15]\n\t"
    "v_perm_b32 v48, v223, v163, s67\n\t"
    "v_perm_b32 v49, v219, v167, s67\n\t"
    "v_perm_b32 v50, v215, v171, s67\n\t"
    "v_perm_b32 v51, v211, v175, s67\n\t"
    "v_mov_b32 v38, v37\n\t"
    "v_mfma_f32_16x16x32_bf16 v[12:15], v[24:27], v[44:47], v[12:15]\n\t"
    "s_nop 0\n\t"
    "v_permlane32_swap_b32 v37, v38\n\t"
    "v_mfma_f32_16x16x32_bf16 v[12:15], v[28:31], v[48:51], v[12:15]\n\t"
    "v_max_f32 v37, v37, v38\n\t"
    "v_cmp_gt_f32 vcc, v37, v32\n\t"
    "s_cbranch_vccz .Lr17_%=\n\t"
    "v_max_f32 v37, v37, v32\n\t"
    "v_sub_f32 v38, v32, v37\n\t"
    "v_mul_f32 v38, s64, v38\n\t"
    "v_exp_f32 v38, v38\n\t"
    "v_mov_b32 v32, v37\n\t"
    "v_mul_f32 v34, s65, v37\n\t"
    "v_mul_f32 v33, v33, v38\n\t"
    "v_mul_f32_dpp v8, v38, v8 quad_perm:[0,0,0,0] row_mask:0xf bank_mask:0xf\n\t"
    "v_mul_f32_dpp v9, v38, v9 quad_perm:[1,1,1,1] row_mask:0xf bank_mask:0xf\n\t"
    "v_mul_f32_dpp v10, v38, v10 quad_perm:[2,2,2,2] row_mask:0xf bank_mask:0xf\n\t"
    "v_mul_f32_dpp v11, v38, v11 quad_perm:[3,3,3,3] row_mask:0xf bank_mask:0xf\n\t"
    "v_mul_f32_dpp v12, v38, v12 quad_perm:[0,0,0,0] row_mask:0xf bank_mask:0xf\n\t"
    "v_mul_f32_dpp v13, v38, v13 quad_perm:[1,1,1,1] row_mask:0xf bank_mask:0xf\n\t"
    "v_mul_f32_dpp v14, v38, v14 quad_perm:[2,2,2,2] row_mask:0xf bank_mask:0xf\n\t"
    "v_mul_f32_dpp v15, v38, v15 quad_perm:[3,3,3,3] row_mask:0xf bank_mask:0xf\n\t"
    ".Lr17_%=:\n\t"
    "v_fma_f32 v35, v35, s64, v34\n\t"
    "v_fma_f32 v36, v36, s64, v34\n\t"
    "v_exp_f32 v35, v35\n\t"
    "v_exp_f32 v36, v36\n\t"
    "s_nop 0\n\t"
    "v_cvt_pk_bf16_f32 v39, v35, v36\n\t"
    "v_dot2_f32_bf16 v33, v39, s68, v33\n\t"
    "s_nop 0\n\t"
    "v_mov_b32_dpp v16, v39 quad_perm:[0,1,2,3] row_mask:0xf bank_mask:0x1\n\t"
    "v_mov_b32_dpp v17, v39 row_ror:12 row_mask:0xf bank_mask:0x1\n\t"
    "v_mov_b32_dpp v18, v39 row_ror:8 row_mask:0xf bank_mask:0x1\n\t"
    "v_mov_b32_dpp v19, v39 row_ror:4 row_mask:0xf bank_mask:0x1\n\t"
    "v_mov_b32_dpp v20, v39 row_ror:4 row_mask:0xf bank_mask:0x2\n\t"
    "v_mov_b32_dpp v21, v39 quad_perm:[0,1,2,3] row_mask:0xf bank_mask:0x2\n\t"
    "v_mov_b32_dpp v22, v39 row_ror:12 row_mask:0xf bank_mask:0x2\n\t"
    "v_mov_b32_dpp v23, v39 row_ror:8 row_mask:0xf bank_mask:0x2\n\t"
    "s_waitcnt vmcnt(9)\n\t"
    "s_add_u32 s57, s33, 0x2000\n\t"
    "s_xor_b32 s57, s57, s80\n\t"
    "buffer_load_dwordx4 v[244:247], v1, s[44:47], s57 offen offset:0\n\t"
    "v_mov_b32_dpp v24, v39 row_ror:8 row_mask:0xf bank_mask:0x4\n\t"
    "v_mov_b32_dpp v25, v39 row_ror:4 row_mask:0xf bank_mask:0x4\n\t"
    "v_mov_b32_dpp v26, v39 quad_perm:[0,1,2,3] row_mask:0xf bank_mask:0x4\n\t"
    "v_mov_b32_dpp v27, v39 row_ror:12 row_mask:0xf bank_mask:0x4\n\t"
    "v_mov_b32_dpp v28, v39 row_ror:12 row_mask:0xf bank_mask:0x8\n\t"
    "v_mov_b32_dpp v29, v39 row_ror:8 row_mask:0xf bank_mask:0x8\n\t"
    "v_mov_b32_dpp v30, v39 row_ror:4 row_mask:0xf bank_mask:0x8\n\t"
    "v_mov_b32_dpp v31, v39 quad_perm:[0,1,2,3] row_mask:0xf bank_mask:0x8\n\t"
    "v_mfma_f32_16x16x32_bf16 v[44:47], a[184:187], v[4:7], 0\n\t"
    "s_waitcnt vmcnt(9)\n\t"
    "s_add_u32 s57, s33, 0x3000\n\t"
    "s_xor_b32 s57, s57, s80\n\t"
    "buffer_load_dwordx4 v[240:243], v1, s[44:47], s57 offen offset:0\n\t"
    "v_mfma_f32_16x16x32_bf16 v[48:51], a[188:191], v[4:7], 0\n\t"
    "s_waitcnt vmcnt(9)\n\t"
    "s_add_u32 s57, s33, 0x4000\n\t"
    "s_xor_b32 s57, s57, s80\n\t"
    "buffer_load_dwordx4 v[236:239], v1, s[44:47], s57 offen offset:0\n\t"
    "v_mfma_f32_16x16x32_bf16 v[52:55], a[192:195], v[4:7], 0\n\t"
    "v_add_f32_dpp v40, v45, v44 row_ror:12 row_mask:0xf bank_mask:0xf\n\t"
    "v_add_f32_dpp v42, v47, v46 row_ror:12 row_mask:0xf bank_mask:0xf\n\t"
    "s_waitcnt vmcnt(9)\n\t"
    "s_add_u32 s57, s33, 0x5000\n\t"
    "s_xor_b32 s57, s57, s80\n\t"
    "buffer_load_dwordx4 v[232:235], v1, s[44:47], s57 offen offset:0\n\t"
    "v_mfma_f32_16x16x32_bf16 v[44:47], a[196:199], v[4:7], 0\n\t"
    "v_add_f32_dpp v41, v50, v49 row_ror:12 row_mask:0xf bank_mask:0xf\n\t"
    "v_add_f32_dpp v43, v48, v51 row_ror:12 row_mask:0xf bank_mask:0xf\n\t"
    "v_add_f32_dpp v35, v42, v40 row_ror:8 row_mask:0xf bank_mask:0x1\n\t"
    "s_waitcnt vmcnt(9)\n\t"
    "s_add_u32 s57, s33, 0x6000\n\t"
    "s_xor_b32 s57, s57, s80\n\t"
    "buffer_load_dwordx4 v[228:231], v1, s[44:47], s57 offen offset:0\n\t"
    "v_mfma_f32_16x16x32_bf16 v[48:51], a[200:203], v[4:7], 0\n\t"
    "v_add_f32_dpp v40, v55, v54 row_ror:12 row_mask:0xf bank_mask:0xf\n\t"
    "v_add_f32_dpp v42, v53, v52 row_ror:12 row_mask:0xf bank_mask:0xf\n\t"
    "v_add_f32_dpp v35, v43, v41 row_ror:8 row_mask:0xf bank_mask:0x2\n\t"
    "s_waitcnt vmcnt(9)\n\t"
    "s_add_u32 s57, s33, 0x7000\n\t"
    "s_xor_b32 s57, s57, s80\n\t"
    "buffer_load_dwordx4 v[224:227], v1, s[44:47], s57 offen offset:0\n\t"
    "v_mfma_f32_16x16x32_bf16 v[52:55], a[204:207], v[4:7], 0\n\t"
    "v_add_f32_dpp v41, v44, v47 row_ror:12 row_mask:0xf bank_mask:0xf\n\t"
    "v_add_f32_dpp v43, v46, v45 row_ror:12 row_mask:0xf bank_mask:0xf\n\t"
    "v_add_f32_dpp v35, v42, v40 row_ror:8 row_mask:0xf bank_mask:0x4\n\t"
    "s_waitcnt vmcnt(9)\n\t"
    "s_add_u32 s32, s34, 0x78000\n\t"
    "s_and_b32 s32, s32, 0x7ffff\n\t"
    "s_add_u32 s32, s32, s56\n\t"
    "s_add_u32 s57, s32, 0\n\t"
    "s_xor_b32 s57, s57, s80\n\t"
    "buffer_load_dwordx4 a[216:219], v0, s[40:43], s57 offen offset:0 nt\n\t"
    "v_mfma_f32_16x16x32_bf16 v[44:47], a[208:211], v[4:7], 0\n\t"
    "v_add_f32_dpp v40, v49, v48 row_ror:12 row_mask:0xf bank_mask:0xf\n\t"
    "v_add_f32_dpp v42, v51, v50 row_ror:12 row_mask:0xf bank_mask:0xf\n\t"
    "v_add_f32_dpp v35, v43, v41 row_ror:8 row_mask:0xf bank_mask:0x8\n\t"
    "s_waitcnt vmcnt(9)\n\t"
    "s_add_u32 s57, s32, 0x1000\n\t"
    "s_xor_b32 s57, s57, s80\n\t"
    "buffer_load_dwordx4 a[220:223], v0, s[40:43], s57 offen offset:0 nt\n\t"
    "v_mfma_f32_16x16x32_bf16 v[48:51], a[212:215], v[4:7], 0\n\t"
    "v_add_f32_dpp v41, v54, v53 row_ror:12 row_mask:0xf bank_mask:0xf\n\t"
    "v_add_f32_dpp v43, v52, v55 row_ror:12 row_mask:0xf bank_mask:0xf\n\t"
    "v_add_f32_dpp v36, v42, v40 row_ror:8 row_mask:0xf bank_mask:0x1\n\t"
    "v_add_f32_dpp v40, v47, v46 row_ror:12 row_mask:0xf bank_mask:0xf\n\t"
    "v_add_f32_dpp v42, v45, v44 row_ror:12 row_mask:0xf bank_mask:0xf\n\t"
    "v_add_f32_dpp v36, v43, v41 row_ror:8 row_mask:0xf bank_mask:0x2\n\t"
    "s_nop 1\n\t"
    "v_add_f32_dpp v41, v48, v51 row_ror:12 row_mask:0xf bank_mask:0xf\n\t"
    "v_add_f32_dpp v43, v50, v49 row_ror:12 row_mask:0xf bank_mask:0xf\n\t"
    "v_add_f32_dpp v36, v42, v40 row_ror:8 row_mask:0xf bank_mask:0x4\n\t"
    "s_nop 0\n\t"
    "v_add_f32_dpp v36, v43, v41 row_ror:8 row_mask:0xf bank_mask:0x8\n\t"
    "s_waitcnt vmcnt(9)\n\t"
    "s_add_u32 s57, s32, 0x2000\n\t"
    "s_xor_b32 s57, s57, s80\n\t"
    "buffer_load_dwordx4 a[224:227], v0, s[40:43], s57 offen offset:0 nt\n\t"
    "s_waitcnt vmcnt(9)\n\t"
    "s_add_u32 s57, s32, 0x3000\n\t"
    "s_xor_b32 s57, s57, s80\n\t"
    "buffer_load_dwordx4 a[228:231], v0, s[40:43], s57 offen offset:0 nt\n\t"
    "s_waitcnt vmcnt(9)\n\t"
    "s_add_u32 s57, s32, 0x4000\n\t"
    "s_xor_b32 s57, s57, s80\n\t"
    "buffer_load_dwordx4 a[232:235], v0, s[40:43], s57 offen offset:0 nt\n\t"
    "s_waitcnt vmcnt(9)\n\t"
    "s_add_u32 s57, s32, 0x5000\n\t"
    "s_xor_b32 s57, s57, s80\n\t"
    "buffer_load_dwordx4 a[236:239], v0, s[40:43], s57 offen offset:0 nt\n\t"
    "s_waitcnt vmcnt(9)\n\t"
    "s_add_u32 s57, s32, 0x6000\n\t"
    "s_xor_b32 s57, s57, s80\n\t"
    "buffer_load_dwordx4 a[240:243], v0, s[40:43], s57 offen offset:0 nt\n\t"
    "s_waitcnt vmcnt(9)\n\t"
    "s_add_u32 s57, s32, 0x7000\n\t"
    "s_xor_b32 s57, s57, s80\n\t"
    "buffer_load_dwordx4 a[244:247], v0, s[40:43], s57 offen offset:0 nt\n\t"
    "s_waitcnt vmcnt(9)\n\t"
    "s_add_u32 s33, s34, 0x70000\n\t"
    "s_and_b32 s33, s33, 0x7ffff\n\t"
    "s_add_u32 s33, s33, s56\n\t"
    "s_add_u32 s57, s33, 0\n\t"
    "s_xor_b32 s57, s57, s80\n\t"
    "buffer_load_dwordx4 v[160:163], v1, s[44:47], s57 offen offset:0 nt\n\t"
    "s_waitcnt vmcnt(9)\n\t"
    "s_add_u32 s57, s33, 0x1000\n\t"
    "s_xor_b32 s57, s57, s80\n\t"
    "buffer_load_dwordx4 v[164:167], v1, s[44:47], s57 offen offset:0 nt\n\t"
    "v_perm_b32 v44, v236, v252, s66\n\t"
    "v_perm_b32 v45, v232, v248, s66\n\t"
    "v_perm_b32 v46, v228, v244, s66\n\t"
    "v_perm_b32 v47, v224, v240, s66\n\t"
    "v_perm_b32 v48, v236, v252, s67\n\t"
    "v_perm_b32 v49, v232, v248, s67\n\t"
    "v_perm_b32 v50, v228, v244, s67\n\t"
    "v_perm_b32 v51, v224, v240, s67\n\t"
    "v_max_f32 v37, v35, v36\n\t"
    "v_mfma_f32_16x16x32_bf16 v[8:11], v[16:19], v[44:47], v[8:11]\n\t"
    "v_perm_b32 v52, v237, v253, s66\n\t"
    "v_perm_b32 v53, v233, v249, s66\n\t"
    "v_perm_b32 v54, v229, v245, s66\n\t"
    "v_perm_b32 v55, v225, v241, s66\n\t"
    "v_max_f32_dpp v37, v37, v37 row_ror:4 row_mask:0xf bank_mask:0xf\n\t"
    "v_mfma_f32_16x16x32_bf16 v[8:11], v[20:23], v[48:51], v[8:11]\n\t"
    "v_perm_b32 v44, v237, v253, s67\n\t"
    "v_perm_b32 v45, v233, v249, s67\n\t"
    "v_perm_b32 v46, v229, v245, s67\n\t"
    "v_perm_b32 v47, v225, v241, s67\n\t"
    "v_max_f32_dpp v37, v37, v37 row_ror:8 row_mask:0xf bank_mask:0xf\n\t"
    "v_mfma_f32_16x16x32_bf16 v[8:11], v[24:27], v[52:55], v[8:11]\n\t"
    "v_perm_b32 v48, v238, v254, s66\n\t"
    "v_perm_b32 v49, v234, v250, s66\n\t"
    "v_perm_b32 v50, v230, v246, s66\n\t"
    "v_perm_b32 v51, v226, v242, s66\n\t"
    "v_mov_b32 v38, v37\n\t"
    "v_mfma_f32_16x16x32_bf16 v[8:11], v[28:31], v[44:47], v[8:11]\n\t"
    "v_perm_b32 v52, v238, v254, s67\n\t"
    "v_perm_b32 v53, v234, v250, s67\n\t"
    "v_perm_b32 v54, v230, v246, s67\n\t"
    "v_perm_b32 v55, v226, v242, s67\n\t"
    "v_permlane16_swap_b32 v37, v38\n\t"
    "v_mfma_f32_16x16x32_bf16 v[12:15], v[16:19], v[48:51], v[12:15]\n\t"
    "v_perm_b32 v44, v239, v255, s66\n\t"
    "v_perm_b32 v45, v235, v251, s66\n\t"
    "v_perm_b32 v46, v231, v247, s66\n\t"
    "v_perm_b32 v47, v227, v243, s66\n\t"
    "v_max_f32 v37, v37, v38\n\t"
    "v_mfma_f32_16x16x32_bf16 v[12:15], v[20:23], v[52:55], v[12:15]\n\t"
    "v_perm_b32 v48, v239, v255, s67\n\t"
    "v_perm_b32 v49, v235, v251, s67\n\t"
    "v_perm_b32 v50, v231, v247, s67\n\t"
    "v_perm_b32 v51, v227, v243, s67\n\t"
    "v_mov_b32 v38, v37\n\t"
    "v_mfma_f32_16x16x32_bf16 v[12:15], v[24:27], v[44:47], v[12:15]\n\t"
    "s_nop 0\n\t"
    "v_permlane32_swap_b32 v37, v38\n\t"
    "v_mfma_f32_16x16x32_bf16 v[12:15], v[28:31], v[48:51], v[12:15]\n\t"
    "v_max_f32 v37, v37, v38\n\t"
    "v_cmp_gt_f32 vcc, v37, v32\n\t"
    "s_cbranch_vccz .Lr18_%=\n\t"
    "v_max_f32 v37, v37, v32\n\t"
    "v_sub_f32 v38, v32, v37\n\t"
    "v_mul_f32 v38, s64, v38\n\t"
    "v_exp_f32 v38, v38\n\t"
    "v_mov_b32 v32, v37\n\t"
    "v_mul_f32 v34, s65, v37\n\t"
    "v_mul_f32 v33, v33, v38\n\t"
    "v_mul_f32_dpp v8, v38, v8 quad_perm:[0,0,0,0] row_mask:0xf bank_mask:0xf\n\t"
    "v_mul_f32_dpp v9, v38, v9 quad_perm:[1,1,1,1] row_mask:0xf bank_mask:0xf\n\t"
    "v_mul_f32_dpp v10, v38, v10 quad_perm:[2,2,2,2] row_mask:0xf bank_mask:0xf\n\t"
    "v_mul_f32_dpp v11, v38, v11 quad_perm:[3,3,3,3] row_mask:0xf bank_mask:0xf\n\t"
    "v_mul_f32_dpp v12, v38, v12 quad_perm:[0,0,0,0] row_mask:0xf bank_mask:0xf\n\t"
    "v_mul_f32_dpp v13, v38, v13 quad_perm:[1,1,1,1] row_mask:0xf bank_mask:0xf\n\t"
    "v_mul_f32_dpp v14, v38, v14 quad_perm:[2,2,2,2] row_mask:0xf bank_mask:0xf\n\t"
    "v_mul_f32_dpp v15, v38, v15 quad_perm:[3,3,3,3] row_mask:0xf bank_mask:0xf\n\t"
    ".Lr18_%=:\n\t"
    "v_fma_f32 v35, v35, s64, v34\n\t"
    "v_fma_f32 v36, v36, s64, v34\n\t"
    "v_exp_f32 v35, v35\n\t"
    "v_exp_f32 v36, v36\n\t"
    "s_nop 0\n\t"
    "v_cvt_pk_bf16_f32 v39, v35, v36\n\t"
    "v_dot2_f32_bf16 v33, v39, s68, v33\n\t"
    "s_nop 0\n\t"
    "v_mov_b32_dpp v16, v39 quad_perm:[0,1,2,3] row_mask:0xf bank_mask:0x1\n\t"
    "v_mov_b32_dpp v17, v39 row_ror:12 row_mask:0xf bank_mask:0x1\n\t"
    "v_mov_b32_dpp v18, v39 row_ror:8 row_mask:0xf bank_mask:0x1\n\t"
    "v_mov_b32_dpp v19, v39 row_ror:4 row_mask:0xf bank_mask:0x1\n\t"
    "v_mov_b32_dpp v20, v39 row_ror:4 row_mask:0xf bank_mask:0x2\n\t"
    "v_mov_b32_dpp v21, v39 quad_perm:[0,1,2,3] row_mask:0xf bank_mask:0x2\n\t"
    "v_mov_b32_dpp v22, v39 row_ror:12 row_mask:0xf bank_mask:0x2\n\t"
    "v_mov_b32_dpp v23, v39 row_ror:8 row_mask:0xf bank_mask:0x2\n\t"
    "s_waitcnt vmcnt(9)\n\t"
    "s_add_u32 s57, s33, 0x2000\n\t"
    "s_xor_b32 s57, s57, s80\n\t"
    "buffer_load_dwordx4 v[168:171], v1, s[44:47], s57 offen offset:0 nt\n\t"
    "v_mov_b32_dpp v24, v39 row_ror:8 row_mask:0xf bank_mask:0x4\n\t"
    "v_mov_b32_dpp v25, v39 row_ror:4 row_mask:0xf bank_mask:0x4\n\t"
    "v_mov_b32_dpp v26, v39 quad_perm:[0,1,2,3] row_mask:0xf bank_mask:0x4\n\t"
    "v_mov_b32_dpp v27, v39 row_ror:12 row_mask:0xf bank_mask:0x4\n\t"
    "v_mov_b32_dpp v28, v39 row_ror:12 row_mask:0xf bank_mask:0x8\n\t"
    "v_mov_b32_dpp v29, v39 row_ror:8 row_mask:0xf bank_mask:0x8\n\t"
    "v_mov_b32_dpp v30, v39 row_ror:4 row_mask:0xf bank_mask:0x8\n\t"
    "v_mov_b32_dpp v31, v39 quad_perm:[0,1,2,3] row_mask:0xf bank_mask:0x8\n\t"
    "v_mfma_f32_16x16x32_bf16 v[44:47], a[216:219], v[4:7], 0\n\t"
    "s_waitcnt vmcnt(9)\n\t"
    "s_add_u32 s57, s33, 0x3000\n\t"
    "s_xor_b32 s57, s57, s80\n\t"
    "buffer_load_dwordx4 v[172:175], v1, s[44:47], s57 offen offset:0 nt\n\t"
    "v_mfma_f32_16x16x32_bf16 v[48:51], a[220:223], v[4:7], 0\n\t"
    "s_waitcnt vmcnt(9)\n\t"
    "s_add_u32 s57, s33, 0x4000\n\t"
    "s_xor_b32 s57, s57, s80\n\t"
    "buffer_load_dwordx4 v[220:223], v1, s[44:47], s57 offen offset:0 nt\n\t"
    "v_mfma_f32_16x16x32_bf16 v[52:55], a[224:227], v[4:7], 0\n\t"
    "v_add_f32_dpp v40, v45, v44 row_ror:12 row_mask:0xf bank_mask:0xf\n\t"
    "v_add_f32_dpp v42, v47, v46 row_ror:12 row_mask:0xf bank_mask:0xf\n\t"
    "s_waitcnt vmcnt(9)\n\t"
    "s_add_u32 s57, s33, 0x5000\n\t"
    "s_xor_b32 s57, s57, s80\n\t"
    "buffer_load_dwordx4 v[216:219], v1, s[44:47], s57 offen offset:0 nt\n\t"
    "v_mfma_f32_16x16x32_bf16 v[44:47], a[228:231], v[4:7], 0\n\t"
    "v_add_f32_dpp v41, v50, v49 row_ror:12 row_mask:0xf bank_mask:0xf\n\t"
    "v_add_f32_dpp v43, v48, v51 row_ror:12 row_mask:0xf bank_mask:0xf\n\t"
    "v_add_f32_dpp v35, v42, v40 row_ror:8 row_mask:0xf bank_mask:0x1\n\t"
    "s_waitcnt vmcnt(9)\n\t"
    "s_add_u32 s57, s33, 0x6000\n\t"
    "s_xor_b32 s57, s57, s80\n\t"
    "buffer_load_dwordx4 v[212:215], v1, s[44:47], s57 offen offset:0 nt\n\t"
    "v_mfma_f32_16x16x32_bf16 v[48:51], a[232:235], v[4:7], 0\n\t"
    "v_add_f32_dpp v40, v55, v54 row_ror:12 row_mask:0xf bank_mask:0xf\n\t"
    "v_add_f32_dpp v42, v53, v52 row_ror:12 row_mask:0xf bank_mask:0xf\n\t"
    "v_add_f32_dpp v35, v43, v41 row_ror:8 row_mask:0xf bank_mask:0x2\n\t"
    "s_waitcnt vmcnt(9)\n\t"
    "s_add_u32 s57, s33, 0x7000\n\t"
    "s_xor_b32 s57, s57, s80\n\t"
    "buffer_load_dwordx4 v[208:211], v1, s[44:47], s57 offen offset:0 nt\n\t"
    "v_mfma_f32_16x16x32_bf16 v[52:55], a[236:239], v[4:7], 0\n\t"
    "v_add_f32_dpp v41, v44, v47 row_ror:12 row_mask:0xf bank_mask:0xf\n\t"
    "v_add_f32_dpp v43, v46, v45 row_ror:12 row_mask:0xf bank_mask:0xf\n\t"
    "v_add_f32_dpp v35, v42, v40 row_ror:8 row_mask:0xf bank_mask:0x4\n\t"
    "s_waitcnt vmcnt(9)\n\t"
    "s_add_u32 s33, s34, 0x78000\n\t"
    "s_and_b32 s33, s33, 0x7ffff\n\t"
    "s_add_u32 s33, s33, s56\n\t"
    "s_add_u32 s57, s33, 0\n\t"
    "s_xor_b32 s57, s57, s80\n\t"
    "buffer_load_dwordx4 v[252:255], v1, s[44:47], s57 offen offset:0 nt\n\t"
    "v_mfma_f32_16x16x32_bf16 v[44:47], a[240:243], v[4:7], 0\n\t"
    "v_add_f32_dpp v40, v49, v48 row_ror:12 row_mask:0xf bank_mask:0xf\n\t"
    "v_add_f32_dpp v42, v51, v50 row_ror:12 row_mask:0xf bank_mask:0xf\n\t"
    "v_add_f32_dpp v35, v43, v41 row_ror:8 row_mask:0xf bank_mask:0x8\n\t"
    "s_waitcnt vmcnt(9)\n\t"
    "s_add_u32 s57, s33, 0x1000\n\t"
    "s_xor_b32 s57, s57, s80\n\t"
    "buffer_load_dwordx4 v[248:251], v1, s[44:47], s57 offen offset:0 nt\n\t"
    "v_mfma_f32_16x16x32_bf16 v[48:51], a[244:247], v[4:7], 0\n\t"
    "v_add_f32_dpp v41, v54, v53 row_ror:12 row_mask:0xf bank_mask:0xf\n\t"
    "v_add_f32_dpp v43, v52, v55 row_ror:12 row_mask:0xf bank_mask:0xf\n\t"
    "v_add_f32_dpp v36, v42, v40 row_ror:8 row_mask:0xf bank_mask:0x1\n\t"
    "v_add_f32_dpp v40, v47, v46 row_ror:12 row_mask:0xf bank_mask:0xf\n\t"
    "v_add_f32_dpp v42, v45, v44 row_ror:12 row_mask:0xf bank_mask:0xf\n\t"
    "v_add_f32_dpp v36, v43, v41 row_ror:8 row_mask:0xf bank_mask:0x2\n\t"
    "s_nop 1\n\t"
    "v_add_f32_dpp v41, v48, v51 row_ror:12 row_mask:0xf bank_mask:0xf\n\t"
    "v_add_f32_dpp v43, v50, v49 row_ror:12 row_mask:0xf bank_mask:0xf\n\t"
    "v_add_f32_dpp v36, v42, v40 row_ror:8 row_mask:0xf bank_mask:0x4\n\t"
    "s_nop 0\n\t"
    "v_add_f32_dpp v36, v43, v41 row_ror:8 row_mask:0xf bank_mask:0x8\n\t"
    "s_waitcnt vmcnt(9)\n\t"
    "s_add_u32 s57, s33, 0x2000\n\t"
    "s_xor_b32 s57, s57, s80\n\t"
    "buffer_load_dwordx4 v[244:247], v1, s[44:47], s57 offen offset:0 nt\n\t"
    "s_waitcnt vmcnt(9)\n\t"
    "s_add_u32 s57, s33, 0x3000\n\t"
    "s_xor_b32 s57, s57, s80\n\t"
    "buffer_load_dwordx4 v[240:243], v1, s[44:47], s57 offen offset:0 nt\n\t"
    "s_waitcnt vmcnt(9)\n\t"
    "s_add_u32 s57, s33, 0x4000\n\t"
    "s_xor_b32 s57, s57, s80\n\t"
    "buffer_load_dwordx4 v[236:239], v1, s[44:47], s57 offen offset:0 nt\n\t"
    "s_waitcnt vmcnt(9)\n\t"
    "s_add_u32 s57, s33, 0x5000\n\t"
    "s_xor_b32 s57, s57, s80\n\t"
    "buffer_load_dwordx4 v[232:235], v1, s[44:47], s57 offen offset:0 nt\n\t"
    "s_waitcnt vmcnt(9)\n\t"
    "s_add_u32 s57, s33, 0x6000\n\t"
    "s_xor_b32 s57, s57, s80\n\t"
    "buffer_load_dwordx4 v[228:231], v1, s[44:47], s57 offen offset:0 nt\n\t"
    "s_waitcnt vmcnt(9)\n\t"
    "s_add_u32 s57, s33, 0x7000\n\t"
    "s_xor_b32 s57, s57, s80\n\t"
    "buffer_load_dwordx4 v[224:227], v1, s[44:47], s57 offen offset:0 nt\n\t"
    "s_waitcnt vmcnt(9)\n\t"
    "s_waitcnt vmcnt(8)\n\t"
    "v_perm_b32 v44, v220, v160, s66\n\t"
    "v_perm_b32 v45, v216, v164, s66\n\t"
    "v_perm_b32 v46, v212, v168, s66\n\t"
    "v_perm_b32 v47, v208, v172, s66\n\t"
    "v_perm_b32 v48, v220, v160, s67\n\t"
    "v_perm_b32 v49, v216, v164, s67\n\t"
    "v_perm_b32 v50, v212, v168, s67\n\t"
    "v_perm_b32 v51, v208, v172, s67\n\t"
    "v_max_f32 v37, v35, v36\n\t"
    "v_mfma_f32_16x16x32_bf16 v[8:11], v[16:19], v[44:47], v[8:11]\n\t"
    "v_perm_b32 v52, v221, v161, s66\n\t"
    "v_perm_b32 v53, v217, v165, s66\n\t"
    "v_perm_b32 v54, v213, v169, s66\n\t"
    "v_perm_b32 v55, v209, v173, s66\n\t"
    "v_max_f32_dpp v37, v37, v37 row_ror:4 row_mask:0xf bank_mask:0xf\n\t"
    "v_mfma_f32_16x16x32_bf16 v[8:11], v[20:23], v[48:51], v[8:11]\n\t"
    "v_perm_b32 v44, v221, v161, s67\n\t"
    "v_perm_b32 v45, v217, v165, s67\n\t"
    "v_perm_b32 v46, v213, v169, s67\n\t"
    "v_perm_b32 v47, v209, v173, s67\n\t"
    "v_max_f32_dpp v37, v37, v37 row_ror:8 row_mask:0xf bank_mask:0xf\n\t"
    "v_mfma_f32_16x16x32_bf16 v[8:11], v[24:27], v[52:55], v[8:11]\n\t"
    "v_perm_b32 v48, v222, v162, s66\n\t"
    "v_perm_b32 v49, v218, v166, s66\n\t"
    "v_perm_b32 v50, v214, v170, s66\n\t"
    "v_perm_b32 v51, v210, v174, s66\n\t"
    "v_mov_b32 v38, v37\n\t"
    "v_mfma_f32_16x16x32_bf16 v[8:11], v[28:31], v[44:47], v[8:11]\n\t"
    "v_perm_b32 v52, v222, v162, s67\n\t"
    "v_perm_b32 v53, v218, v166, s67\n\t"
    "v_perm_b32 v54, v214, v170, s67\n\t"
    "v_perm_b32 v55, v210, v174, s67\n\t"
    "v_permlane16_swap_b32 v37, v38\n\t"
    "v_mfma_f32_16x16x32_bf16 v[12:15], v[16:19], v[48:51], v[12:15]\n\t"
    "v_perm_b32 v44, v223, v163, s66\n\t"
    "v_perm_b32 v45, v219, v167, s66\n\t"
    "v_perm_b32 v46, v215, v171, s66\n\t"
    "v_perm_b32 v47, v211, v175, s66\n\t"
    "v_max_f32 v37, v37, v38\n\t"
    "v_mfma_f32_16x16x32_bf16 v[12:15], v[20:23], v[52:55], v[12:15]\n\t"
    "v_perm_b32 v48, v223, v163, s67\n\t"
    "v_perm_b32 v49, v219, v167, s67\n\t"
    "v_perm_b32 v50, v215, v171, s67\n\t"
    "v_perm_b32 v51, v211, v175, s67\n\t"
    "v_mov_b32 v38, v37\n\t"
    "v_mfma_f32_16x16x32_bf16 v[12:15], v[24:27], v[44:47], v[12:15]\n\t"
    "s_nop 0\n\t"
    "v_permlane32_swap_b32 v37, v38\n\t"
    "v_mfma_f32_16x16x32_bf16 v[12:15], v[28:31], v[48:51], v[12:15]\n\t"
    "v_max_f32 v37, v37, v38\n\t"
    "v_cmp_gt_f32 vcc, v37, v32\n\t"
    "s_cbranch_vccz .Lr19_%=\n\t"
    "v_max_f32 v37, v37, v32\n\t"
    "v_sub_f32 v38, v32, v37\n\t"
    "v_mul_f32 v38, s64, v38\n\t"
    "v_exp_f32 v38, v38\n\t"
    "v_mov_b32 v32, v37\n\t"
    "v_mul_f32 v34, s65, v37\n\t"
    "v_mul_f32 v33, v33, v38\n\t"
    "v_mul_f32_dpp v8, v38, v8 quad_perm:[0,0,0,0] row_mask:0xf bank_mask:0xf\n\t"
    "v_mul_f32_dpp v9, v38, v9 quad_perm:[1,1,1,1] row_mask:0xf bank_mask:0xf\n\t"
    "v_mul_f32_dpp v10, v38, v10 quad_perm:[2,2,2,2] row_mask:0xf bank_mask:0xf\n\t"
    "v_mul_f32_dpp v11, v38, v11 quad_perm:[3,3,3,3] row_mask:0xf bank_mask:0xf\n\t"
    "v_mul_f32_dpp v12, v38, v12 quad_perm:[0,0,0,0] row_mask:0xf bank_mask:0xf\n\t"
    "v_mul_f32_dpp v13, v38, v13 quad_perm:[1,1,1,1] row_mask:0xf bank_mask:0xf\n\t"
    "v_mul_f32_dpp v14, v38, v14 quad_perm:[2,2,2,2] row_mask:0xf bank_mask:0xf\n\t"
    "v_mul_f32_dpp v15, v38, v15 quad_perm:[3,3,3,3] row_mask:0xf bank_mask:0xf\n\t"
    ".Lr19_%=:\n\t"
    "v_fma_f32 v35, v35, s64, v34\n\t"
    "v_fma_f32 v36, v36, s64, v34\n\t"
    "v_exp_f32 v35, v35\n\t"
    "v_exp_f32 v36, v36\n\t"
    "s_nop 0\n\t"
    "v_cvt_pk_bf16_f32 v39, v35, v36\n\t"
    "v_dot2_f32_bf16 v33, v39, s68, v33\n\t"
    "s_nop 0\n\t"
    "v_mov_b32_dpp v16, v39 quad_perm:[0,1,2,3] row_mask:0xf bank_mask:0x1\n\t"
    "v_mov_b32_dpp v17, v39 row_ror:12 row_mask:0xf bank_mask:0x1\n\t"
    "v_mov_b32_dpp v18, v39 row_ror:8 row_mask:0xf bank_mask:0x1\n\t"
    "v_mov_b32_dpp v19, v39 row_ror:4 row_mask:0xf bank_mask:0x1\n\t"
    "v_mov_b32_dpp v20, v39 row_ror:4 row_mask:0xf bank_mask:0x2\n\t"
    "v_mov_b32_dpp v21, v39 quad_perm:[0,1,2,3] row_mask:0xf bank_mask:0x2\n\t"
    "v_mov_b32_dpp v22, v39 row_ror:12 row_mask:0xf bank_mask:0x2\n\t"
    "v_mov_b32_dpp v23, v39 row_ror:8 row_mask:0xf bank_mask:0x2\n\t"
    "v_mov_b32_dpp v24, v39 row_ror:8 row_mask:0xf bank_mask:0x4\n\t"
    "v_mov_b32_dpp v25, v39 row_ror:4 row_mask:0xf bank_mask:0x4\n\t"
    "v_mov_b32_dpp v26, v39 quad_perm:[0,1,2,3] row_mask:0xf bank_mask:0x4\n\t"
    "v_mov_b32_dpp v27, v39 row_ror:12 row_mask:0xf bank_mask:0x4\n\t"
    "v_mov_b32_dpp v28, v39 row_ror:12 row_mask:0xf bank_mask:0x8\n\t"
    "v_mov_b32_dpp v29, v39 row_ror:8 row_mask:0xf bank_mask:0x8\n\t"
    "v_mov_b32_dpp v30, v39 row_ror:4 row_mask:0xf bank_mask:0x8\n\t"
    "v_mov_b32_dpp v31, v39 quad_perm:[0,1,2,3] row_mask:0xf bank_mask:0x8\n\t"
    "v_mbcnt_lo_u32_b32 v3, -1, 0\n\t"
    "v_mbcnt_hi_u32_b32 v3, -1, v3\n\t"
    "v_add_f32_dpp v33, v33, v33 row_ror:4 row_mask:0xf bank_mask:0xf\n\t"
    "v_add_u32 v37, s60, v3\n\t"
    "v_and_b32 v37, 3, v37\n\t"
    "v_add_f32_dpp v33, v33, v33 row_ror:8 row_mask:0xf bank_mask:0xf\n\t"
    "v_and_b32 v39, 3, v3\n\t"
    "v_lshlrev_b32 v39, 3, v39\n\t"
    "v_lshl_add_u32 v37, v37, 5, v39\n\t"
    "v_mov_b32 v38, v33\n\t"
    "s_nop 1\n\t"
    "v_permlane16_swap_b32 v33, v38\n\t"
    "v_add_f32 v33, v33, v38\n\t"
    "s_add_u32 s72, s60, 1\n\t"
    "s_and_b32 s72, s72, 3\n\t"
    "s_mul_i32 s72, s72, 0x600\n\t"
    "s_add_u32 s72, s72, 0xc000\n\t"
    "s_add_u32 s73, s60, 2\n\t"
    "s_and_b32 s73, s73, 3\n\t"
    "s_mul_i32 s73, s73, 0x600\n\t"
    "s_add_u32 s73, s73, 0xc200\n\t"
    "s_add_u32 s74, s60, 3\n\t"
    "s_and_b32 s74, s74, 3\n\t"
    "s_mul_i32 s74, s74, 0x600\n\t"
    "s_add_u32 s74, s74, 0xc400\n\t"
    "v_mov_b32 v38, v33\n\t"
    "s_nop 1\n\t"
    "v_permlane32_swap_b32 v33, v38\n\t"
    "v_add_f32 v33, v33, v38\n\t"
    "ds_write_b64 v37, v[32:33] offset:55296\n\t"
    "s_mul_i32 s75, s60, 0x600\n\t"
    "s_add_u32 s75, s75, 0xc000\n\t"
    "s_lshl_b32 s76, s60, 5\n\t"
    "s_add_u32 s76, s76, 0xd800\n\t"
    "v_mov_b32 v37, s76\n\t"
    "s_lshr_b32 s77, s59, 5\n\t"
    "s_lshl_b32 s77, s77, 2\n\t"
    "s_add_u32 s77, s77, s60\n\t"
    "s_lshl_b32 s77, s77, 13\n\t"
    "s_bfe_u32 s78, s59, 0x10004\n\t"
    "s_lshl_b32 s78, s78, 12\n\t"
    "s_add_u32 s77, s77, s78\n\t"
    "s_and_b32 s78, s59, 15\n\t"
    "s_lshl_b32 s78, s78, 6\n\t"
    "s_add_u32 s77, s77, s78\n\t"
    "s_add_u32 s77, s77, s62\n\t"
    "s_add_u32 s77, s77, 0x10000\n\t"
    "v_lshrrev_b32 v40, 4, v3\n\t"
    "v_lshlrev_b32 v40, 10, v40\n\t"
    "v_and_b32 v39, 15, v3\n\t"
    "v_lshl_add_u32 v40, v39, 2, v40\n\t"
    "s_lshl_b32 s78, s59, 7\n\t"
    "s_lshl_b32 s79, s60, 3\n\t"
    "s_add_u32 s78, s78, s79\n\t"
    "s_add_u32 s78, s78, s62\n\t"
    "s_add_u32 s78, s78, 0x2000\n\t"
    "v_cmp_eq_u32 vcc, 0, v3\n\t"
    "v_mov_b32 v41, 0x7fffff00\n\t"
    "s_nop 0\n\t"
    "v_cndmask_b32 v41, v41, v3, vcc\n\t"
    "s_waitcnt vmcnt(7)\n\t"
    "s_waitcnt vmcnt(6)\n\t"
    "s_waitcnt vmcnt(5)\n\t"
    "s_waitcnt vmcnt(4)\n\t"
    "s_waitcnt vmcnt(3)\n\t"
    "s_waitcnt vmcnt(2)\n\t"
    "s_waitcnt vmcnt(1)\n\t"
    "s_waitcnt vmcnt(0)\n\t"
    "v_perm_b32 v44, v236, v252, s66\n\t"
    "v_perm_b32 v45, v232, v248, s66\n\t"
    "v_perm_b32 v46, v228, v244, s66\n\t"
    "v_perm_b32 v47, v224, v240, s66\n\t"
    "v_perm_b32 v48, v236, v252, s67\n\t"
    "v_perm_b32 v49, v232, v248, s67\n\t"
    "v_perm_b32 v50, v228, v244, s67\n\t"
    "v_perm_b32 v51, v224, v240, s67\n\t"
    "v_mfma_f32_16x16x32_bf16 v[8:11], v[16:19], v[44:47], v[8:11]\n\t"
    "v_perm_b32 v52, v237, v253, s66\n\t"
    "v_perm_b32 v53, v233, v249, s66\n\t"
    "v_perm_b32 v54, v229, v245, s66\n\t"
    "v_perm_b32 v55, v225, v241, s66\n\t"
    "v_mfma_f32_16x16x32_bf16 v[8:11], v[20:23], v[48:51], v[8:11]\n\t"
    "v_perm_b32 v44, v237, v253, s67\n\t"
    "v_perm_b32 v45, v233, v249, s67\n\t"
    "v_perm_b32 v46, v229, v245, s67\n\t"
    "v_perm_b32 v47, v225, v241, s67\n\t"
    "v_mfma_f32_16x16x32_bf16 v[8:11], v[24:27], v[52:55], v[8:11]\n\t"
    "v_perm_b32 v48, v238, v254, s66\n\t"
    "v_perm_b32 v49, v234, v250, s66\n\t"
    "v_perm_b32 v50, v230, v246, s66\n\t"
    "v_perm_b32 v51, v226, v242, s66\n\t"
    "v_mfma_f32_16x16x32_bf16 v[8:11], v[28:31], v[44:47], v[8:11]\n\t"
    "v_perm_b32 v52, v238, v254, s67\n\t"
    "v_perm_b32 v53, v234, v250, s67\n\t"
    "v_perm_b32 v54, v230, v246, s67\n\t"
    "v_perm_b32 v55, v226, v242, s67\n\t"
    "v_mfma_f32_16x16x32_bf16 v[12:15], v[16:19], v[48:51], v[12:15]\n\t"
    "v_perm_b32 v44, v239, v255, s66\n\t"
    "v_perm_b32 v45, v235, v251, s66\n\t"
    "v_perm_b32 v46, v231, v247, s66\n\t"
    "v_perm_b32 v47, v227, v243, s66\n\t"
    "v_mfma_f32_16x16x32_bf16 v[12:15], v[20:23], v[52:55], v[12:15]\n\t"
    "v_perm_b32 v48, v239, v255, s67\n\t"
    "v_perm_b32 v49, v235, v251, s67\n\t"
    "v_perm_b32 v50, v231, v247, s67\n\t"
    "v_perm_b32 v51, v227, v243, s67\n\t"
    "v_mfma_f32_16x16x32_bf16 v[12:15], v[24:27], v[44:47], v[12:15]\n\t"
    "s_nop 0\n\t"
    "v_mfma_f32_16x16x32_bf16 v[12:15], v[28:31], v[48:51], v[12:15]\n\t"
    "s_nop 7\n\t"
    "s_branch .Lchain_%=\n\t"
    ".Lpass2_%=:\n\t"
    "v_mbcnt_lo_u32_b32 v37, -1, 0\n\t"
    "v_mbcnt_hi_u32_b32 v37, -1, v37\n\t"
    "v_add_u32 v39, s60, v37\n\t"
    "v_and_b32 v39, 3, v39\n\t"
    "v_lshlrev_b32 v38, 4, v39\n\t"
    "v_lshrrev_b32 v39, 2, v37\n\t"
    "v_lshl_add_u32 v38, v39, 6, v38\n\t"
    "s_lshr_b32 s76, s59, 5\n\t"
    "s_lshl_b32 s76, s76, 10\n\t"
    "s_add_u32 s76, s76, s62\n\t"
    "v_lshlrev_b32 v1, 4, v37\n\t"
    "v_bfe_u32 v39, v37, 2, 2\n\t"
    "v_lshlrev_b32 v0, 8, v39\n\t"
    "v_lshrrev_b32 v39, 4, v37\n\t"
    "v_lshl_add_u32 v0, v39, 6, v0\n\t"
    "v_and_b32 v39, 3, v37\n\t"
    "v_lshl_add_u32 v0, v39, 4, v0\n\t"
    "s_mul_i32 s72, s60, 0x6800\n\t"
    "s_add_u32 s72, s72, 0xdf00\n\t"
    "v_add_u32 v2, s72, v1\n\t"
    "v_mov_b32 v32, 0xff800000\n\t"
    "v_mov_b32 v33, 0\n\t"
    "v_mov_b32 v34, 0\n\t"
    "v_mov_b32 v8, 0\n\t"
    "v_mov_b32 v9, 0\n\t"
    "v_mov_b32 v10, 0\n\t"
    "v_mov_b32 v11, 0\n\t"
    "v_mov_b32 v12, 0\n\t"
    "v_mov_b32 v13, 0\n\t"
    "v_mov_b32 v14, 0\n\t"
    "v_mov_b32 v15, 0\n\t"
    "v_mov_b32 v16, 0\n\t"
    "v_mov_b32 v17, 0\n\t"
    "v_mov_b32 v18, 0\n\t"
    "v_mov_b32 v19, 0\n\t"
    "v_mov_b32 v20, 0\n\t"
    "v_mov_b32 v21, 0\n\t"
    "v_mov_b32 v22, 0\n\t"
    "v_mov_b32 v23, 0\n\t"
    "v_mov_b32 v24, 0\n\t"
    "v_mov_b32 v25, 0\n\t"
    "v_mov_b32 v26, 0\n\t"
    "v_mov_b32 v27, 0\n\t"
    "v_mov_b32 v28, 0\n\t"
    "v_mov_b32 v29, 0\n\t"
    "v_mov_b32 v30, 0\n\t"
    "v_mov_b32 v31, 0\n\t"
    "s_sleep 15\n\t"
    "s_mov_b32 s58, 0x100000\n\t"
    ".Lp20_%=:\n\t"
    "buffer_load_dwordx4 v[4:7], v38, s[48:51], s76 offen sc1\n\t"
    "s_waitcnt vmcnt(0)\n\t"
    ".Lp21_%=:\n\t"
    "v_max3_u32 v37, v4, v5, v6\n\t"
    "v_max_u32 v37, v37, v7\n\t"
    "v_cmp_eq_u32 vcc, -1, v37\n\t"
    "s_cbranch_vccz .Lp22_%=\n\t"
    "s_mov_b64 exec, vcc\n\t"
    "s_sub_u32 s58, s58, 1\n\t"
    "s_cbranch_scc1 .Lp22_%=\n\t"
    "s_sleep 3\n\t"
    "s_branch .Lp20_%=\n\t"
    ".Lp22_%=:\n\t"
    "s_mov_b64 exec, -1\n\t"
    "v_mfma_f32_16x16x32_bf16 v[44:47], a[248:251], v[4:7], 0\n\t"
    "s_add_u32 s32, s34, 0x20000\n\t"
    "s_and_b32 s32, s32, 0x7ffff\n\t"
    "s_add_u32 s32, s32, s56\n\t"
    "s_add_u32 s57, s32, 0\n\t"
    "s_xor_b32 s57, s57, s80\n\t"
    "buffer_load_dwordx4 a[248:251], v0, s[40:43], s57 offen offset:0 nt\n\t"
    "v_mfma_f32_16x16x32_bf16 v[48:51], a[252:255], v[4:7], 0\n\t"
    "s_add_u32 s57, s32, 0x1000\n\t"
    "s_xor_b32 s57, s57, s80\n\t"
    "buffer_load_dwordx4 a[252:255], v0, s[40:43], s57 offen offset:0 nt\n\t"
    "v_mfma_f32_16x16x32_bf16 v[52:55], a[64:67], v[4:7], 0\n\t"
    "s_add_u32 s57, s32, 0x2000\n\t"
    "s_xor_b32 s57, s57, s80\n\t"
    "buffer_load_dwordx4 a[64:67], v0, s[40:43], s57 offen offset:0 nt\n\t"
    "v_add_f32_dpp v40, v45, v44 row_ror:12 row_mask:0xf bank_mask:0xf\n\t"
    "v_add_f32_dpp v42, v47, v46 row_ror:12 row_mask:0xf bank_mask:0xf\n\t"
    "v_mfma_f32_16x16x32_bf16 v[44:47], a[68:71], v[4:7], 0\n\t"
    "s_add_u32 s57, s32, 0x3000\n\t"
    "s_xor_b32 s57, s57, s80\n\t"
    "buffer_load_dwordx4 a[68:71], v0, s[40:43], s57 offen offset:0 nt\n\t"
    "v_add_f32_dpp v41, v50, v49 row_ror:12 row_mask:0xf bank_mask:0xf\n\t"
    "v_add_f32_dpp v43, v48, v51 row_ror:12 row_mask:0xf bank_mask:0xf\n\t"
    "v_add_f32_dpp v35, v42, v40 row_ror:8 row_mask:0xf bank_mask:0x1\n\t"
    "v_mfma_f32_16x16x32_bf16 v[48:51], a[72:75], v[4:7], 0\n\t"
    "s_add_u32 s57, s32, 0x4000\n\t"
    "s_xor_b32 s57, s57, s80\n\t"
    "buffer_load_dwordx4 a[72:75], v0, s[40:43], s57 offen offset:0 nt\n\t"
    "v_add_f32_dpp v40, v55, v54 row_ror:12 row_mask:0xf bank_mask:0xf\n\t"
    "v_add_f32_dpp v42, v53, v52 row_ror:12 row_mask:0xf bank_mask:0xf\n\t"
    "v_add_f32_dpp v35, v43, v41 row_ror:8 row_mask:0xf bank_mask:0x2\n\t"
    "v_mfma_f32_16x16x32_bf16 v[52:55], a[76:79], v[4:7], 0\n\t"
    "s_add_u32 s57, s32, 0x5000\n\t"
    "s_xor_b32 s57, s57, s80\n\t"
    "buffer_load_dwordx4 a[76:79], v0, s[40:43], s57 offen offset:0 nt\n\t"
    "v_add_f32_dpp v41, v44, v47 row_ror:12 row_mask:0xf bank_mask:0xf\n\t"
    "v_add_f32_dpp v43, v46, v45 row_ror:12 row_mask:0xf bank_mask:0xf\n\t"
    "v_add_f32_dpp v35, v42, v40 row_ror:8 row_mask:0xf bank_mask:0x4\n\t"
    "v_mfma_f32_16x16x32_bf16 v[44:47], a[80:83], v[4:7], 0\n\t"
    "s_add_u32 s57, s32, 0x6000\n\t"
    "s_xor_b32 s57, s57, s80\n\t"
    "buffer_load_dwordx4 a[80:83], v0, s[40:43], s57 offen offset:0 nt\n\t"
    "v_add_f32_dpp v40, v49, v48 row_ror:12 row_mask:0xf bank_mask:0xf\n\t"
    "v_add_f32_dpp v42, v51, v50 row_ror:12 row_mask:0xf bank_mask:0xf\n\t"
    "v_add_f32_dpp v35, v43, v41 row_ror:8 row_mask:0xf bank_mask:0x8\n\t"
    "v_mfma_f32_16x16x32_bf16 v[48:51], a[84:87], v[4:7], 0\n\t"
    "s_add_u32 s57, s32, 0x7000\n\t"
    "s_xor_b32 s57, s57, s80\n\t"
    "buffer_load_dwordx4 a[84:87], v0, s[40:43], s57 offen offset:0 nt\n\t"
    "v_add_f32_dpp v41, v54, v53 row_ror:12 row_mask:0xf bank_mask:0xf\n\t"
    "v_add_f32_dpp v43, v52, v55 row_ror:12 row_mask:0xf bank_mask:0xf\n\t"
    "v_add_f32_dpp v36, v42, v40 row_ror:8 row_mask:0xf bank_mask:0x1\n\t"
    "v_add_f32_dpp v40, v47, v46 row_ror:12 row_mask:0xf bank_mask:0xf\n\t"
    "v_add_f32_dpp v42, v45, v44 row_ror:12 row_mask:0xf bank_mask:0xf\n\t"
    "v_add_f32_dpp v36, v43, v41 row_ror:8 row_mask:0xf bank_mask:0x2\n\t"
    "v_add_f32_dpp v41, v48, v51 row_ror:12 row_mask:0xf bank_mask:0xf\n\t"
    "v_add_f32_dpp v43, v50, v49 row_ror:12 row_mask:0xf bank_mask:0xf\n\t"
    "v_add_f32_dpp v36, v42, v40 row_ror:8 row_mask:0xf bank_mask:0x4\n\t"
    "s_nop 0\n\t"
    "v_add_f32_dpp v36, v43, v41 row_ror:8 row_mask:0xf bank_mask:0x8\n\t"
    "v_max_f32 v37, v35, v36\n\t"
    "s_nop 1\n\t"
    "v_max_f32_dpp v37, v37, v37 row_ror:4 row_mask:0xf bank_mask:0xf\n\t"
    "s_nop 1\n\t"
    "v_max_f32_dpp v37, v37, v37 row_ror:8 row_mask:0xf bank_mask:0xf\n\t"
    "v_mov_b32 v38, v37\n\t"
    "s_nop 1\n\t"
    "v_permlane16_swap_b32 v37, v38\n\t"
    "v_max_f32 v37, v37, v38\n\t"
    "v_mov_b32 v38, v37\n\t"
    "s_nop 1\n\t"
    "v_permlane32_swap_b32 v37, v38\n\t"
    "v_max_f32 v37, v37, v38\n\t"
    "v_cmp_gt_f32 vcc, v37, v32\n\t"
    "s_cbranch_vccz .Lr23_%=\n\t"
    "v_max_f32 v37, v37, v32\n\t"
    "v_sub_f32 v38, v32, v37\n\t"
    "v_mul_f32 v38, s64, v38\n\t"
    "v_exp_f32 v38, v38\n\t"
    "v_mov_b32 v32, v37\n\t"
    "v_mul_f32 v34, s65, v37\n\t"
    "v_mul_f32 v33, v33, v38\n\t"
    "v_mul_f32_dpp v8, v38, v8 quad_perm:[0,0,0,0] row_mask:0xf bank_mask:0xf\n\t"
    "v_mul_f32_dpp v9, v38, v9 quad_perm:[1,1,1,1] row_mask:0xf bank_mask:0xf\n\t"
    "v_mul_f32_dpp v10, v38, v10 quad_perm:[2,2,2,2] row_mask:0xf bank_mask:0xf\n\t"
    "v_mul_f32_dpp v11, v38, v11 quad_perm:[3,3,3,3] row_mask:0xf bank_mask:0xf\n\t"
    "v_mul_f32_dpp v12, v38, v12 quad_perm:[0,0,0,0] row_mask:0xf bank_mask:0xf\n\t"
    "v_mul_f32_dpp v13, v38, v13 quad_perm:[1,1,1,1] row_mask:0xf bank_mask:0xf\n\t"
    "v_mul_f32_dpp v14, v38, v14 quad_perm:[2,2,2,2] row_mask:0xf bank_mask:0xf\n\t"
    "v_mul_f32_dpp v15, v38, v15 quad_perm:[3,3,3,3] row_mask:0xf bank_mask:0xf\n\t"
    ".Lr23_%=:\n\t"
    "v_fma_f32 v35, v35, s64, v34\n\t"
    "v_fma_f32 v36, v36, s64, v34\n\t"
    "v_exp_f32 v35, v35\n\t"
    "v_exp_f32 v36, v36\n\t"
    "s_nop 0\n\t"
    "v_cvt_pk_bf16_f32 v39, v35, v36\n\t"
    "v_dot2_f32_bf16 v33, v39, s68, v33\n\t"
    "s_nop 0\n\t"
    "v_mov_b32_dpp v16, v39 quad_perm:[0,1,2,3] row_mask:0xf bank_mask:0x1\n\t"
    "v_mov_b32_dpp v17, v39 row_ror:12 row_mask:0xf bank_mask:0x1\n\t"
    "v_mov_b32_dpp v18, v39 row_ror:8 row_mask:0xf bank_mask:0x1\n\t"
    "v_mov_b32_dpp v19, v39 row_ror:4 row_mask:0xf bank_mask:0x1\n\t"
    "v_mov_b32_dpp v20, v39 row_ror:4 row_mask:0xf bank_mask:0x2\n\t"
    "v_mov_b32_dpp v21, v39 quad_perm:[0,1,2,3] row_mask:0xf bank_mask:0x2\n\t"
    "v_mov_b32_dpp v22, v39 row_ror:12 row_mask:0xf bank_mask:0x2\n\t"
    "v_mov_b32_dpp v23, v39 row_ror:8 row_mask:0xf bank_mask:0x2\n\t"
    "v_mov_b32_dpp v24, v39 row_ror:8 row_mask:0xf bank_mask:0x4\n\t"
    "v_mov_b32_dpp v25, v39 row_ror:4 row_mask:0xf bank_mask:0x4\n\t"
    "v_mov_b32_dpp v26, v39 quad_perm:[0,1,2,3] row_mask:0xf bank_mask:0x4\n\t"
    "v_mov_b32_dpp v27, v39 row_ror:12 row_mask:0xf bank_mask:0x4\n\t"
    "v_mov_b32_dpp v28, v39 row_ror:12 row_mask:0xf bank_mask:0x8\n\t"
    "v_mov_b32_dpp v29, v39 row_ror:8 row_mask:0xf bank_mask:0x8\n\t"
    "v_mov_b32_dpp v30, v39 row_ror:4 row_mask:0xf bank_mask:0x8\n\t"
    "v_mov_b32_dpp v31, v39 quad_perm:[0,1,2,3] row_mask:0xf bank_mask:0x8\n\t"
    "v_mfma_f32_16x16x32_bf16 v[44:47], a[88:91], v[4:7], 0\n\t"
    "v_mfma_f32_16x16x32_bf16 v[48:51], a[92:95], v[4:7], 0\n\t"
    "v_mfma_f32_16x16x32_bf16 v[52:55], a[96:99], v[4:7], 0\n\t"
    "s_nop 5\n\t"
    "v_add_f32_dpp v40, v45, v44 row_ror:12 row_mask:0xf bank_mask:0xf\n\t"
    "v_add_f32_dpp v42, v47, v46 row_ror:12 row_mask:0xf bank_mask:0xf\n\t"
    "v_mfma_f32_16x16x32_bf16 v[44:47], a[100:103], v[4:7], 0\n\t"
    "v_add_f32_dpp v41, v50, v49 row_ror:12 row_mask:0xf bank_mask:0xf\n\t"
    "v_add_f32_dpp v43, v48, v51 row_ror:12 row_mask:0xf bank_mask:0xf\n\t"
    "v_add_f32_dpp v35, v42, v40 row_ror:8 row_mask:0xf bank_mask:0x1\n\t"
    "v_mfma_f32_16x16x32_bf16 v[48:51], a[104:107], v[4:7], 0\n\t"
    "v_add_f32_dpp v40, v55, v54 row_ror:12 row_mask:0xf bank_mask:0xf\n\t"
    "v_add_f32_dpp v42, v53, v52 row_ror:12 row_mask:0xf bank_mask:0xf\n\t"
    "v_add_f32_dpp v35, v43, v41 row_ror:8 row_mask:0xf bank_mask:0x2\n\t"
    "v_mfma_f32_16x16x32_bf16 v[52:55], a[108:111], v[4:7], 0\n\t"
    "v_add_f32_dpp v41, v44, v47 row_ror:12 row_mask:0xf bank_mask:0xf\n\t"
    "v_add_f32_dpp v43, v46, v45 row_ror:12 row_mask:0xf bank_mask:0xf\n\t"
    "v_add_f32_dpp v35, v42, v40 row_ror:8 row_mask:0xf bank_mask:0x4\n\t"
    "v_mfma_f32_16x16x32_bf16 v[44:47], a[112:115], v[4:7], 0\n\t"
    "v_add_f32_dpp v40, v49, v48 row_ror:12 row_mask:0xf bank_mask:0xf\n\t"
    "v_add_f32_dpp v42, v51, v50 row_ror:12 row_mask:0xf bank_mask:0xf\n\t"
    "v_add_f32_dpp v35, v43, v41 row_ror:8 row_mask:0xf bank_mask:0x8\n\t"
    "v_mfma_f32_16x16x32_bf16 v[48:51], a[116:119], v[4:7], 0\n\t"
    "v_add_f32_dpp v41, v54, v53 row_ror:12 row_mask:0xf bank_mask:0xf\n\t"
    "v_add_f32_dpp v43, v52, v55 row_ror:12 row_mask:0xf bank_mask:0xf\n\t"
    "v_add_f32_dpp v36, v42, v40 row_ror:8 row_mask:0xf bank_mask:0x1\n\t"
    "s_nop 0\n\t"
    "v_add_f32_dpp v40, v47, v46 row_ror:12 row_mask:0xf bank_mask:0xf\n\t"
    "v_add_f32_dpp v42, v45, v44 row_ror:12 row_mask:0xf bank_mask:0xf\n\t"
    "v_add_f32_dpp v36, v43, v41 row_ror:8 row_mask:0xf bank_mask:0x2\n\t"
    "s_nop 0\n\t"
    "v_add_f32_dpp v41, v48, v51 row_ror:12 row_mask:0xf bank_mask:0xf\n\t"
    "v_add_f32_dpp v43, v50, v49 row_ror:12 row_mask:0xf bank_mask:0xf\n\t"
    "v_add_f32_dpp v36, v42, v40 row_ror:8 row_mask:0xf bank_mask:0x4\n\t"
    "s_nop 0\n\t"
    "v_add_f32_dpp v36, v43, v41 row_ror:8 row_mask:0xf bank_mask:0x8\n\t"
    "v_perm_b32 v44, v192, v176, s66\n\t"
    "v_perm_b32 v45, v196, v180, s66\n\t"
    "v_perm_b32 v46, v200, v184, s66\n\t"
    "v_perm_b32 v47, v204, v188, s66\n\t"
    "v_perm_b32 v48, v192, v176, s67\n\t"
    "v_perm_b32 v49, v196, v180, s67\n\t"
    "v_perm_b32 v50, v200, v184, s67\n\t"
    "v_perm_b32 v51, v204, v188, s67\n\t"
    "v_max_f32 v37, v35, v36\n\t"
    "v_mfma_f32_16x16x32_bf16 v[8:11], v[16:19], v[44:47], v[8:11]\n\t"
    "v_perm_b32 v52, v193, v177, s66\n\t"
    "v_perm_b32 v53, v197, v181, s66\n\t"
    "v_perm_b32 v54, v201, v185, s66\n\t"
    "v_perm_b32 v55, v205, v189, s66\n\t"
    "v_max_f32_dpp v37, v37, v37 row_ror:4 row_mask:0xf bank_mask:0xf\n\t"
    "v_mfma_f32_16x16x32_bf16 v[8:11], v[20:23], v[48:51], v[8:11]\n\t"
    "v_perm_b32 v44, v193, v177, s67\n\t"
    "v_perm_b32 v45, v197, v181, s67\n\t"
    "v_perm_b32 v46, v201, v185, s67\n\t"
    "v_perm_b32 v47, v205, v189, s67\n\t"
    "v_max_f32_dpp v37, v37, v37 row_ror:8 row_mask:0xf bank_mask:0xf\n\t"
    "v_mfma_f32_16x16x32_bf16 v[8:11], v[24:27], v[52:55], v[8:11]\n\t"
    "v_perm_b32 v48, v194, v178, s66\n\t"
    "v_perm_b32 v49, v198, v182, s66\n\t"
    "v_perm_b32 v50, v202, v186, s66\n\t"
    "v_perm_b32 v51, v206, v190, s66\n\t"
    "v_mov_b32 v38, v37\n\t"
    "v_mfma_f32_16x16x32_bf16 v[8:11], v[28:31], v[44:47], v[8:11]\n\t"
    "v_perm_b32 v52, v194, v178, s67\n\t"
    "v_perm_b32 v53, v198, v182, s67\n\t"
    "v_perm_b32 v54, v202, v186, s67\n\t"
    "v_perm_b32 v55, v206, v190, s67\n\t"
    "v_permlane16_swap_b32 v37, v38\n\t"
    "v_mfma_f32_16x16x32_bf16 v[12:15], v[16:19], v[48:51], v[12:15]\n\t"
    "v_perm_b32 v44, v195, v179, s66\n\t"
    "v_perm_b32 v45, v199, v183, s66\n\t"
    "v_perm_b32 v46, v203, v187, s66\n\t"
    "v_perm_b32 v47, v207, v191, s66\n\t"
    "v_max_f32 v37, v37, v38\n\t"
    "v_mfma_f32_16x16x32_bf16 v[12:15], v[20:23], v[52:55], v[12:15]\n\t"
    "v_perm_b32 v48, v195, v179, s67\n\t"
    "v_perm_b32 v49, v199, v183, s67\n\t"
    "v_perm_b32 v50, v203, v187, s67\n\t"
    "v_perm_b32 v51, v207, v191, s67\n\t"
    "v_mov_b32 v38, v37\n\t"
    "v_mfma_f32_16x16x32_bf16 v[12:15], v[24:27], v[44:47], v[12:15]\n\t"
    "s_nop 0\n\t"
    "v_permlane32_swap_b32 v37, v38\n\t"
    "v_mfma_f32_16x16x32_bf16 v[12:15], v[28:31], v[48:51], v[12:15]\n\t"
    "s_add_u32 s33, s34, 0x18000\n\t"
    "s_and_b32 s33, s33, 0x7ffff\n\t"
    "s_add_u32 s33, s33, s56\n\t"
    "s_add_u32 s57, s33, 0\n\t"
    "s_xor_b32 s57, s57, s80\n\t"
    "buffer_load_dwordx4 v[176:179], v1, s[44:47], s57 offen offset:0 nt\n\t"
    "s_add_u32 s57, s33, 0x1000\n\t"
    "s_xor_b32 s57, s57, s80\n\t"
    "buffer_load_dwordx4 v[180:183], v1, s[44:47], s57 offen offset:0 nt\n\t"
    "s_add_u32 s57, s33, 0x2000\n\t"
    "s_xor_b32 s57, s57, s80\n\t"
    "buffer_load_dwordx4 v[184:187], v1, s[44:47], s57 offen offset:0 nt\n\t"
    "s_add_u32 s57, s33, 0x3000\n\t"
    "s_xor_b32 s57, s57, s80\n\t"
    "buffer_load_dwordx4 v[188:191], v1, s[44:47], s57 offen offset:0 nt\n\t"
    "s_add_u32 s57, s33, 0x4000\n\t"
    "s_xor_b32 s57, s57, s80\n\t"
    "buffer_load_dwordx4 v[192:195], v1, s[44:47], s57 offen offset:0 nt\n\t"
    "s_add_u32 s57, s33, 0x5000\n\t"
    "s_xor_b32 s57, s57, s80\n\t"
    "buffer_load_dwordx4 v[196:199], v1, s[44:47], s57 offen offset:0 nt\n\t"
    "s_add_u32 s57, s33, 0x6000\n\t"
    "s_xor_b32 s57, s57, s80\n\t"
    "buffer_load_dwordx4 v[200:203], v1, s[44:47], s57 offen offset:0 nt\n\t"
    "s_add_u32 s57, s33, 0x7000\n\t"
    "s_xor_b32 s57, s57, s80\n\t"
    "buffer_load_dwordx4 v[204:207], v1, s[44:47], s57 offen offset:0 nt\n\t"
    "s_add_u32 s32, s34, 0x68000\n\t"
    "s_and_b32 s32, s32, 0x7ffff\n\t"
    "s_add_u32 s32, s32, s56\n\t"
    "s_add_u32 s57, s32, 0\n\t"
    "s_xor_b32 s57, s57, s80\n\t"
    "buffer_load_dwordx4 a[88:91], v0, s[40:43], s57 offen offset:0 nt\n\t"
    "s_add_u32 s57, s32, 0x1000\n\t"
    "s_xor_b32 s57, s57, s80\n\t"
    "buffer_load_dwordx4 a[92:95], v0, s[40:43], s57 offen offset:0 nt\n\t"
    "s_add_u32 s57, s32, 0x2000\n\t"
    "s_xor_b32 s57, s57, s80\n\t"
    "buffer_load_dwordx4 a[96:99], v0, s[40:43], s57 offen offset:0 nt\n\t"
    "s_add_u32 s57, s32, 0x3000\n\t"
    "s_xor_b32 s57, s57, s80\n\t"
    "buffer_load_dwordx4 a[100:103], v0, s[40:43], s57 offen offset:0 nt\n\t"
    "s_add_u32 s57, s32, 0x4000\n\t"
    "s_xor_b32 s57, s57, s80\n\t"
    "buffer_load_dwordx4 a[104:107], v0, s[40:43], s57 offen offset:0 nt\n\t"
    "s_add_u32 s57, s32, 0x5000\n\t"
    "s_xor_b32 s57, s57, s80\n\t"
    "buffer_load_dwordx4 a[108:111], v0, s[40:43], s57 offen offset:0 nt\n\t"
    "s_add_u32 s57, s32, 0x6000\n\t"
    "s_xor_b32 s57, s57, s80\n\t"
    "buffer_load_dwordx4 a[112:115], v0, s[40:43], s57 offen offset:0 nt\n\t"
    "s_add_u32 s57, s32, 0x7000\n\t"
    "s_xor_b32 s57, s57, s80\n\t"
    "buffer_load_dwordx4 a[116:119], v0, s[40:43], s57 offen offset:0 nt\n\t"
    "v_max_f32 v37, v37, v38\n\t"
    "v_cmp_gt_f32 vcc, v37, v32\n\t"
    "s_cbranch_vccz .Lr24_%=\n\t"
    "v_max_f32 v37, v37, v32\n\t"
    "v_sub_f32 v38, v32, v37\n\t"
    "v_mul_f32 v38, s64, v38\n\t"
    "v_exp_f32 v38, v38\n\t"
    "v_mov_b32 v32, v37\n\t"
    "v_mul_f32 v34, s65, v37\n\t"
    "v_mul_f32 v33, v33, v38\n\t"
    "v_mul_f32_dpp v8, v38, v8 quad_perm:[0,0,0,0] row_mask:0xf bank_mask:0xf\n\t"
    "v_mul_f32_dpp v9, v38, v9 quad_perm:[1,1,1,1] row_mask:0xf bank_mask:0xf\n\t"
    "v_mul_f32_dpp v10, v38, v10 quad_perm:[2,2,2,2] row_mask:0xf bank_mask:0xf\n\t"
    "v_mul_f32_dpp v11, v38, v11 quad_perm:[3,3,3,3] row_mask:0xf bank_mask:0xf\n\t"
    "v_mul_f32_dpp v12, v38, v12 quad_perm:[0,0,0,0] row_mask:0xf bank_mask:0xf\n\t"
    "v_mul_f32_dpp v13, v38, v13 quad_perm:[1,1,1,1] row_mask:0xf bank_mask:0xf\n\t"
    "v_mul_f32_dpp v14, v38, v14 quad_perm:[2,2,2,2] row_mask:0xf bank_mask:0xf\n\t"
    "v_mul_f32_dpp v15, v38, v15 quad_perm:[3,3,3,3] row_mask:0xf bank_mask:0xf\n\t"
    ".Lr24_%=:\n\t"
    "v_fma_f32 v35, v35, s64, v34\n\t"
    "v_fma_f32 v36, v36, s64, v34\n\t"
    "v_exp_f32 v35, v35\n\t"
    "v_exp_f32 v36, v36\n\t"
    "s_nop 0\n\t"
    "v_cvt_pk_bf16_f32 v39, v35, v36\n\t"
    "v_dot2_f32_bf16 v33, v39, s68, v33\n\t"
    "s_nop 0\n\t"
    "v_mov_b32_dpp v16, v39 quad_perm:[0,1,2,3] row_mask:0xf bank_mask:0x1\n\t"
    "v_mov_b32_dpp v17, v39 row_ror:12 row_mask:0xf bank_mask:0x1\n\t"
    "v_mov_b32_dpp v18, v39 row_ror:8 row_mask:0xf bank_mask:0x1\n\t"
    "v_mov_b32_dpp v19, v39 row_ror:4 row_mask:0xf bank_mask:0x1\n\t"
    "v_mov_b32_dpp v20, v39 row_ror:4 row_mask:0xf bank_mask:0x2\n\t"
    "v_mov_b32_dpp v21, v39 quad_perm:[0,1,2,3] row_mask:0xf bank_mask:0x2\n\t"
    "v_mov_b32_dpp v22, v39 row_ror:12 row_mask:0xf bank_mask:0x2\n\t"
    "v_mov_b32_dpp v23, v39 row_ror:8 row_mask:0xf bank_mask:0x2\n\t"
    "v_mov_b32_dpp v24, v39 row_ror:8 row_mask:0xf bank_mask:0x4\n\t"
    "v_mov_b32_dpp v25, v39 row_ror:4 row_mask:0xf bank_mask:0x4\n\t"
    "v_mov_b32_dpp v26, v39 quad_perm:[0,1,2,3] row_mask:0xf bank_mask:0x4\n\t"
    "v_mov_b32_dpp v27, v39 row_ror:12 row_mask:0xf bank_mask:0x4\n\t"
    "v_mov_b32_dpp v28, v39 row_ror:12 row_mask:0xf bank_mask:0x8\n\t"
    "v_mov_b32_dpp v29, v39 row_ror:8 row_mask:0xf bank_mask:0x8\n\t"
    "v_mov_b32_dpp v30, v39 row_ror:4 row_mask:0xf bank_mask:0x8\n\t"
    "v_mov_b32_dpp v31, v39 quad_perm:[0,1,2,3] row_mask:0xf bank_mask:0x8\n\t"
    "v_mfma_f32_16x16x32_bf16 v[44:47], a[120:123], v[4:7], 0\n\t"
    "v_mfma_f32_16x16x32_bf16 v[48:51], a[124:127], v[4:7], 0\n\t"
    "v_mfma_f32_16x16x32_bf16 v[52:55], a[128:131], v[4:7], 0\n\t"
    "s_nop 5\n\t"
    "v_add_f32_dpp v40, v45, v44 row_ror:12 row_mask:0xf bank_mask:0xf\n\t"
    "v_add_f32_dpp v42, v47, v46 row_ror:12 row_mask:0xf bank_mask:0xf\n\t"
    "v_mfma_f32_16x16x32_bf16 v[44:47], a[132:135], v[4:7], 0\n\t"
    "v_add_f32_dpp v41, v50, v49 row_ror:12 row_mask:0xf bank_mask:0xf\n\t"
    "v_add_f32_dpp v43, v48, v51 row_ror:12 row_mask:0xf bank_mask:0xf\n\t"
    "v_add_f32_dpp v35, v42, v40 row_ror:8 row_mask:0xf bank_mask:0x1\n\t"
    "v_mfma_f32_16x16x32_bf16 v[48:51], a[136:139], v[4:7], 0\n\t"
    "v_add_f32_dpp v40, v55, v54 row_ror:12 row_mask:0xf bank_mask:0xf\n\t"
    "v_add_f32_dpp v42, v53, v52 row_ror:12 row_mask:0xf bank_mask:0xf\n\t"
    "v_add_f32_dpp v35, v43, v41 row_ror:8 row_mask:0xf bank_mask:0x2\n\t"
    "v_mfma_f32_16x16x32_bf16 v[52:55], a[140:143], v[4:7], 0\n\t"
    "v_add_f32_dpp v41, v44, v47 row_ror:12 row_mask:0xf bank_mask:0xf\n\t"
    "v_add_f32_dpp v43, v46, v45 row_ror:12 row_mask:0xf bank_mask:0xf\n\t"
    "v_add_f32_dpp v35, v42, v40 row_ror:8 row_mask:0xf bank_mask:0x4\n\t"
    "v_mfma_f32_16x16x32_bf16 v[44:47], a[144:147], v[4:7], 0\n\t"
    "v_add_f32_dpp v40, v49, v48 row_ror:12 row_mask:0xf bank_mask:0xf\n\t"
    "v_add_f32_dpp v42, v51, v50 row_ror:12 row_mask:0xf bank_mask:0xf\n\t"
    "v_add_f32_dpp v35, v43, v41 row_ror:8 row_mask:0xf bank_mask:0x8\n\t"
    "v_mfma_f32_16x16x32_bf16 v[48:51], a[148:151], v[4:7], 0\n\t"
    "v_add_f32_dpp v41, v54, v53 row_ror:12 row_mask:0xf bank_mask:0xf\n\t"
    "v_add_f32_dpp v43, v52, v55 row_ror:12 row_mask:0xf bank_mask:0xf\n\t"
    "v_add_f32_dpp v36, v42, v40 row_ror:8 row_mask:0xf bank_mask:0x1\n\t"
    "s_nop 0\n\t"
    "v_add_f32_dpp v40, v47, v46 row_ror:12 row_mask:0xf bank_mask:0xf\n\t"
    "v_add_f32_dpp v42, v45, v44 row_ror:12 row_mask:0xf bank_mask:0xf\n\t"
    "v_add_f32_dpp v36, v43, v41 row_ror:8 row_mask:0xf bank_mask:0x2\n\t"
    "s_nop 0\n\t"
    "v_add_f32_dpp v41, v48, v51 row_ror:12 row_mask:0xf bank_mask:0xf\n\t"
    "v_add_f32_dpp v43, v50, v49 row_ror:12 row_mask:0xf bank_mask:0xf\n\t"
    "v_add_f32_dpp v36, v42, v40 row_ror:8 row_mask:0xf bank_mask:0x4\n\t"
    "s_nop 0\n\t"
    "v_add_f32_dpp v36, v43, v41 row_ror:8 row_mask:0xf bank_mask:0x8\n\t"
    "v_perm_b32 v44, v72, v56, s66\n\t"
    "v_perm_b32 v45, v76, v60, s66\n\t"
    "v_perm_b32 v46, v80, v64, s66\n\t"
    "v_perm_b32 v47, v84, v68, s66\n\t"
    "v_perm_b32 v48, v72, v56, s67\n\t"
    "v_perm_b32 v49, v76, v60, s67\n\t"
    "v_perm_b32 v50, v80, v64, s67\n\t"
    "v_perm_b32 v51, v84, v68, s67\n\t"
    "v_max_f32 v37, v35, v36\n\t"
    "v_mfma_f32_16x16x32_bf16 v[8:11], v[16:19], v[44:47], v[8:11]\n\t"
    "v_perm_b32 v52, v73, v57, s66\n\t"
    "v_perm_b32 v53, v77, v61, s66\n\t"
    "v_perm_b32 v54, v81, v65, s66\n\t"
    "v_perm_b32 v55, v85, v69, s66\n\t"
    "v_max_f32_dpp v37, v37, v37 row_ror:4 row_mask:0xf bank_mask:0xf\n\t"
    "v_mfma_f32_16x16x32_bf16 v[8:11], v[20:23], v[48:51], v[8:11]\n\t"
    "v_perm_b32 v44, v73, v57, s67\n\t"
    "v_perm_b32 v45, v77, v61, s67\n\t"
    "v_perm_b32 v46, v81, v65, s67\n\t"
    "v_perm_b32 v47, v85, v69, s67\n\t"
    "v_max_f32_dpp v37, v37, v37 row_ror:8 row_mask:0xf bank_mask:0xf\n\t"
    "v_mfma_f32_16x16x32_bf16 v[8:11], v[24:27], v[52:55], v[8:11]\n\t"
    "v_perm_b32 v48, v74, v58, s66\n\t"
    "v_perm_b32 v49, v78, v62, s66\n\t"
    "v_perm_b32 v50, v82, v66, s66\n\t"
    "v_perm_b32 v51, v86, v70, s66\n\t"
    "v_mov_b32 v38, v37\n\t"
    "v_mfma_f32_16x16x32_bf16 v[8:11], v[28:31], v[44:47], v[8:11]\n\t"
    "v_perm_b32 v52, v74, v58, s67\n\t"
    "v_perm_b32 v53, v78, v62, s67\n\t"
    "v_perm_b32 v54, v82, v66, s67\n\t"
    "v_perm_b32 v55, v86, v70, s67\n\t"
    "v_permlane16_swap_b32 v37, v38\n\t"
    "v_mfma_f32_16x16x32_bf16 v[12:15], v[16:19], v[48:51], v[12:15]\n\t"
    "v_perm_b32 v44, v75, v59, s66\n\t"
    "v_perm_b32 v45, v79, v63, s66\n\t"
    "v_perm_b32 v46, v83, v67, s66\n\t"
    "v_perm_b32 v47, v87, v71, s66\n\t"
    "v_max_f32 v37, v37, v38\n\t"
    "v_mfma_f32_16x16x32_bf16 v[12:15], v[20:23], v[52:55], v[12:15]\n\t"
    "v_perm_b32 v48, v75, v59, s67\n\t"
    "v_perm_b32 v49, v79, v63, s67\n\t"
    "v_perm_b32 v50, v83, v67, s67\n\t"
    "v_perm_b32 v51, v87, v71, s67\n\t"
    "v_mov_b32 v38, v37\n\t"
    "v_mfma_f32_16x16x32_bf16 v[12:15], v[24:27], v[44:47], v[12:15]\n\t"
    "s_nop 0\n\t"
    "v_permlane32_swap_b32 v37, v38\n\t"
    "v_mfma_f32_16x16x32_bf16 v[12:15], v[28:31], v[48:51], v[12:15]\n\t"
    "s_add_u32 s33, s34, 0x20000\n\t"
    "s_and_b32 s33, s33, 0x7ffff\n\t"
    "s_add_u32 s33, s33, s56\n\t"
    "s_add_u32 s57, s33, 0\n\t"
    "s_xor_b32 s57, s57, s80\n\t"
    "buffer_load_dwordx4 v[56:59], v1, s[44:47], s57 offen offset:0 nt\n\t"
    "s_add_u32 s57, s33, 0x1000\n\t"
    "s_xor_b32 s57, s57, s80\n\t"
    "buffer_load_dwordx4 v[60:63], v1, s[44:47], s57 offen offset:0 nt\n\t"
    "s_add_u32 s57, s33, 0x2000\n\t"
    "s_xor_b32 s57, s57, s80\n\t"
    "buffer_load_dwordx4 v[64:67], v1, s[44:47], s57 offen offset:0 nt\n\t"
    "s_add_u32 s57, s33, 0x3000\n\t"
    "s_xor_b32 s57, s57, s80\n\t"
    "buffer_load_dwordx4 v[68:71], v1, s[44:47], s57 offen offset:0 nt\n\t"
    "s_waitcnt vmcnt(27)\n\t"
    "s_add_u32 s57, s33, 0x4000\n\t"
    "s_xor_b32 s57, s57, s80\n\t"
    "buffer_load_dwordx4 v[72:75], v1, s[44:47], s57 offen offset:0 nt\n\t"
    "s_waitcnt vmcnt(27)\n\t"
    "s_add_u32 s57, s33, 0x5000\n\t"
    "s_xor_b32 s57, s57, s80\n\t"
    "buffer_load_dwordx4 v[76:79], v1, s[44:47], s57 offen offset:0 nt\n\t"
    "s_waitcnt vmcnt(27)\n\t"
    "s_add_u32 s57, s33, 0x6000\n\t"
    "s_xor_b32 s57, s57, s80\n\t"
    "buffer_load_dwordx4 v[80:83], v1, s[44:47], s57 offen offset:0 nt\n\t"
    "v_max_f32 v37, v37, v38\n\t"
    "v_cmp_gt_f32 vcc, v37, v32\n\t"
    "s_cbranch_vccz .Lr25_%=\n\t"
    "v_max_f32 v37, v37, v32\n\t"
    "v_sub_f32 v38, v32, v37\n\t"
    "v_mul_f32 v38, s64, v38\n\t"
    "v_exp_f32 v38, v38\n\t"
    "v_mov_b32 v32, v37\n\t"
    "v_mul_f32 v34, s65, v37\n\t"
    "v_mul_f32 v33, v33, v38\n\t"
    "v_mul_f32_dpp v8, v38, v8 quad_perm:[0,0,0,0] row_mask:0xf bank_mask:0xf\n\t"
    "v_mul_f32_dpp v9, v38, v9 quad_perm:[1,1,1,1] row_mask:0xf bank_mask:0xf\n\t"
    "v_mul_f32_dpp v10, v38, v10 quad_perm:[2,2,2,2] row_mask:0xf bank_mask:0xf\n\t"
    "v_mul_f32_dpp v11, v38, v11 quad_perm:[3,3,3,3] row_mask:0xf bank_mask:0xf\n\t"
    "v_mul_f32_dpp v12, v38, v12 quad_perm:[0,0,0,0] row_mask:0xf bank_mask:0xf\n\t"
    "v_mul_f32_dpp v13, v38, v13 quad_perm:[1,1,1,1] row_mask:0xf bank_mask:0xf\n\t"
    "v_mul_f32_dpp v14, v38, v14 quad_perm:[2,2,2,2] row_mask:0xf bank_mask:0xf\n\t"
    "v_mul_f32_dpp v15, v38, v15 quad_perm:[3,3,3,3] row_mask:0xf bank_mask:0xf\n\t"
    ".Lr25_%=:\n\t"
    "v_fma_f32 v35, v35, s64, v34\n\t"
    "v_fma_f32 v36, v36, s64, v34\n\t"
    "v_exp_f32 v35, v35\n\t"
    "v_exp_f32 v36, v36\n\t"
    "s_nop 0\n\t"
    "v_cvt_pk_bf16_f32 v39, v35, v36\n\t"
    "v_dot2_f32_bf16 v33, v39, s68, v33\n\t"
    "s_waitcnt vmcnt(27)\n\t"
    "s_add_u32 s57, s33, 0x7000\n\t"
    "s_xor_b32 s57, s57, s80\n\t"
    "buffer_load_dwordx4 v[84:87], v1, s[44:47], s57 offen offset:0 nt\n\t"
    "v_mov_b32_dpp v16, v39 quad_perm:[0,1,2,3] row_mask:0xf bank_mask:0x1\n\t"
    "v_mov_b32_dpp v17, v39 row_ror:12 row_mask:0xf bank_mask:0x1\n\t"
    "v_mov_b32_dpp v18, v39 row_ror:8 row_mask:0xf bank_mask:0x1\n\t"
    "v_mov_b32_dpp v19, v39 row_ror:4 row_mask:0xf bank_mask:0x1\n\t"
    "v_mov_b32_dpp v20, v39 row_ror:4 row_mask:0xf bank_mask:0x2\n\t"
    "v_mov_b32_dpp v21, v39 quad_perm:[0,1,2,3] row_mask:0xf bank_mask:0x2\n\t"
    "v_mov_b32_dpp v22, v39 row_ror:12 row_mask:0xf bank_mask:0x2\n\t"
    "v_mov_b32_dpp v23, v39 row_ror:8 row_mask:0xf bank_mask:0x2\n\t"
    "v_mov_b32_dpp v24, v39 row_ror:8 row_mask:0xf bank_mask:0x4\n\t"
    "v_mov_b32_dpp v25, v39 row_ror:4 row_mask:0xf bank_mask:0x4\n\t"
    "v_mov_b32_dpp v26, v39 quad_perm:[0,1,2,3] row_mask:0xf bank_mask:0x4\n\t"
    "v_mov_b32_dpp v27, v39 row_ror:12 row_mask:0xf bank_mask:0x4\n\t"
    "v_mov_b32_dpp v28, v39 row_ror:12 row_mask:0xf bank_mask:0x8\n\t"
    "v_mov_b32_dpp v29, v39 row_ror:8 row_mask:0xf bank_mask:0x8\n\t"
    "v_mov_b32_dpp v30, v39 row_ror:4 row_mask:0xf bank_mask:0x8\n\t"
    "v_mov_b32_dpp v31, v39 quad_perm:[0,1,2,3] row_mask:0xf bank_mask:0x8\n\t"
    "v_mfma_f32_16x16x32_bf16 v[44:47], a[152:155], v[4:7], 0\n\t"
    "v_mfma_f32_16x16x32_bf16 v[48:51], a[156:159], v[4:7], 0\n\t"
    "v_mfma_f32_16x16x32_bf16 v[52:55], a[160:163], v[4:7], 0\n\t"
    "s_nop 5\n\t"
    "v_add_f32_dpp v40, v45, v44 row_ror:12 row_mask:0xf bank_mask:0xf\n\t"
    "v_add_f32_dpp v42, v47, v46 row_ror:12 row_mask:0xf bank_mask:0xf\n\t"
    "v_mfma_f32_16x16x32_bf16 v[44:47], a[164:167], v[4:7], 0\n\t"
    "v_add_f32_dpp v41, v50, v49 row_ror:12 row_mask:0xf bank_mask:0xf\n\t"
    "v_add_f32_dpp v43, v48, v51 row_ror:12 row_mask:0xf bank_mask:0xf\n\t"
    "v_add_f32_dpp v35, v42, v40 row_ror:8 row_mask:0xf bank_mask:0x1\n\t"
    "v_mfma_f32_16x16x32_bf16 v[48:51], a[168:171], v[4:7], 0\n\t"
    "v_add_f32_dpp v40, v55, v54 row_ror:12 row_mask:0xf bank_mask:0xf\n\t"
    "v_add_f32_dpp v42, v53, v52 row_ror:12 row_mask:0xf bank_mask:0xf\n\t"
    "v_add_f32_dpp v35, v43, v41 row_ror:8 row_mask:0xf bank_mask:0x2\n\t"
    "v_mfma_f32_16x16x32_bf16 v[52:55], a[172:175], v[4:7], 0\n\t"
    "v_add_f32_dpp v41, v44, v47 row_ror:12 row_mask:0xf bank_mask:0xf\n\t"
    "v_add_f32_dpp v43, v46, v45 row_ror:12 row_mask:0xf bank_mask:0xf\n\t"
    "v_add_f32_dpp v35, v42, v40 row_ror:8 row_mask:0xf bank_mask:0x4\n\t"
    "v_mfma_f32_16x16x32_bf16 v[44:47], a[176:179], v[4:7], 0\n\t"
    "v_add_f32_dpp v40, v49, v48 row_ror:12 row_mask:0xf bank_mask:0xf\n\t"
    "v_add_f32_dpp v42, v51, v50 row_ror:12 row_mask:0xf bank_mask:0xf\n\t"
    "v_add_f32_dpp v35, v43, v41 row_ror:8 row_mask:0xf bank_mask:0x8\n\t"
    "v_mfma_f32_16x16x32_bf16 v[48:51], a[180:183], v[4:7], 0\n\t"
    "v_add_f32_dpp v41, v54, v53 row_ror:12 row_mask:0xf bank_mask:0xf\n\t"
    "v_add_f32_dpp v43, v52, v55 row_ror:12 row_mask:0xf bank_mask:0xf\n\t"
    "v_add_f32_dpp v36, v42, v40 row_ror:8 row_mask:0xf bank_mask:0x1\n\t"
    "s_nop 0\n\t"
    "v_add_f32_dpp v40, v47, v46 row_ror:12 row_mask:0xf bank_mask:0xf\n\t"
    "v_add_f32_dpp v42, v45, v44 row_ror:12 row_mask:0xf bank_mask:0xf\n\t"
    "v_add_f32_dpp v36, v43, v41 row_ror:8 row_mask:0xf bank_mask:0x2\n\t"
    "s_nop 0\n\t"
    "v_add_f32_dpp v41, v48, v51 row_ror:12 row_mask:0xf bank_mask:0xf\n\t"
    "v_add_f32_dpp v43, v50, v49 row_ror:12 row_mask:0xf bank_mask:0xf\n\t"
    "v_add_f32_dpp v36, v42, v40 row_ror:8 row_mask:0xf bank_mask:0x4\n\t"
    "s_nop 0\n\t"
    "v_add_f32_dpp v36, v43, v41 row_ror:8 row_mask:0xf bank_mask:0x8\n\t"
    "v_perm_b32 v44, v104, v88, s66\n\t"
    "v_perm_b32 v45, v108, v92, s66\n\t"
    "v_perm_b32 v46, v112, v96, s66\n\t"
    "v_perm_b32 v47, v116, v100, s66\n\t"
    "v_perm_b32 v48, v104, v88, s67\n\t"
    "v_perm_b32 v49, v108, v92, s67\n\t"
    "v_perm_b32 v50, v112, v96, s67\n\t"
    "v_perm_b32 v51, v116, v100, s67\n\t"
    "v_max_f32 v37, v35, v36\n\t"
    "v_mfma_f32_16x16x32_bf16 v[8:11], v[16:19], v[44:47], v[8:11]\n\t"
    "s_waitcnt vmcnt(27)\n\t"
    "s_add_u32 s32, s34, 0x28000\n\t"
    "s_and_b32 s32, s32, 0x7ffff\n\t"
    "s_add_u32 s32, s32, s56\n\t"
    "s_add_u32 s57, s32, 0\n\t"
    "s_xor_b32 s57, s57, s80\n\t"
    "buffer_load_dwordx4 a[120:123], v0, s[40:43], s57 offen offset:0 nt\n\t"
    "v_perm_b32 v52, v105, v89, s66\n\t"
    "v_perm_b32 v53, v109, v93, s66\n\t"
    "v_perm_b32 v54, v113, v97, s66\n\t"
    "v_perm_b32 v55, v117, v101, s66\n\t"
    "v_max_f32_dpp v37, v37, v37 row_ror:4 row_mask:0xf bank_mask:0xf\n\t"
    "v_mfma_f32_16x16x32_bf16 v[8:11], v[20:23], v[48:51], v[8:11]\n\t"
    "v_perm_b32 v44, v105, v89, s67\n\t"
    "v_perm_b32 v45, v109, v93, s67\n\t"
    "v_perm_b32 v46, v113, v97, s67\n\t"
    "v_perm_b32 v47, v117, v101, s67\n\t"
    "v_max_f32_dpp v37, v37, v37 row_ror:8 row_mask:0xf bank_mask:0xf\n\t"
    "v_mfma_f32_16x16x32_bf16 v[8:11], v[24:27], v[52:55], v[8:11]\n\t"
    "v_perm_b32 v48, v106, v90, s66\n\t"
    "v_perm_b32 v49, v110, v94, s66\n\t"
    "v_perm_b32 v50, v114, v98, s66\n\t"
    "v_perm_b32 v51, v118, v102, s66\n\t"
    "v_mov_b32 v38, v37\n\t"
    "v_mfma_f32_16x16x32_bf16 v[8:11], v[28:31], v[44:47], v[8:11]\n\t"
    "v_perm_b32 v52, v106, v90, s67\n\t"
    "v_perm_b32 v53, v110, v94, s67\n\t"
    "v_perm_b32 v54, v114, v98, s67\n\t"
    "v_perm_b32 v55, v118, v102, s67\n\t"
    "v_permlane16_swap_b32 v37, v38\n\t"
    "v_mfma_f32_16x16x32_bf16 v[12:15], v[16:19], v[48:51], v[12:15]\n\t"
    "v_perm_b32 v44, v107, v91, s66\n\t"
    "v_perm_b32 v45, v111, v95, s66\n\t"
    "v_perm_b32 v46, v115, v99, s66\n\t"
    "v_perm_b32 v47, v119, v103, s66\n\t"
    "v_max_f32 v37, v37, v38\n\t"
    "v_mfma_f32_16x16x32_bf16 v[12:15], v[20:23], v[52:55], v[12:15]\n\t"
    "v_perm_b32 v48, v107, v91, s67\n\t"
    "v_perm_b32 v49, v111, v95, s67\n\t"
    "v_perm_b32 v50, v115, v99, s67\n\t"
    "v_perm_b32 v51, v119, v103, s67\n\t"
    "v_mov_b32 v38, v37\n\t"
    "v_mfma_f32_16x16x32_bf16 v[12:15], v[24:27], v[44:47], v[12:15]\n\t"
    "s_nop 0\n\t"
    "v_permlane32_swap_b32 v37, v38\n\t"
    "v_mfma_f32_16x16x32_bf16 v[12:15], v[28:31], v[48:51], v[12:15]\n\t"
    "v_max_f32 v37, v37, v38\n\t"
    "v_cmp_gt_f32 vcc, v37, v32\n\t"
    "s_cbranch_vccz .Lr26_%=\n\t"
    "v_max_f32 v37, v37, v32\n\t"
    "v_sub_f32 v38, v32, v37\n\t"
    "v_mul_f32 v38, s64, v38\n\t"
    "v_exp_f32 v38, v38\n\t"
    "v_mov_b32 v32, v37\n\t"
    "v_mul_f32 v34, s65, v37\n\t"
    "v_mul_f32 v33, v33, v38\n\t"
    "v_mul_f32_dpp v8, v38, v8 quad_perm:[0,0,0,0] row_mask:0xf bank_mask:0xf\n\t"
    "v_mul_f32_dpp v9, v38, v9 quad_perm:[1,1,1,1] row_mask:0xf bank_mask:0xf\n\t"
    "v_mul_f32_dpp v10, v38, v10 quad_perm:[2,2,2,2] row_mask:0xf bank_mask:0xf\n\t"
    "v_mul_f32_dpp v11, v38, v11 quad_perm:[3,3,3,3] row_mask:0xf bank_mask:0xf\n\t"
    "v_mul_f32_dpp v12, v38, v12 quad_perm:[0,0,0,0] row_mask:0xf bank_mask:0xf\n\t"
    "v_mul_f32_dpp v13, v38, v13 quad_perm:[1,1,1,1] row_mask:0xf bank_mask:0xf\n\t"
    "v_mul_f32_dpp v14, v38, v14 quad_perm:[2,2,2,2] row_mask:0xf bank_mask:0xf\n\t"
    "v_mul_f32_dpp v15, v38, v15 quad_perm:[3,3,3,3] row_mask:0xf bank_mask:0xf\n\t"
    ".Lr26_%=:\n\t"
    "v_fma_f32 v35, v35, s64, v34\n\t"
    "v_fma_f32 v36, v36, s64, v34\n\t"
    "v_exp_f32 v35, v35\n\t"
    "v_exp_f32 v36, v36\n\t"
    "s_nop 0\n\t"
    "v_cvt_pk_bf16_f32 v39, v35, v36\n\t"
    "v_dot2_f32_bf16 v33, v39, s68, v33\n\t"
    "s_waitcnt vmcnt(27)\n\t"
    "s_add_u32 s57, s32, 0x1000\n\t"
    "s_xor_b32 s57, s57, s80\n\t"
    "buffer_load_dwordx4 a[124:127], v0, s[40:43], s57 offen offset:0 nt\n\t"
    "v_mov_b32_dpp v16, v39 quad_perm:[0,1,2,3] row_mask:0xf bank_mask:0x1\n\t"
    "v_mov_b32_dpp v17, v39 row_ror:12 row_mask:0xf bank_mask:0x1\n\t"
    "v_mov_b32_dpp v18, v39 row_ror:8 row_mask:0xf bank_mask:0x1\n\t"
    "v_mov_b32_dpp v19, v39 row_ror:4 row_mask:0xf bank_mask:0x1\n\t"
    "v_mov_b32_dpp v20, v39 row_ror:4 row_mask:0xf bank_mask:0x2\n\t"
    "v_mov_b32_dpp v21, v39 quad_perm:[0,1,2,3] row_mask:0xf bank_mask:0x2\n\t"
    "v_mov_b32_dpp v22, v39 row_ror:12 row_mask:0xf bank_mask:0x2\n\t"
    "v_mov_b32_dpp v23, v39 row_ror:8 row_mask:0xf bank_mask:0x2\n\t"
    "v_mov_b32_dpp v24, v39 row_ror:8 row_mask:0xf bank_mask:0x4\n\t"
    "v_mov_b32_dpp v25, v39 row_ror:4 row_mask:0xf bank_mask:0x4\n\t"
    "v_mov_b32_dpp v26, v39 quad_perm:[0,1,2,3] row_mask:0xf bank_mask:0x4\n\t"
    "v_mov_b32_dpp v27, v39 row_ror:12 row_mask:0xf bank_mask:0x4\n\t"
    "v_mov_b32_dpp v28, v39 row_ror:12 row_mask:0xf bank_mask:0x8\n\t"
    "v_mov_b32_dpp v29, v39 row_ror:8 row_mask:0xf bank_mask:0x8\n\t"
    "v_mov_b32_dpp v30, v39 row_ror:4 row_mask:0xf bank_mask:0x8\n\t"
    "v_mov_b32_dpp v31, v39 quad_perm:[0,1,2,3] row_mask:0xf bank_mask:0x8\n\t"
    "v_mfma_f32_16x16x32_bf16 v[44:47], a[184:187], v[4:7], 0\n\t"
    "v_mfma_f32_16x16x32_bf16 v[48:51], a[188:191], v[4:7], 0\n\t"
    "v_mfma_f32_16x16x32_bf16 v[52:55], a[192:195], v[4:7], 0\n\t"
    "s_nop 5\n\t"
    "v_add_f32_dpp v40, v45, v44 row_ror:12 row_mask:0xf bank_mask:0xf\n\t"
    "v_add_f32_dpp v42, v47, v46 row_ror:12 row_mask:0xf bank_mask:0xf\n\t"
    "v_mfma_f32_16x16x32_bf16 v[44:47], a[196:199], v[4:7], 0\n\t"
    "v_add_f32_dpp v41, v50, v49 row_ror:12 row_mask:0xf bank_mask:0xf\n\t"
    "v_add_f32_dpp v43, v48, v51 row_ror:12 row_mask:0xf bank_mask:0xf\n\t"
    "v_add_f32_dpp v35, v42, v40 row_ror:8 row_mask:0xf bank_mask:0x1\n\t"
    "v_mfma_f32_16x16x32_bf16 v[48:51], a[200:203], v[4:7], 0\n\t"
    "v_add_f32_dpp v40, v55, v54 row_ror:12 row_mask:0xf bank_mask:0xf\n\t"
    "v_add_f32_dpp v42, v53, v52 row_ror:12 row_mask:0xf bank_mask:0xf\n\t"
    "v_add_f32_dpp v35, v43, v41 row_ror:8 row_mask:0xf bank_mask:0x2\n\t"
    "v_mfma_f32_16x16x32_bf16 v[52:55], a[204:207], v[4:7], 0\n\t"
    "v_add_f32_dpp v41, v44, v47 row_ror:12 row_mask:0xf bank_mask:0xf\n\t"
    "v_add_f32_dpp v43, v46, v45 row_ror:12 row_mask:0xf bank_mask:0xf\n\t"
    "v_add_f32_dpp v35, v42, v40 row_ror:8 row_mask:0xf bank_mask:0x4\n\t"
    "v_mfma_f32_16x16x32_bf16 v[44:47], a[208:211], v[4:7], 0\n\t"
    "v_add_f32_dpp v40, v49, v48 row_ror:12 row_mask:0xf bank_mask:0xf\n\t"
    "v_add_f32_dpp v42, v51, v50 row_ror:12 row_mask:0xf bank_mask:0xf\n\t"
    "v_add_f32_dpp v35, v43, v41 row_ror:8 row_mask:0xf bank_mask:0x8\n\t"
    "v_mfma_f32_16x16x32_bf16 v[48:51], a[212:215], v[4:7], 0\n\t"
    "s_waitcnt vmcnt(27)\n\t"
    "s_add_u32 s57, s32, 0x2000\n\t"
    "s_xor_b32 s57, s57, s80\n\t"
    "buffer_load_dwordx4 a[128:131], v0, s[40:43], s57 offen offset:0 nt\n\t"
    "v_add_f32_dpp v41, v54, v53 row_ror:12 row_mask:0xf bank_mask:0xf\n\t"
    "v_add_f32_dpp v43, v52, v55 row_ror:12 row_mask:0xf bank_mask:0xf\n\t"
    "v_add_f32_dpp v36, v42, v40 row_ror:8 row_mask:0xf bank_mask:0x1\n\t"
    "v_add_f32_dpp v40, v47, v46 row_ror:12 row_mask:0xf bank_mask:0xf\n\t"
    "v_add_f32_dpp v42, v45, v44 row_ror:12 row_mask:0xf bank_mask:0xf\n\t"
    "v_add_f32_dpp v36, v43, v41 row_ror:8 row_mask:0xf bank_mask:0x2\n\t"
    "v_add_f32_dpp v41, v48, v51 row_ror:12 row_mask:0xf bank_mask:0xf\n\t"
    "v_add_f32_dpp v43, v50, v49 row_ror:12 row_mask:0xf bank_mask:0xf\n\t"
    "v_add_f32_dpp v36, v42, v40 row_ror:8 row_mask:0xf bank_mask:0x4\n\t"
    "s_nop 0\n\t"
    "v_add_f32_dpp v36, v43, v41 row_ror:8 row_mask:0xf bank_mask:0x8\n\t"
    "v_perm_b32 v44, v144, v128, s66\n\t"
    "v_perm_b32 v45, v148, v132, s66\n\t"
    "v_perm_b32 v46, v152, v136, s66\n\t"
    "v_perm_b32 v47, v156, v140, s66\n\t"
    "v_perm_b32 v48, v144, v128, s67\n\t"
    "v_perm_b32 v49, v148, v132, s67\n\t"
    "v_perm_b32 v50, v152, v136, s67\n\t"
    "v_perm_b32 v51, v156, v140, s67\n\t"
    "v_max_f32 v37, v35, v36\n\t"
    "v_mfma_f32_16x16x32_bf16 v[8:11], v[16:19], v[44:47], v[8:11]\n\t"
    "v_perm_b32 v52, v145, v129, s66\n\t"
    "v_perm_b32 v53, v149, v133, s66\n\t"
    "v_perm_b32 v54, v153, v137, s66\n\t"
    "v_perm_b32 v55, v157, v141, s66\n\t"
    "v_max_f32_dpp v37, v37, v37 row_ror:4 row_mask:0xf bank_mask:0xf\n\t"
    "v_mfma_f32_16x16x32_bf16 v[8:11], v[20:23], v[48:51], v[8:11]\n\t"
    "v_perm_b32 v44, v145, v129, s67\n\t"
    "v_perm_b32 v45, v149, v133, s67\n\t"
    "v_perm_b32 v46, v153, v137, s67\n\t"
    "v_perm_b32 v47, v157, v141, s67\n\t"
    "v_max_f32_dpp v37, v37, v37 row_ror:8 row_mask:0xf bank_mask:0xf\n\t"
    "v_mfma_f32_16x16x32_bf16 v[8:11], v[24:27], v[52:55], v[8:11]\n\t"
    "v_perm_b32 v48, v146, v130, s66\n\t"
    "v_perm_b32 v49, v150, v134, s66\n\t"
    "v_perm_b32 v50, v154, v138, s66\n\t"
    "v_perm_b32 v51, v158, v142, s66\n\t"
    "v_mov_b32 v38, v37\n\t"
    "v_mfma_f32_16x16x32_bf16 v[8:11], v[28:31], v[44:47], v[8:11]\n\t"
    "v_perm_b32 v52, v146, v130, s67\n\t"
    "v_perm_b32 v53, v150, v134, s67\n\t"
    "v_perm_b32 v54, v154, v138, s67\n\t"
    "v_perm_b32 v55, v158, v142, s67\n\t"
    "v_permlane16_swap_b32 v37, v38\n\t"
    "v_mfma_f32_16x16x32_bf16 v[12:15], v[16:19], v[48:51], v[12:15]\n\t"
    "v_perm_b32 v44, v147, v131, s66\n\t"
    "v_perm_b32 v45, v151, v135, s66\n\t"
    "v_perm_b32 v46, v155, v139, s66\n\t"
    "v_perm_b32 v47, v159, v143, s66\n\t"
    "v_max_f32 v37, v37, v38\n\t"
    "v_mfma_f32_16x16x32_bf16 v[12:15], v[20:23], v[52:55], v[12:15]\n\t"
    "v_perm_b32 v48, v147, v131, s67\n\t"
    "v_perm_b32 v49, v151, v135, s67\n\t"
    "v_perm_b32 v50, v155, v139, s67\n\t"
    "v_perm_b32 v51, v159, v143, s67\n\t"
    "v_mov_b32 v38, v37\n\t"
    "v_mfma_f32_16x16x32_bf16 v[12:15], v[24:27], v[44:47], v[12:15]\n\t"
    "s_waitcnt vmcnt(27)\n\t"
    "s_add_u32 s57, s32, 0x3000\n\t"
    "s_xor_b32 s57, s57, s80\n\t"
    "buffer_load_dwordx4 a[132:135], v0, s[40:43], s57 offen offset:0 nt\n\t"
    "v_permlane32_swap_b32 v37, v38\n\t"
    "v_mfma_f32_16x16x32_bf16 v[12:15], v[28:31], v[48:51], v[12:15]\n\t"
    "v_max_f32 v37, v37, v38\n\t"
    "v_cmp_gt_f32 vcc, v37, v32\n\t"
    "s_cbranch_vccz .Lr27_%=\n\t"
    "v_max_f32 v37, v37, v32\n\t"
    "v_sub_f32 v38, v32, v37\n\t"
    "v_mul_f32 v38, s64, v38\n\t"
    "v_exp_f32 v38, v38\n\t"
    "v_mov_b32 v32, v37\n\t"
    "v_mul_f32 v34, s65, v37\n\t"
    "v_mul_f32 v33, v33, v38\n\t"
    "v_mul_f32_dpp v8, v38, v8 quad_perm:[0,0,0,0] row_mask:0xf bank_mask:0xf\n\t"
    "v_mul_f32_dpp v9, v38, v9 quad_perm:[1,1,1,1] row_mask:0xf bank_mask:0xf\n\t"
    "v_mul_f32_dpp v10, v38, v10 quad_perm:[2,2,2,2] row_mask:0xf bank_mask:0xf\n\t"
    "v_mul_f32_dpp v11, v38, v11 quad_perm:[3,3,3,3] row_mask:0xf bank_mask:0xf\n\t"
    "v_mul_f32_dpp v12, v38, v12 quad_perm:[0,0,0,0] row_mask:0xf bank_mask:0xf\n\t"
    "v_mul_f32_dpp v13, v38, v13 quad_perm:[1,1,1,1] row_mask:0xf bank_mask:0xf\n\t"
    "v_mul_f32_dpp v14, v38, v14 quad_perm:[2,2,2,2] row_mask:0xf bank_mask:0xf\n\t"
    "v_mul_f32_dpp v15, v38, v15 quad_perm:[3,3,3,3] row_mask:0xf bank_mask:0xf\n\t"
    ".Lr27_%=:\n\t"
    "v_fma_f32 v35, v35, s64, v34\n\t"
    "v_fma_f32 v36, v36, s64, v34\n\t"
    "v_exp_f32 v35, v35\n\t"
    "v_exp_f32 v36, v36\n\t"
    "s_nop 0\n\t"
    "v_cvt_pk_bf16_f32 v39, v35, v36\n\t"
    "v_dot2_f32_bf16 v33, v39, s68, v33\n\t"
    "s_nop 0\n\t"
    "v_mov_b32_dpp v16, v39 quad_perm:[0,1,2,3] row_mask:0xf bank_mask:0x1\n\t"
    "v_mov_b32_dpp v17, v39 row_ror:12 row_mask:0xf bank_mask:0x1\n\t"
    "v_mov_b32_dpp v18, v39 row_ror:8 row_mask:0xf bank_mask:0x1\n\t"
    "v_mov_b32_dpp v19, v39 row_ror:4 row_mask:0xf bank_mask:0x1\n\t"
    "v_mov_b32_dpp v20, v39 row_ror:4 row_mask:0xf bank_mask:0x2\n\t"
    "v_mov_b32_dpp v21, v39 quad_perm:[0,1,2,3] row_mask:0xf bank_mask:0x2\n\t"
    "v_mov_b32_dpp v22, v39 row_ror:12 row_mask:0xf bank_mask:0x2\n\t"
    "v_mov_b32_dpp v23, v39 row_ror:8 row_mask:0xf bank_mask:0x2\n\t"
    "v_mov_b32_dpp v24, v39 row_ror:8 row_mask:0xf bank_mask:0x4\n\t"
    "v_mov_b32_dpp v25, v39 row_ror:4 row_mask:0xf bank_mask:0x4\n\t"
    "v_mov_b32_dpp v26, v39 quad_perm:[0,1,2,3] row_mask:0xf bank_mask:0x4\n\t"
    "v_mov_b32_dpp v27, v39 row_ror:12 row_mask:0xf bank_mask:0x4\n\t"
    "v_mov_b32_dpp v28, v39 row_ror:12 row_mask:0xf bank_mask:0x8\n\t"
    "v_mov_b32_dpp v29, v39 row_ror:8 row_mask:0xf bank_mask:0x8\n\t"
    "v_mov_b32_dpp v30, v39 row_ror:4 row_mask:0xf bank_mask:0x8\n\t"
    "v_mov_b32_dpp v31, v39 quad_perm:[0,1,2,3] row_mask:0xf bank_mask:0x8\n\t"
    "v_mfma_f32_16x16x32_bf16 v[44:47], a[216:219], v[4:7], 0\n\t"
    "v_mfma_f32_16x16x32_bf16 v[48:51], a[220:223], v[4:7], 0\n\t"
    "v_mfma_f32_16x16x32_bf16 v[52:55], a[224:227], v[4:7], 0\n\t"
    "s_nop 5\n\t"
    "v_add_f32_dpp v40, v45, v44 row_ror:12 row_mask:0xf bank_mask:0xf\n\t"
    "v_add_f32_dpp v42, v47, v46 row_ror:12 row_mask:0xf bank_mask:0xf\n\t"
    "v_mfma_f32_16x16x32_bf16 v[44:47], a[228:231], v[4:7], 0\n\t"
    "v_add_f32_dpp v41, v50, v49 row_ror:12 row_mask:0xf bank_mask:0xf\n\t"
    "v_add_f32_dpp v43, v48, v51 row_ror:12 row_mask:0xf bank_mask:0xf\n\t"
    "v_add_f32_dpp v35, v42, v40 row_ror:8 row_mask:0xf bank_mask:0x1\n\t"
    "v_mfma_f32_16x16x32_bf16 v[48:51], a[232:235], v[4:7], 0\n\t"
    "v_add_f32_dpp v40, v55, v54 row_ror:12 row_mask:0xf bank_mask:0xf\n\t"
    "v_add_f32_dpp v42, v53, v52 row_ror:12 row_mask:0xf bank_mask:0xf\n\t"
    "v_add_f32_dpp v35, v43, v41 row_ror:8 row_mask:0xf bank_mask:0x2\n\t"
    "v_mfma_f32_16x16x32_bf16 v[52:55], a[236:239], v[4:7], 0\n\t"
    "v_add_f32_dpp v41, v44, v47 row_ror:12 row_mask:0xf bank_mask:0xf\n\t"
    "v_add_f32_dpp v43, v46, v45 row_ror:12 row_mask:0xf bank_mask:0xf\n\t"
    "v_add_f32_dpp v35, v42, v40 row_ror:8 row_mask:0xf bank_mask:0x4\n\t"
    "v_mfma_f32_16x16x32_bf16 v[44:47], a[240:243], v[4:7], 0\n\t"
    "s_waitcnt vmcnt(27)\n\t"
    "s_add_u32 s57, s32, 0x4000\n\t"
    "s_xor_b32 s57, s57, s80\n\t"
    "buffer_load_dwordx4 a[136:139], v0, s[40:43], s57 offen offset:0 nt\n\t"
    "v_add_f32_dpp v40, v49, v48 row_ror:12 row_mask:0xf bank_mask:0xf\n\t"
    "v_add_f32_dpp v42, v51, v50 row_ror:12 row_mask:0xf bank_mask:0xf\n\t"
    "v_add_f32_dpp v35, v43, v41 row_ror:8 row_mask:0xf bank_mask:0x8\n\t"
    "v_mfma_f32_16x16x32_bf16 v[48:51], a[244:247], v[4:7], 0\n\t"
    "v_add_f32_dpp v41, v54, v53 row_ror:12 row_mask:0xf bank_mask:0xf\n\t"
    "v_add_f32_dpp v43, v52, v55 row_ror:12 row_mask:0xf bank_mask:0xf\n\t"
    "v_add_f32_dpp v36, v42, v40 row_ror:8 row_mask:0xf bank_mask:0x1\n\t"
    "v_add_f32_dpp v40, v47, v46 row_ror:12 row_mask:0xf bank_mask:0xf\n\t"
    "v_add_f32_dpp v42, v45, v44 row_ror:12 row_mask:0xf bank_mask:0xf\n\t"
    "v_add_f32_dpp v36, v43, v41 row_ror:8 row_mask:0xf bank_mask:0x2\n\t"
    "s_nop 1\n\t"
    "v_add_f32_dpp v41, v48, v51 row_ror:12 row_mask:0xf bank_mask:0xf\n\t"
    "v_add_f32_dpp v43, v50, v49 row_ror:12 row_mask:0xf bank_mask:0xf\n\t"
    "v_add_f32_dpp v36, v42, v40 row_ror:8 row_mask:0xf bank_mask:0x4\n\t"
    "s_nop 0\n\t"
    "v_add_f32_dpp v36, v43, v41 row_ror:8 row_mask:0xf bank_mask:0x8\n\t"
    "v_perm_b32 v44, v220, v160, s66\n\t"
    "v_perm_b32 v45, v216, v164, s66\n\t"
    "v_perm_b32 v46, v212, v168, s66\n\t"
    "v_perm_b32 v47, v208, v172, s66\n\t"
    "v_perm_b32 v48, v220, v160, s67\n\t"
    "v_perm_b32 v49, v216, v164, s67\n\t"
    "v_perm_b32 v50, v212, v168, s67\n\t"
    "v_perm_b32 v51, v208, v172, s67\n\t"
    "v_max_f32 v37, v35, v36\n\t"
    "v_mfma_f32_16x16x32_bf16 v[8:11], v[16:19], v[44:47], v[8:11]\n\t"
    "v_perm_b32 v52, v221, v161, s66\n\t"
    "v_perm_b32 v53, v217, v165, s66\n\t"
    "v_perm_b32 v54, v213, v169, s66\n\t"
    "v_perm_b32 v55, v209, v173, s66\n\t"
    "v_max_f32_dpp v37, v37, v37 row_ror:4 row_mask:0xf bank_mask:0xf\n\t"
    "v_mfma_f32_16x16x32_bf16 v[8:11], v[20:23], v[48:51], v[8:11]\n\t"
    "v_perm_b32 v44, v221, v161, s67\n\t"
    "v_perm_b32 v45, v217, v165, s67\n\t"
    "v_perm_b32 v46, v213, v169, s67\n\t"
    "v_perm_b32 v47, v209, v173, s67\n\t"
    "v_max_f32_dpp v37, v37, v37 row_ror:8 row_mask:0xf bank_mask:0xf\n\t"
    "v_mfma_f32_16x16x32_bf16 v[8:11], v[24:27], v[52:55], v[8:11]\n\t"
    "v_perm_b32 v48, v222, v162, s66\n\t"
    "v_perm_b32 v49, v218, v166, s66\n\t"
    "v_perm_b32 v50, v214, v170, s66\n\t"
    "v_perm_b32 v51, v210, v174, s66\n\t"
    "v_mov_b32 v38, v37\n\t"
    "v_mfma_f32_16x16x32_bf16 v[8:11], v[28:31], v[44:47], v[8:11]\n\t"
    "v_perm_b32 v52, v222, v162, s67\n\t"
    "v_perm_b32 v53, v218, v166, s67\n\t"
    "v_perm_b32 v54, v214, v170, s67\n\t"
    "v_perm_b32 v55, v210, v174, s67\n\t"
    "v_permlane16_swap_b32 v37, v38\n\t"
    "v_mfma_f32_16x16x32_bf16 v[12:15], v[16:19], v[48:51], v[12:15]\n\t"
    "v_perm_b32 v44, v223, v163, s66\n\t"
    "v_perm_b32 v45, v219, v167, s66\n\t"
    "v_perm_b32 v46, v215, v171, s66\n\t"
    "v_perm_b32 v47, v211, v175, s66\n\t"
    "v_max_f32 v37, v37, v38\n\t"
    "v_mfma_f32_16x16x32_bf16 v[12:15], v[20:23], v[52:55], v[12:15]\n\t"
    "s_waitcnt vmcnt(27)\n\t"
    "s_add_u32 s57, s32, 0x5000\n\t"
    "s_xor_b32 s57, s57, s80\n\t"
    "buffer_load_dwordx4 a[140:143], v0, s[40:43], s57 offen offset:0 nt\n\t"
    "v_perm_b32 v48, v223, v163, s67\n\t"
    "v_perm_b32 v49, v219, v167, s67\n\t"
    "v_perm_b32 v50, v215, v171, s67\n\t"
    "v_perm_b32 v51, v211, v175, s67\n\t"
    "v_mov_b32 v38, v37\n\t"
    "v_mfma_f32_16x16x32_bf16 v[12:15], v[24:27], v[44:47], v[12:15]\n\t"
    "s_nop 0\n\t"
    "v_permlane32_swap_b32 v37, v38\n\t"
    "v_mfma_f32_16x16x32_bf16 v[12:15], v[28:31], v[48:51], v[12:15]\n\t"
    "v_max_f32 v37, v37, v38\n\t"
    "v_cmp_gt_f32 vcc, v37, v32\n\t"
    "s_cbranch_vccz .Lr28_%=\n\t"
    "v_max_f32 v37, v37, v32\n\t"
    "v_sub_f32 v38, v32, v37\n\t"
    "v_mul_f32 v38, s64, v38\n\t"
    "v_exp_f32 v38, v38\n\t"
    "v_mov_b32 v32, v37\n\t"
    "v_mul_f32 v34, s65, v37\n\t"
    "v_mul_f32 v33, v33, v38\n\t"
    "v_mul_f32_dpp v8, v38, v8 quad_perm:[0,0,0,0] row_mask:0xf bank_mask:0xf\n\t"
    "v_mul_f32_dpp v9, v38, v9 quad_perm:[1,1,1,1] row_mask:0xf bank_mask:0xf\n\t"
    "v_mul_f32_dpp v10, v38, v10 quad_perm:[2,2,2,2] row_mask:0xf bank_mask:0xf\n\t"
    "v_mul_f32_dpp v11, v38, v11 quad_perm:[3,3,3,3] row_mask:0xf bank_mask:0xf\n\t"
    "v_mul_f32_dpp v12, v38, v12 quad_perm:[0,0,0,0] row_mask:0xf bank_mask:0xf\n\t"
    "v_mul_f32_dpp v13, v38, v13 quad_perm:[1,1,1,1] row_mask:0xf bank_mask:0xf\n\t"
    "v_mul_f32_dpp v14, v38, v14 quad_perm:[2,2,2,2] row_mask:0xf bank_mask:0xf\n\t"
    "v_mul_f32_dpp v15, v38, v15 quad_perm:[3,3,3,3] row_mask:0xf bank_mask:0xf\n\t"
    ".Lr28_%=:\n\t"
    "v_fma_f32 v35, v35, s64, v34\n\t"
    "v_fma_f32 v36, v36, s64, v34\n\t"
    "v_exp_f32 v35, v35\n\t"
    "v_exp_f32 v36, v36\n\t"
    "s_nop 0\n\t"
    "v_cvt_pk_bf16_f32 v39, v35, v36\n\t"
    "v_dot2_f32_bf16 v33, v39, s68, v33\n\t"
    "s_nop 0\n\t"
    "v_mov_b32_dpp v16, v39 quad_perm:[0,1,2,3] row_mask:0xf bank_mask:0x1\n\t"
    "v_mov_b32_dpp v17, v39 row_ror:12 row_mask:0xf bank_mask:0x1\n\t"
    "v_mov_b32_dpp v18, v39 row_ror:8 row_mask:0xf bank_mask:0x1\n\t"
    "v_mov_b32_dpp v19, v39 row_ror:4 row_mask:0xf bank_mask:0x1\n\t"
    "v_mov_b32_dpp v20, v39 row_ror:4 row_mask:0xf bank_mask:0x2\n\t"
    "v_mov_b32_dpp v21, v39 quad_perm:[0,1,2,3] row_mask:0xf bank_mask:0x2\n\t"
    "v_mov_b32_dpp v22, v39 row_ror:12 row_mask:0xf bank_mask:0x2\n\t"
    "v_mov_b32_dpp v23, v39 row_ror:8 row_mask:0xf bank_mask:0x2\n\t"
    "v_mov_b32_dpp v24, v39 row_ror:8 row_mask:0xf bank_mask:0x4\n\t"
    "v_mov_b32_dpp v25, v39 row_ror:4 row_mask:0xf bank_mask:0x4\n\t"
    "v_mov_b32_dpp v26, v39 quad_perm:[0,1,2,3] row_mask:0xf bank_mask:0x4\n\t"
    "v_mov_b32_dpp v27, v39 row_ror:12 row_mask:0xf bank_mask:0x4\n\t"
    "v_mov_b32_dpp v28, v39 row_ror:12 row_mask:0xf bank_mask:0x8\n\t"
    "v_mov_b32_dpp v29, v39 row_ror:8 row_mask:0xf bank_mask:0x8\n\t"
    "v_mov_b32_dpp v30, v39 row_ror:4 row_mask:0xf bank_mask:0x8\n\t"
    "v_mov_b32_dpp v31, v39 quad_perm:[0,1,2,3] row_mask:0xf bank_mask:0x8\n\t"
    "v_mfma_f32_16x16x32_bf16 v[44:47], a[0:3], v[4:7], 0\n\t"
    "v_mfma_f32_16x16x32_bf16 v[48:51], a[4:7], v[4:7], 0\n\t"
    "v_mfma_f32_16x16x32_bf16 v[52:55], a[8:11], v[4:7], 0\n\t"
    "s_nop 5\n\t"
    "v_add_f32_dpp v40, v45, v44 row_ror:12 row_mask:0xf bank_mask:0xf\n\t"
    "v_add_f32_dpp v42, v47, v46 row_ror:12 row_mask:0xf bank_mask:0xf\n\t"
    "v_mfma_f32_16x16x32_bf16 v[44:47], a[12:15], v[4:7], 0\n\t"
    "v_add_f32_dpp v41, v50, v49 row_ror:12 row_mask:0xf bank_mask:0xf\n\t"
    "v_add_f32_dpp v43, v48, v51 row_ror:12 row_mask:0xf bank_mask:0xf\n\t"
    "v_add_f32_dpp v35, v42, v40 row_ror:8 row_mask:0xf bank_mask:0x1\n\t"
    "v_mfma_f32_16x16x32_bf16 v[48:51], a[16:19], v[4:7], 0\n\t"
    "s_waitcnt vmcnt(27)\n\t"
    "s_add_u32 s57, s32, 0x6000\n\t"
    "s_xor_b32 s57, s57, s80\n\t"
    "buffer_load_dwordx4 a[144:147], v0, s[40:43], s57 offen offset:0 nt\n\t"
    "v_add_f32_dpp v40, v55, v54 row_ror:12 row_mask:0xf bank_mask:0xf\n\t"
    "v_add_f32_dpp v42, v53, v52 row_ror:12 row_mask:0xf bank_mask:0xf\n\t"
    "v_add_f32_dpp v35, v43, v41 row_ror:8 row_mask:0xf bank_mask:0x2\n\t"
    "v_mfma_f32_16x16x32_bf16 v[52:55], a[20:23], v[4:7], 0\n\t"
    "v_add_f32_dpp v41, v44, v47 row_ror:12 row_mask:0xf bank_mask:0xf\n\t"
    "v_add_f32_dpp v43, v46, v45 row_ror:12 row_mask:0xf bank_mask:0xf\n\t"
    "v_add_f32_dpp v35, v42, v40 row_ror:8 row_mask:0xf bank_mask:0x4\n\t"
    "v_mfma_f32_16x16x32_bf16 v[44:47], a[24:27], v[4:7], 0\n\t"
    "v_add_f32_dpp v40, v49, v48 row_ror:12 row_mask:0xf bank_mask:0xf\n\t"
    "v_add_f32_dpp v42, v51, v50 row_ror:12 row_mask:0xf bank_mask:0xf\n\t"
    "v_add_f32_dpp v35, v43, v41 row_ror:8 row_mask:0xf bank_mask:0x8\n\t"
    "v_mfma_f32_16x16x32_bf16 v[48:51], a[28:31], v[4:7], 0\n\t"
    "v_add_f32_dpp v41, v54, v53 row_ror:12 row_mask:0xf bank_mask:0xf\n\t"
    "v_add_f32_dpp v43, v52, v55 row_ror:12 row_mask:0xf bank_mask:0xf\n\t"
    "v_add_f32_dpp v36, v42, v40 row_ror:8 row_mask:0xf bank_mask:0x1\n\t"
    "s_nop 0\n\t"
    "v_add_f32_dpp v40, v47, v46 row_ror:12 row_mask:0xf bank_mask:0xf\n\t"
    "v_add_f32_dpp v42, v45, v44 row_ror:12 row_mask:0xf bank_mask:0xf\n\t"
    "v_add_f32_dpp v36, v43, v41 row_ror:8 row_mask:0xf bank_mask:0x2\n\t"
    "s_nop 0\n\t"
    "v_add_f32_dpp v41, v48, v51 row_ror:12 row_mask:0xf bank_mask:0xf\n\t"
    "v_add_f32_dpp v43, v50, v49 row_ror:12 row_mask:0xf bank_mask:0xf\n\t"
    "v_add_f32_dpp v36, v42, v40 row_ror:8 row_mask:0xf bank_mask:0x4\n\t"
    "s_nop 0\n\t"
    "v_add_f32_dpp v36, v43, v41 row_ror:8 row_mask:0xf bank_mask:0x8\n\t"
    "v_perm_b32 v44, v236, v252, s66\n\t"
    "v_perm_b32 v45, v232, v248, s66\n\t"
    "v_perm_b32 v46, v228, v244, s66\n\t"
    "v_perm_b32 v47, v224, v240, s66\n\t"
    "v_perm_b32 v48, v236, v252, s67\n\t"
    "v_perm_b32 v49, v232, v248, s67\n\t"
    "v_perm_b32 v50, v228, v244, s67\n\t"
    "v_perm_b32 v51, v224, v240, s67\n\t"
    "v_max_f32 v37, v35, v36\n\t"
    "v_mfma_f32_16x16x32_bf16 v[8:11], v[16:19], v[44:47], v[8:11]\n\t"
    "v_perm_b32 v52, v237, v253, s66\n\t"
    "v_perm_b32 v53, v233, v249, s66\n\t"
    "v_perm_b32 v54, v229, v245, s66\n\t"
    "v_perm_b32 v55, v225, v241, s66\n\t"
    "v_max_f32_dpp v37, v37, v37 row_ror:4 row_mask:0xf bank_mask:0xf\n\t"
    "v_mfma_f32_16x16x32_bf16 v[8:11], v[20:23], v[48:51], v[8:11]\n\t"
    "v_perm_b32 v44, v237, v253, s67\n\t"
    "v_perm_b32 v45, v233, v249, s67\n\t"
    "v_perm_b32 v46, v229, v245, s67\n\t"
    "v_perm_b32 v47, v225, v241, s67\n\t"
    "v_max_f32_dpp v37, v37, v37 row_ror:8 row_mask:0xf bank_mask:0xf\n\t"
    "v_mfma_f32_16x16x32_bf16 v[8:11], v[24:27], v[52:55], v[8:11]\n\t"
    "v_perm_b32 v48, v238, v254, s66\n\t"
    "v_perm_b32 v49, v234, v250, s66\n\t"
    "v_perm_b32 v50, v230, v246, s66\n\t"
    "v_perm_b32 v51, v226, v242, s66\n\t"
    "v_mov_b32 v38, v37\n\t"
    "v_mfma_f32_16x16x32_bf16 v[8:11], v[28:31], v[44:47], v[8:11]\n\t"
    "v_perm_b32 v52, v238, v254, s67\n\t"
    "v_perm_b32 v53, v234, v250, s67\n\t"
    "v_perm_b32 v54, v230, v246, s67\n\t"
    "v_perm_b32 v55, v226, v242, s67\n\t"
    "v_permlane16_swap_b32 v37, v38\n\t"
    "v_mfma_f32_16x16x32_bf16 v[12:15], v[16:19], v[48:51], v[12:15]\n\t"
    "s_waitcnt vmcnt(27)\n\t"
    "s_add_u32 s57, s32, 0x7000\n\t"
    "s_xor_b32 s57, s57, s80\n\t"
    "buffer_load_dwordx4 a[148:151], v0, s[40:43], s57 offen offset:0 nt\n\t"
    "s_add_u32 s33, s34, 0x68000\n\t"
    "s_and_b32 s33, s33, 0x7ffff\n\t"
    "s_add_u32 s33, s33, s56\n\t"
    "s_add_u32 s57, s33, 0\n\t"
    "s_xor_b32 s57, s57, s80\n\t"
    "buffer_load_dwordx4 v[88:91], v1, s[44:47], s57 offen offset:0 nt\n\t"
    "s_add_u32 s57, s33, 0x1000\n\t"
    "s_xor_b32 s57, s57, s80\n\t"
    "buffer_load_dwordx4 v[92:95], v1, s[44:47], s57 offen offset:0 nt\n\t"
    "s_add_u32 s57, s33, 0x2000\n\t"
    "s_xor_b32 s57, s57, s80\n\t"
    "buffer_load_dwordx4 v[96:99], v1, s[44:47], s57 offen offset:0 nt\n\t"
    "s_add_u32 s57, s33, 0x3000\n\t"
    "s_xor_b32 s57, s57, s80\n\t"
    "buffer_load_dwordx4 v[100:103], v1, s[44:47], s57 offen offset:0 nt\n\t"
    "s_add_u32 s57, s33, 0x4000\n\t"
    "s_xor_b32 s57, s57, s80\n\t"
    "buffer_load_dwordx4 v[104:107], v1, s[44:47], s57 offen offset:0 nt\n\t"
    "s_add_u32 s57, s33, 0x5000\n\t"
    "s_xor_b32 s57, s57, s80\n\t"
    "buffer_load_dwordx4 v[108:111], v1, s[44:47], s57 offen offset:0 nt\n\t"
    "s_add_u32 s57, s33, 0x6000\n\t"
    "s_xor_b32 s57, s57, s80\n\t"
    "buffer_load_dwordx4 v[112:115], v1, s[44:47], s57 offen offset:0 nt\n\t"
    "s_add_u32 s57, s33, 0x7000\n\t"
    "s_xor_b32 s57, s57, s80\n\t"
    "buffer_load_dwordx4 v[116:119], v1, s[44:47], s57 offen offset:0 nt\n\t"
    "s_add_u32 s32, s34, 0x60000\n\t"
    "s_and_b32 s32, s32, 0x7ffff\n\t"
    "s_add_u32 s32, s32, s56\n\t"
    "s_add_u32 s57, s32, 0\n\t"
    "s_xor_b32 s57, s57, s80\n\t"
    "buffer_load_dwordx4 a[152:155], v0, s[40:43], s57 offen offset:0 nt\n\t"
    "s_add_u32 s57, s32, 0x1000\n\t"
    "s_xor_b32 s57, s57, s80\n\t"
    "buffer_load_dwordx4 a[156:159], v0, s[40:43], s57 offen offset:0 nt\n\t"
    "s_add_u32 s57, s32, 0x2000\n\t"
    "s_xor_b32 s57, s57, s80\n\t"
    "buffer_load_dwordx4 a[160:163], v0, s[40:43], s57 offen offset:0 nt\n\t"
    "s_add_u32 s57, s32, 0x3000\n\t"
    "s_xor_b32 s57, s57, s80\n\t"
    "buffer_load_dwordx4 a[164:167], v0, s[40:43], s57 offen offset:0 nt\n\t"
    "s_add_u32 s57, s32, 0x4000\n\t"
    "s_xor_b32 s57, s57, s80\n\t"
    "buffer_load_dwordx4 a[168:171], v0, s[40:43], s57 offen offset:0 nt\n\t"
    "s_add_u32 s57, s32, 0x5000\n\t"
    "s_xor_b32 s57, s57, s80\n\t"
    "buffer_load_dwordx4 a[172:175], v0, s[40:43], s57 offen offset:0 nt\n\t"
    "s_add_u32 s57, s32, 0x6000\n\t"
    "s_xor_b32 s57, s57, s80\n\t"
    "buffer_load_dwordx4 a[176:179], v0, s[40:43], s57 offen offset:0 nt\n\t"
    "s_add_u32 s57, s32, 0x7000\n\t"
    "s_xor_b32 s57, s57, s80\n\t"
    "buffer_load_dwordx4 a[180:183], v0, s[40:43], s57 offen offset:0 nt\n\t"
    "s_waitcnt vmcnt(43)\n\t"
    "s_add_u32 s33, s34, 0x28000\n\t"
    "s_and_b32 s33, s33, 0x7ffff\n\t"
    "s_add_u32 s33, s33, s56\n\t"
    "s_add_u32 s57, s33, 0\n\t"
    "s_xor_b32 s57, s57, s80\n\t"
    "buffer_load_dwordx4 v[128:131], v1, s[44:47], s57 offen offset:0 nt\n\t"
    "v_perm_b32 v44, v239, v255, s66\n\t"
    "v_perm_b32 v45, v235, v251, s66\n\t"
    "v_perm_b32 v46, v231, v247, s66\n\t"
    "v_perm_b32 v47, v227, v243, s66\n\t"
    "v_max_f32 v37, v37, v38\n\t"
    "v_mfma_f32_16x16x32_bf16 v[12:15], v[20:23], v[52:55], v[12:15]\n\t"
    "v_perm_b32 v48, v239, v255, s67\n\t"
    "v_perm_b32 v49, v235, v251, s67\n\t"
    "v_perm_b32 v50, v231, v247, s67\n\t"
    "v_perm_b32 v51, v227, v243, s67\n\t"
    "v_mov_b32 v38, v37\n\t"
    "v_mfma_f32_16x16x32_bf16 v[12:15], v[24:27], v[44:47], v[12:15]\n\t"
    "s_nop 0\n\t"
    "v_permlane32_swap_b32 v37, v38\n\t"
    "v_mfma_f32_16x16x32_bf16 v[12:15], v[28:31], v[48:51], v[12:15]\n\t"
    "v_max_f32 v37, v37, v38\n\t"
    "ds_read_b128 a[184:187], v2\n\t"
    "ds_read_b128 a[188:191], v2 offset:1024\n\t"
    "ds_read_b128 a[192:195], v2 offset:2048\n\t"
    "ds_read_b128 a[196:199], v2 offset:3072\n\t"
    "ds_read_b128 a[200:203], v2 offset:4096\n\t"
    "ds_read_b128 a[204:207], v2 offset:5120\n\t"
    "ds_read_b128 a[208:211], v2 offset:6144\n\t"
    "ds_read_b128 a[212:215], v2 offset:7168\n\t"
    "v_cmp_gt_f32 vcc, v37, v32\n\t"
    "s_cbranch_vccz .Lr29_%=\n\t"
    "v_max_f32 v37, v37, v32\n\t"
    "v_sub_f32 v38, v32, v37\n\t"
    "v_mul_f32 v38, s64, v38\n\t"
    "v_exp_f32 v38, v38\n\t"
    "v_mov_b32 v32, v37\n\t"
    "v_mul_f32 v34, s65, v37\n\t"
    "v_mul_f32 v33, v33, v38\n\t"
    "v_mul_f32_dpp v8, v38, v8 quad_perm:[0,0,0,0] row_mask:0xf bank_mask:0xf\n\t"
    "v_mul_f32_dpp v9, v38, v9 quad_perm:[1,1,1,1] row_mask:0xf bank_mask:0xf\n\t"
    "v_mul_f32_dpp v10, v38, v10 quad_perm:[2,2,2,2] row_mask:0xf bank_mask:0xf\n\t"
    "v_mul_f32_dpp v11, v38, v11 quad_perm:[3,3,3,3] row_mask:0xf bank_mask:0xf\n\t"
    "v_mul_f32_dpp v12, v38, v12 quad_perm:[0,0,0,0] row_mask:0xf bank_mask:0xf\n\t"
    "v_mul_f32_dpp v13, v38, v13 quad_perm:[1,1,1,1] row_mask:0xf bank_mask:0xf\n\t"
    "v_mul_f32_dpp v14, v38, v14 quad_perm:[2,2,2,2] row_mask:0xf bank_mask:0xf\n\t"
    "v_mul_f32_dpp v15, v38, v15 quad_perm:[3,3,3,3] row_mask:0xf bank_mask:0xf\n\t"
    ".Lr29_%=:\n\t"
    "v_fma_f32 v35, v35, s64, v34\n\t"
    "v_fma_f32 v36, v36, s64, v34\n\t"
    "v_exp_f32 v35, v35\n\t"
    "v_exp_f32 v36, v36\n\t"
    "s_nop 0\n\t"
    "v_cvt_pk_bf16_f32 v39, v35, v36\n\t"
    "v_dot2_f32_bf16 v33, v39, s68, v33\n\t"
    "s_waitcnt vmcnt(43)\n\t"
    "s_add_u32 s57, s33, 0x1000\n\t"
    "s_xor_b32 s57, s57, s80\n\t"
    "buffer_load_dwordx4 v[132:135], v1, s[44:47], s57 offen offset:0 nt\n\t"
    "v_mov_b32_dpp v16, v39 quad_perm:[0,1,2,3] row_mask:0xf bank_mask:0x1\n\t"
    "v_mov_b32_dpp v17, v39 row_ror:12 row_mask:0xf bank_mask:0x1\n\t"
    "v_mov_b32_dpp v18, v39 row_ror:8 row_mask:0xf bank_mask:0x1\n\t"
    "v_mov_b32_dpp v19, v39 row_ror:4 row_mask:0xf bank_mask:0x1\n\t"
    "v_mov_b32_dpp v20, v39 row_ror:4 row_mask:0xf bank_mask:0x2\n\t"
    "v_mov_b32_dpp v21, v39 quad_perm:[0,1,2,3] row_mask:0xf bank_mask:0x2\n\t"
    "v_mov_b32_dpp v22, v39 row_ror:12 row_mask:0xf bank_mask:0x2\n\t"
    "v_mov_b32_dpp v23, v39 row_ror:8 row_mask:0xf bank_mask:0x2\n\t"
    "v_mov_b32_dpp v24, v39 row_ror:8 row_mask:0xf bank_mask:0x4\n\t"
    "v_mov_b32_dpp v25, v39 row_ror:4 row_mask:0xf bank_mask:0x4\n\t"
    "v_mov_b32_dpp v26, v39 quad_perm:[0,1,2,3] row_mask:0xf bank_mask:0x4\n\t"
    "v_mov_b32_dpp v27, v39 row_ror:12 row_mask:0xf bank_mask:0x4\n\t"
    "v_mov_b32_dpp v28, v39 row_ror:12 row_mask:0xf bank_mask:0x8\n\t"
    "v_mov_b32_dpp v29, v39 row_ror:8 row_mask:0xf bank_mask:0x8\n\t"
    "v_mov_b32_dpp v30, v39 row_ror:4 row_mask:0xf bank_mask:0x8\n\t"
    "v_mov_b32_dpp v31, v39 quad_perm:[0,1,2,3] row_mask:0xf bank_mask:0x8\n\t"
    "v_mfma_f32_16x16x32_bf16 v[44:47], a[32:35], v[4:7], 0\n\t"
    "v_mfma_f32_16x16x32_bf16 v[48:51], a[36:39], v[4:7], 0\n\t"
    "v_mfma_f32_16x16x32_bf16 v[52:55], a[40:43], v[4:7], 0\n\t"
    "s_nop 5\n\t"
    "v_add_f32_dpp v40, v45, v44 row_ror:12 row_mask:0xf bank_mask:0xf\n\t"
    "v_add_f32_dpp v42, v47, v46 row_ror:12 row_mask:0xf bank_mask:0xf\n\t"
    "v_mfma_f32_16x16x32_bf16 v[44:47], a[44:47], v[4:7], 0\n\t"
    "v_add_f32_dpp v41, v50, v49 row_ror:12 row_mask:0xf bank_mask:0xf\n\t"
    "v_add_f32_dpp v43, v48, v51 row_ror:12 row_mask:0xf bank_mask:0xf\n\t"
    "v_add_f32_dpp v35, v42, v40 row_ror:8 row_mask:0xf bank_mask:0x1\n\t"
    "v_mfma_f32_16x16x32_bf16 v[48:51], a[48:51], v[4:7], 0\n\t"
    "v_add_f32_dpp v40, v55, v54 row_ror:12 row_mask:0xf bank_mask:0xf\n\t"
    "v_add_f32_dpp v42, v53, v52 row_ror:12 row_mask:0xf bank_mask:0xf\n\t"
    "v_add_f32_dpp v35, v43, v41 row_ror:8 row_mask:0xf bank_mask:0x2\n\t"
    "v_mfma_f32_16x16x32_bf16 v[52:55], a[52:55], v[4:7], 0\n\t"
    "v_add_f32_dpp v41, v44, v47 row_ror:12 row_mask:0xf bank_mask:0xf\n\t"
    "v_add_f32_dpp v43, v46, v45 row_ror:12 row_mask:0xf bank_mask:0xf\n\t"
    "v_add_f32_dpp v35, v42, v40 row_ror:8 row_mask:0xf bank_mask:0x4\n\t"
    "v_mfma_f32_16x16x32_bf16 v[44:47], a[56:59], v[4:7], 0\n\t"
    "v_add_f32_dpp v40, v49, v48 row_ror:12 row_mask:0xf bank_mask:0xf\n\t"
    "v_add_f32_dpp v42, v51, v50 row_ror:12 row_mask:0xf bank_mask:0xf\n\t"
    "v_add_f32_dpp v35, v43, v41 row_ror:8 row_mask:0xf bank_mask:0x8\n\t"
    "v_mfma_f32_16x16x32_bf16 v[48:51], a[60:63], v[4:7], 0\n\t"
    "v_add_f32_dpp v41, v54, v53 row_ror:12 row_mask:0xf bank_mask:0xf\n\t"
    "v_add_f32_dpp v43, v52, v55 row_ror:12 row_mask:0xf bank_mask:0xf\n\t"
    "v_add_f32_dpp v36, v42, v40 row_ror:8 row_mask:0xf bank_mask:0x1\n\t"
    "s_nop 0\n\t"
    "v_add_f32_dpp v40, v47, v46 row_ror:12 row_mask:0xf bank_mask:0xf\n\t"
    "v_add_f32_dpp v42, v45, v44 row_ror:12 row_mask:0xf bank_mask:0xf\n\t"
    "v_add_f32_dpp v36, v43, v41 row_ror:8 row_mask:0xf bank_mask:0x2\n\t"
    "s_nop 0\n\t"
    "v_add_f32_dpp v41, v48, v51 row_ror:12 row_mask:0xf bank_mask:0xf\n\t"
    "v_add_f32_dpp v43, v50, v49 row_ror:12 row_mask:0xf bank_mask:0xf\n\t"
    "v_add_f32_dpp v36, v42, v40 row_ror:8 row_mask:0xf bank_mask:0x4\n\t"
    "s_nop 0\n\t"
    "v_add_f32_dpp v36, v43, v41 row_ror:8 row_mask:0xf bank_mask:0x8\n\t"
    "s_waitcnt lgkmcnt(7)\n\t"
    "v_mfma_f32_16x16x32_bf16 v[8:11], v[16:19], a[184:187], v[8:11]\n\t"
    "v_max_f32 v37, v35, v36\n\t"
    "s_waitcnt vmcnt(43)\n\t"
    "s_add_u32 s57, s33, 0x2000\n\t"
    "s_xor_b32 s57, s57, s80\n\t"
    "buffer_load_dwordx4 v[136:139], v1, s[44:47], s57 offen offset:0 nt\n\t"
    "s_waitcnt lgkmcnt(6)\n\t"
    "v_mfma_f32_16x16x32_bf16 v[8:11], v[20:23], a[188:191], v[8:11]\n\t"
    "v_max_f32_dpp v37, v37, v37 row_ror:4 row_mask:0xf bank_mask:0xf\n\t"
    "s_waitcnt lgkmcnt(5)\n\t"
    "v_mfma_f32_16x16x32_bf16 v[8:11], v[24:27], a[192:195], v[8:11]\n\t"
    "v_max_f32_dpp v37, v37, v37 row_ror:8 row_mask:0xf bank_mask:0xf\n\t"
    "s_waitcnt lgkmcnt(4)\n\t"
    "v_mfma_f32_16x16x32_bf16 v[8:11], v[28:31], a[196:199], v[8:11]\n\t"
    "v_mov_b32 v38, v37\n\t"
    "s_waitcnt lgkmcnt(3)\n\t"
    "v_mfma_f32_16x16x32_bf16 v[12:15], v[16:19], a[200:203], v[12:15]\n\t"
    "v_permlane16_swap_b32 v37, v38\n\t"
    "s_waitcnt lgkmcnt(2)\n\t"
    "v_mfma_f32_16x16x32_bf16 v[12:15], v[20:23], a[204:207], v[12:15]\n\t"
    "v_max_f32 v37, v37, v38\n\t"
    "s_waitcnt lgkmcnt(1)\n\t"
    "v_mfma_f32_16x16x32_bf16 v[12:15], v[24:27], a[208:211], v[12:15]\n\t"
    "v_mov_b32 v38, v37\n\t"
    "s_waitcnt lgkmcnt(0)\n\t"
    "v_mfma_f32_16x16x32_bf16 v[12:15], v[28:31], a[212:215], v[12:15]\n\t"
    "v_permlane32_swap_b32 v37, v38\n\t"
    "v_max_f32 v37, v37, v38\n\t"
    "ds_read_b128 a[216:219], v2 offset:16384\n\t"
    "ds_read_b128 a[220:223], v2 offset:17408\n\t"
    "ds_read_b128 a[224:227], v2 offset:18432\n\t"
    "ds_read_b128 a[228:231], v2 offset:19456\n\t"
    "ds_read_b128 a[232:235], v2 offset:20480\n\t"
    "ds_read_b128 a[236:239], v2 offset:21504\n\t"
    "ds_read_b128 a[240:243], v2 offset:22528\n\t"
    "ds_read_b128 a[244:247], v2 offset:23552\n\t"
    "v_cmp_gt_f32 vcc, v37, v32\n\t"
    "s_cbranch_vccz .Lr30_%=\n\t"
    "v_max_f32 v37, v37, v32\n\t"
    "v_sub_f32 v38, v32, v37\n\t"
    "v_mul_f32 v38, s64, v38\n\t"
    "v_exp_f32 v38, v38\n\t"
    "v_mov_b32 v32, v37\n\t"
    "v_mul_f32 v34, s65, v37\n\t"
    "v_mul_f32 v33, v33, v38\n\t"
    "v_mul_f32_dpp v8, v38, v8 quad_perm:[0,0,0,0] row_mask:0xf bank_mask:0xf\n\t"
    "v_mul_f32_dpp v9, v38, v9 quad_perm:[1,1,1,1] row_mask:0xf bank_mask:0xf\n\t"
    "v_mul_f32_dpp v10, v38, v10 quad_perm:[2,2,2,2] row_mask:0xf bank_mask:0xf\n\t"
    "v_mul_f32_dpp v11, v38, v11 quad_perm:[3,3,3,3] row_mask:0xf bank_mask:0xf\n\t"
    "v_mul_f32_dpp v12, v38, v12 quad_perm:[0,0,0,0] row_mask:0xf bank_mask:0xf\n\t"
    "v_mul_f32_dpp v13, v38, v13 quad_perm:[1,1,1,1] row_mask:0xf bank_mask:0xf\n\t"
    "v_mul_f32_dpp v14, v38, v14 quad_perm:[2,2,2,2] row_mask:0xf bank_mask:0xf\n\t"
    "v_mul_f32_dpp v15, v38, v15 quad_perm:[3,3,3,3] row_mask:0xf bank_mask:0xf\n\t"
    ".Lr30_%=:\n\t"
    "v_fma_f32 v35, v35, s64, v34\n\t"
    "v_fma_f32 v36, v36, s64, v34\n\t"
    "v_exp_f32 v35, v35\n\t"
    "v_exp_f32 v36, v36\n\t"
    "s_nop 0\n\t"
    "v_cvt_pk_bf16_f32 v39, v35, v36\n\t"
    "v_dot2_f32_bf16 v33, v39, s68, v33\n\t"
    "s_waitcnt vmcnt(43)\n\t"
    "s_add_u32 s57, s33, 0x3000\n\t"
    "s_xor_b32 s57, s57, s80\n\t"
    "buffer_load_dwordx4 v[140:143], v1, s[44:47], s57 offen offset:0 nt\n\t"
    "v_mov_b32_dpp v16, v39 quad_perm:[0,1,2,3] row_mask:0xf bank_mask:0x1\n\t"
    "v_mov_b32_dpp v17, v39 row_ror:12 row_mask:0xf bank_mask:0x1\n\t"
    "v_mov_b32_dpp v18, v39 row_ror:8 row_mask:0xf bank_mask:0x1\n\t"
    "v_mov_b32_dpp v19, v39 row_ror:4 row_mask:0xf bank_mask:0x1\n\t"
    "v_mov_b32_dpp v20, v39 row_ror:4 row_mask:0xf bank_mask:0x2\n\t"
    "v_mov_b32_dpp v21, v39 quad_perm:[0,1,2,3] row_mask:0xf bank_mask:0x2\n\t"
    "v_mov_b32_dpp v22, v39 row_ror:12 row_mask:0xf bank_mask:0x2\n\t"
    "v_mov_b32_dpp v23, v39 row_ror:8 row_mask:0xf bank_mask:0x2\n\t"
    "v_mov_b32_dpp v24, v39 row_ror:8 row_mask:0xf bank_mask:0x4\n\t"
    "v_mov_b32_dpp v25, v39 row_ror:4 row_mask:0xf bank_mask:0x4\n\t"
    "v_mov_b32_dpp v26, v39 quad_perm:[0,1,2,3] row_mask:0xf bank_mask:0x4\n\t"
    "v_mov_b32_dpp v27, v39 row_ror:12 row_mask:0xf bank_mask:0x4\n\t"
    "v_mov_b32_dpp v28, v39 row_ror:12 row_mask:0xf bank_mask:0x8\n\t"
    "v_mov_b32_dpp v29, v39 row_ror:8 row_mask:0xf bank_mask:0x8\n\t"
    "v_mov_b32_dpp v30, v39 row_ror:4 row_mask:0xf bank_mask:0x8\n\t"
    "v_mov_b32_dpp v31, v39 quad_perm:[0,1,2,3] row_mask:0xf bank_mask:0x8\n\t"
    "ds_read_b128 a[0:3], v2 offset:24576\n\t"
    "ds_read_b128 a[4:7], v2 offset:25600\n\t"
    "s_lshl_b32 s72, s60, 10\n\t"
    "v_add_u32 v37, s72, v1\n\t"
    "ds_read_b128 a[8:11], v37\n\t"
    "ds_read_b128 a[12:15], v37 offset:4096\n\t"
    "ds_read_b128 a[16:19], v37 offset:8192\n\t"
    "ds_read_b128 a[20:23], v37 offset:12288\n\t"
    "s_waitcnt lgkmcnt(5)\n\t"
    "v_mfma_f32_16x16x32_bf16 v[44:47], a[0:3], v[4:7], 0\n\t"
    "s_waitcnt lgkmcnt(4)\n\t"
    "v_mfma_f32_16x16x32_bf16 v[48:51], a[4:7], v[4:7], 0\n\t"
    "v_mfma_f32_16x16x32_bf16 v[52:55], v[120:123], v[4:7], 0\n\t"
    "s_waitcnt vmcnt(35)\n\t"
    "s_add_u32 s57, s33, 0x4000\n\t"
    "s_xor_b32 s57, s57, s80\n\t"
    "buffer_load_dwordx4 v[144:147], v1, s[44:47], s57 offen offset:0 nt\n\t"
    "s_nop 0\n\t"
    "v_add_f32_dpp v40, v45, v44 row_ror:12 row_mask:0xf bank_mask:0xf\n\t"
    "v_add_f32_dpp v42, v47, v46 row_ror:12 row_mask:0xf bank_mask:0xf\n\t"
    "v_mfma_f32_16x16x32_bf16 v[44:47], v[124:127], v[4:7], 0\n\t"
    "v_add_f32_dpp v41, v50, v49 row_ror:12 row_mask:0xf bank_mask:0xf\n\t"
    "v_add_f32_dpp v43, v48, v51 row_ror:12 row_mask:0xf bank_mask:0xf\n\t"
    "v_add_f32_dpp v35, v42, v40 row_ror:8 row_mask:0xf bank_mask:0x1\n\t"
    "s_waitcnt lgkmcnt(3)\n\t"
    "v_mfma_f32_16x16x32_bf16 v[48:51], a[8:11], v[4:7], 0\n\t"
    "v_add_f32_dpp v40, v55, v54 row_ror:12 row_mask:0xf bank_mask:0xf\n\t"
    "v_add_f32_dpp v42, v53, v52 row_ror:12 row_mask:0xf bank_mask:0xf\n\t"
    "v_add_f32_dpp v35, v43, v41 row_ror:8 row_mask:0xf bank_mask:0x2\n\t"
    "s_waitcnt lgkmcnt(2)\n\t"
    "v_mfma_f32_16x16x32_bf16 v[52:55], a[12:15], v[4:7], 0\n\t"
    "v_add_f32_dpp v41, v44, v47 row_ror:12 row_mask:0xf bank_mask:0xf\n\t"
    "v_add_f32_dpp v43, v46, v45 row_ror:12 row_mask:0xf bank_mask:0xf\n\t"
    "v_add_f32_dpp v35, v42, v40 row_ror:8 row_mask:0xf bank_mask:0x4\n\t"
    "s_waitcnt lgkmcnt(1)\n\t"
    "v_mfma_f32_16x16x32_bf16 v[44:47], a[16:19], v[4:7], 0\n\t"
    "v_add_f32_dpp v40, v49, v48 row_ror:12 row_mask:0xf bank_mask:0xf\n\t"
    "v_add_f32_dpp v42, v51, v50 row_ror:12 row_mask:0xf bank_mask:0xf\n\t"
    "v_add_f32_dpp v35, v43, v41 row_ror:8 row_mask:0xf bank_mask:0x8\n\t"
    "s_waitcnt lgkmcnt(0)\n\t"
    "v_mfma_f32_16x16x32_bf16 v[48:51], a[20:23], v[4:7], 0\n\t"
    "v_add_f32_dpp v41, v54, v53 row_ror:12 row_mask:0xf bank_mask:0xf\n\t"
    "v_add_f32_dpp v43, v52, v55 row_ror:12 row_mask:0xf bank_mask:0xf\n\t"
    "v_add_f32_dpp v36, v42, v40 row_ror:8 row_mask:0xf bank_mask:0x1\n\t"
    "v_add_f32_dpp v40, v47, v46 row_ror:12 row_mask:0xf bank_mask:0xf\n\t"
    "v_add_f32_dpp v42, v45, v44 row_ror:12 row_mask:0xf bank_mask:0xf\n\t"
    "v_add_f32_dpp v36, v43, v41 row_ror:8 row_mask:0xf bank_mask:0x2\n\t"
    "s_nop 1\n\t"
    "v_add_f32_dpp v41, v48, v51 row_ror:12 row_mask:0xf bank_mask:0xf\n\t"
    "v_add_f32_dpp v43, v50, v49 row_ror:12 row_mask:0xf bank_mask:0xf\n\t"
    "v_add_f32_dpp v36, v42, v40 row_ror:8 row_mask:0xf bank_mask:0x4\n\t"
    "s_nop 0\n\t"
    "v_add_f32_dpp v36, v43, v41 row_ror:8 row_mask:0xf bank_mask:0x8\n\t"
    "v_mfma_f32_16x16x32_bf16 v[8:11], v[16:19], a[216:219], v[8:11]\n\t"
    "v_max_f32 v37, v35, v36\n\t"
    "v_mfma_f32_16x16x32_bf16 v[8:11], v[20:23], a[220:223], v[8:11]\n\t"
    "s_nop 0\n\t"
    "v_max_f32_dpp v37, v37, v37 row_ror:4 row_mask:0xf bank_mask:0xf\n\t"
    "v_mfma_f32_16x16x32_bf16 v[8:11], v[24:27], a[224:227], v[8:11]\n\t"
    "s_nop 0\n\t"
    "v_max_f32_dpp v37, v37, v37 row_ror:8 row_mask:0xf bank_mask:0xf\n\t"
    "v_mfma_f32_16x16x32_bf16 v[8:11], v[28:31], a[228:231], v[8:11]\n\t"
    "v_mov_b32 v38, v37\n\t"
    "v_mfma_f32_16x16x32_bf16 v[12:15], v[16:19], a[232:235], v[12:15]\n\t"
    "s_nop 0\n\t"
    "v_permlane16_swap_b32 v37, v38\n\t"
    "v_mfma_f32_16x16x32_bf16 v[12:15], v[20:23], a[236:239], v[12:15]\n\t"
    "v_max_f32 v37, v37, v38\n\t"
    "s_waitcnt vmcnt(35)\n\t"
    "s_add_u32 s57, s33, 0x5000\n\t"
    "s_xor_b32 s57, s57, s80\n\t"
    "buffer_load_dwordx4 v[148:151], v1, s[44:47], s57 offen offset:0 nt\n\t"
    "v_mfma_f32_16x16x32_bf16 v[12:15], v[24:27], a[240:243], v[12:15]\n\t"
    "v_mov_b32 v38, v37\n\t"
    "v_mfma_f32_16x16x32_bf16 v[12:15], v[28:31], a[244:247], v[12:15]\n\t"
    "s_nop 0\n\t"
    "v_permlane32_swap_b32 v37, v38\n\t"
    "v_max_f32 v37, v37, v38\n\t"
    "v_cmp_gt_f32 vcc, v37, v32\n\t"
    "s_cbranch_vccz .Lr31_%=\n\t"
    "v_max_f32 v37, v37, v32\n\t"
    "v_sub_f32 v38, v32, v37\n\t"
    "v_mul_f32 v38, s64, v38\n\t"
    "v_exp_f32 v38, v38\n\t"
    "v_mov_b32 v32, v37\n\t"
    "v_mul_f32 v34, s65, v37\n\t"
    "v_mul_f32 v33, v33, v38\n\t"
    "v_mul_f32_dpp v8, v38, v8 quad_perm:[0,0,0,0] row_mask:0xf bank_mask:0xf\n\t"
    "v_mul_f32_dpp v9, v38, v9 quad_perm:[1,1,1,1] row_mask:0xf bank_mask:0xf\n\t"
    "v_mul_f32_dpp v10, v38, v10 quad_perm:[2,2,2,2] row_mask:0xf bank_mask:0xf\n\t"
    "v_mul_f32_dpp v11, v38, v11 quad_perm:[3,3,3,3] row_mask:0xf bank_mask:0xf\n\t"
    "v_mul_f32_dpp v12, v38, v12 quad_perm:[0,0,0,0] row_mask:0xf bank_mask:0xf\n\t"
    "v_mul_f32_dpp v13, v38, v13 quad_perm:[1,1,1,1] row_mask:0xf bank_mask:0xf\n\t"
    "v_mul_f32_dpp v14, v38, v14 quad_perm:[2,2,2,2] row_mask:0xf bank_mask:0xf\n\t"
    "v_mul_f32_dpp v15, v38, v15 quad_perm:[3,3,3,3] row_mask:0xf bank_mask:0xf\n\t"
    ".Lr31_%=:\n\t"
    "v_fma_f32 v35, v35, s64, v34\n\t"
    "v_fma_f32 v36, v36, s64, v34\n\t"
    "v_exp_f32 v35, v35\n\t"
    "v_exp_f32 v36, v36\n\t"
    "s_nop 0\n\t"
    "v_cvt_pk_bf16_f32 v39, v35, v36\n\t"
    "v_dot2_f32_bf16 v33, v39, s68, v33\n\t"
    "s_nop 0\n\t"
    "v_mov_b32_dpp v16, v39 quad_perm:[0,1,2,3] row_mask:0xf bank_mask:0x1\n\t"
    "v_mov_b32_dpp v17, v39 row_ror:12 row_mask:0xf bank_mask:0x1\n\t"
    "v_mov_b32_dpp v18, v39 row_ror:8 row_mask:0xf bank_mask:0x1\n\t"
    "v_mov_b32_dpp v19, v39 row_ror:4 row_mask:0xf bank_mask:0x1\n\t"
    "v_mov_b32_dpp v20, v39 row_ror:4 row_mask:0xf bank_mask:0x2\n\t"
    "v_mov_b32_dpp v21, v39 quad_perm:[0,1,2,3] row_mask:0xf bank_mask:0x2\n\t"
    "v_mov_b32_dpp v22, v39 row_ror:12 row_mask:0xf bank_mask:0x2\n\t"
    "v_mov_b32_dpp v23, v39 row_ror:8 row_mask:0xf bank_mask:0x2\n\t"
    "v_mov_b32_dpp v24, v39 row_ror:8 row_mask:0xf bank_mask:0x4\n\t"
    "v_mov_b32_dpp v25, v39 row_ror:4 row_mask:0xf bank_mask:0x4\n\t"
    "v_mov_b32_dpp v26, v39 quad_perm:[0,1,2,3] row_mask:0xf bank_mask:0x4\n\t"
    "v_mov_b32_dpp v27, v39 row_ror:12 row_mask:0xf bank_mask:0x4\n\t"
    "v_mov_b32_dpp v28, v39 row_ror:12 row_mask:0xf bank_mask:0x8\n\t"
    "v_mov_b32_dpp v29, v39 row_ror:8 row_mask:0xf bank_mask:0x8\n\t"
    "v_mov_b32_dpp v30, v39 row_ror:4 row_mask:0xf bank_mask:0x8\n\t"
    "v_mov_b32_dpp v31, v39 quad_perm:[0,1,2,3] row_mask:0xf bank_mask:0x8\n\t"
    "v_mfma_f32_16x16x32_bf16 v[44:47], a[248:251], v[4:7], 0\n\t"
    "v_mfma_f32_16x16x32_bf16 v[48:51], a[252:255], v[4:7], 0\n\t"
    "v_mfma_f32_16x16x32_bf16 v[52:55], a[64:67], v[4:7], 0\n\t"
    "s_nop 5\n\t"
    "v_add_f32_dpp v40, v45, v44 row_ror:12 row_mask:0xf bank_mask:0xf\n\t"
    "v_add_f32_dpp v42, v47, v46 row_ror:12 row_mask:0xf bank_mask:0xf\n\t"
    "v_mfma_f32_16x16x32_bf16 v[44:47], a[68:71], v[4:7], 0\n\t"
    "v_add_f32_dpp v41, v50, v49 row_ror:12 row_mask:0xf bank_mask:0xf\n\t"
    "v_add_f32_dpp v43, v48, v51 row_ror:12 row_mask:0xf bank_mask:0xf\n\t"
    "v_add_f32_dpp v35, v42, v40 row_ror:8 row_mask:0xf bank_mask:0x1\n\t"
    "v_mfma_f32_16x16x32_bf16 v[48:51], a[72:75], v[4:7], 0\n\t"
    "s_waitcnt vmcnt(35)\n\t"
    "s_add_u32 s57, s33, 0x6000\n\t"
    "s_xor_b32 s57, s57, s80\n\t"
    "buffer_load_dwordx4 v[152:155], v1, s[44:47], s57 offen offset:0 nt\n\t"
    "v_add_f32_dpp v40, v55, v54 row_ror:12 row_mask:0xf bank_mask:0xf\n\t"
    "v_add_f32_dpp v42, v53, v52 row_ror:12 row_mask:0xf bank_mask:0xf\n\t"
    "v_add_f32_dpp v35, v43, v41 row_ror:8 row_mask:0xf bank_mask:0x2\n\t"
    "v_mfma_f32_16x16x32_bf16 v[52:55], a[76:79], v[4:7], 0\n\t"
    "v_add_f32_dpp v41, v44, v47 row_ror:12 row_mask:0xf bank_mask:0xf\n\t"
    "v_add_f32_dpp v43, v46, v45 row_ror:12 row_mask:0xf bank_mask:0xf\n\t"
    "v_add_f32_dpp v35, v42, v40 row_ror:8 row_mask:0xf bank_mask:0x4\n\t"
    "v_mfma_f32_16x16x32_bf16 v[44:47], a[80:83], v[4:7], 0\n\t"
    "v_add_f32_dpp v40, v49, v48 row_ror:12 row_mask:0xf bank_mask:0xf\n\t"
    "v_add_f32_dpp v42, v51, v50 row_ror:12 row_mask:0xf bank_mask:0xf\n\t"
    "v_add_f32_dpp v35, v43, v41 row_ror:8 row_mask:0xf bank_mask:0x8\n\t"
    "v_mfma_f32_16x16x32_bf16 v[48:51], a[84:87], v[4:7], 0\n\t"
    "v_add_f32_dpp v41, v54, v53 row_ror:12 row_mask:0xf bank_mask:0xf\n\t"
    "v_add_f32_dpp v43, v52, v55 row_ror:12 row_mask:0xf bank_mask:0xf\n\t"
    "v_add_f32_dpp v36, v42, v40 row_ror:8 row_mask:0xf bank_mask:0x1\n\t"
    "s_nop 0\n\t"
    "v_add_f32_dpp v40, v47, v46 row_ror:12 row_mask:0xf bank_mask:0xf\n\t"
    "v_add_f32_dpp v42, v45, v44 row_ror:12 row_mask:0xf bank_mask:0xf\n\t"
    "v_add_f32_dpp v36, v43, v41 row_ror:8 row_mask:0xf bank_mask:0x2\n\t"
    "s_nop 0\n\t"
    "v_add_f32_dpp v41, v48, v51 row_ror:12 row_mask:0xf bank_mask:0xf\n\t"
    "v_add_f32_dpp v43, v50, v49 row_ror:12 row_mask:0xf bank_mask:0xf\n\t"
    "v_add_f32_dpp v36, v42, v40 row_ror:8 row_mask:0xf bank_mask:0x4\n\t"
    "s_nop 0\n\t"
    "v_add_f32_dpp v36, v43, v41 row_ror:8 row_mask:0xf bank_mask:0x8\n\t"
    "v_perm_b32 v44, v192, v176, s66\n\t"
    "v_perm_b32 v45, v196, v180, s66\n\t"
    "v_perm_b32 v46, v200, v184, s66\n\t"
    "v_perm_b32 v47, v204, v188, s66\n\t"
    "v_perm_b32 v48, v192, v176, s67\n\t"
    "v_perm_b32 v49, v196, v180, s67\n\t"
    "v_perm_b32 v50, v200, v184, s67\n\t"
    "v_perm_b32 v51, v204, v188, s67\n\t"
    "v_max_f32 v37, v35, v36\n\t"
    "v_mfma_f32_16x16x32_bf16 v[8:11], v[16:19], v[44:47], v[8:11]\n\t"
    "v_perm_b32 v52, v193, v177, s66\n\t"
    "v_perm_b32 v53, v197, v181, s66\n\t"
    "v_perm_b32 v54, v201, v185, s66\n\t"
    "v_perm_b32 v55, v205, v189, s66\n\t"
    "v_max_f32_dpp v37, v37, v37 row_ror:4 row_mask:0xf bank_mask:0xf\n\t"
    "v_mfma_f32_16x16x32_bf16 v[8:11], v[20:23], v[48:51], v[8:11]\n\t"
    "v_perm_b32 v44, v193, v177, s67\n\t"
    "v_perm_b32 v45, v197, v181, s67\n\t"
    "v_perm_b32 v46, v201, v185, s67\n\t"
    "v_perm_b32 v47, v205, v189, s67\n\t"
    "v_max_f32_dpp v37, v37, v37 row_ror:8 row_mask:0xf bank_mask:0xf\n\t"
    "v_mfma_f32_16x16x32_bf16 v[8:11], v[24:27], v[52:55], v[8:11]\n\t"
    "v_perm_b32 v48, v194, v178, s66\n\t"
    "v_perm_b32 v49, v198, v182, s66\n\t"
    "v_perm_b32 v50, v202, v186, s66\n\t"
    "v_perm_b32 v51, v206, v190, s66\n\t"
    "v_mov_b32 v38, v37\n\t"
    "v_mfma_f32_16x16x32_bf16 v[8:11], v[28:31], v[44:47], v[8:11]\n\t"
    "v_perm_b32 v52, v194, v178, s67\n\t"
    "v_perm_b32 v53, v198, v182, s67\n\t"
    "v_perm_b32 v54, v202, v186, s67\n\t"
    "v_perm_b32 v55, v206, v190, s67\n\t"
    "v_permlane16_swap_b32 v37, v38\n\t"
    "v_mfma_f32_16x16x32_bf16 v[12:15], v[16:19], v[48:51], v[12:15]\n\t"
    "s_waitcnt vmcnt(35)\n\t"
    "s_add_u32 s57, s33, 0x7000\n\t"
    "s_xor_b32 s57, s57, s80\n\t"
    "buffer_load_dwordx4 v[156:159], v1, s[44:47], s57 offen offset:0 nt\n\t"
    "v_perm_b32 v44, v195, v179, s66\n\t"
    "v_perm_b32 v45, v199, v183, s66\n\t"
    "v_perm_b32 v46, v203, v187, s66\n\t"
    "v_perm_b32 v47, v207, v191, s66\n\t"
    "v_max_f32 v37, v37, v38\n\t"
    "v_mfma_f32_16x16x32_bf16 v[12:15], v[20:23], v[52:55], v[12:15]\n\t"
    "v_perm_b32 v48, v195, v179, s67\n\t"
    "v_perm_b32 v49, v199, v183, s67\n\t"
    "v_perm_b32 v50, v203, v187, s67\n\t"
    "v_perm_b32 v51, v207, v191, s67\n\t"
    "v_mov_b32 v38, v37\n\t"
    "v_mfma_f32_16x16x32_bf16 v[12:15], v[24:27], v[44:47], v[12:15]\n\t"
    "s_nop 0\n\t"
    "v_permlane32_swap_b32 v37, v38\n\t"
    "v_mfma_f32_16x16x32_bf16 v[12:15], v[28:31], v[48:51], v[12:15]\n\t"
    "v_max_f32 v37, v37, v38\n\t"
    "v_cmp_gt_f32 vcc, v37, v32\n\t"
    "s_cbranch_vccz .Lr32_%=\n\t"
    "v_max_f32 v37, v37, v32\n\t"
    "v_sub_f32 v38, v32, v37\n\t"
    "v_mul_f32 v38, s64, v38\n\t"
    "v_exp_f32 v38, v38\n\t"
    "v_mov_b32 v32, v37\n\t"
    "v_mul_f32 v34, s65, v37\n\t"
    "v_mul_f32 v33, v33, v38\n\t"
    "v_mul_f32_dpp v8, v38, v8 quad_perm:[0,0,0,0] row_mask:0xf bank_mask:0xf\n\t"
    "v_mul_f32_dpp v9, v38, v9 quad_perm:[1,1,1,1] row_mask:0xf bank_mask:0xf\n\t"
    "v_mul_f32_dpp v10, v38, v10 quad_perm:[2,2,2,2] row_mask:0xf bank_mask:0xf\n\t"
    "v_mul_f32_dpp v11, v38, v11 quad_perm:[3,3,3,3] row_mask:0xf bank_mask:0xf\n\t"
    "v_mul_f32_dpp v12, v38, v12 quad_perm:[0,0,0,0] row_mask:0xf bank_mask:0xf\n\t"
    "v_mul_f32_dpp v13, v38, v13 quad_perm:[1,1,1,1] row_mask:0xf bank_mask:0xf\n\t"
    "v_mul_f32_dpp v14, v38, v14 quad_perm:[2,2,2,2] row_mask:0xf bank_mask:0xf\n\t"
    "v_mul_f32_dpp v15, v38, v15 quad_perm:[3,3,3,3] row_mask:0xf bank_mask:0xf\n\t"
    ".Lr32_%=:\n\t"
    "v_fma_f32 v35, v35, s64, v34\n\t"
    "v_fma_f32 v36, v36, s64, v34\n\t"
    "v_exp_f32 v35, v35\n\t"
    "v_exp_f32 v36, v36\n\t"
    "s_nop 0\n\t"
    "v_cvt_pk_bf16_f32 v39, v35, v36\n\t"
    "v_dot2_f32_bf16 v33, v39, s68, v33\n\t"
    "s_nop 0\n\t"
    "v_mov_b32_dpp v16, v39 quad_perm:[0,1,2,3] row_mask:0xf bank_mask:0x1\n\t"
    "v_mov_b32_dpp v17, v39 row_ror:12 row_mask:0xf bank_mask:0x1\n\t"
    "v_mov_b32_dpp v18, v39 row_ror:8 row_mask:0xf bank_mask:0x1\n\t"
    "v_mov_b32_dpp v19, v39 row_ror:4 row_mask:0xf bank_mask:0x1\n\t"
    "v_mov_b32_dpp v20, v39 row_ror:4 row_mask:0xf bank_mask:0x2\n\t"
    "v_mov_b32_dpp v21, v39 quad_perm:[0,1,2,3] row_mask:0xf bank_mask:0x2\n\t"
    "v_mov_b32_dpp v22, v39 row_ror:12 row_mask:0xf bank_mask:0x2\n\t"
    "v_mov_b32_dpp v23, v39 row_ror:8 row_mask:0xf bank_mask:0x2\n\t"
    "v_mov_b32_dpp v24, v39 row_ror:8 row_mask:0xf bank_mask:0x4\n\t"
    "v_mov_b32_dpp v25, v39 row_ror:4 row_mask:0xf bank_mask:0x4\n\t"
    "v_mov_b32_dpp v26, v39 quad_perm:[0,1,2,3] row_mask:0xf bank_mask:0x4\n\t"
    "v_mov_b32_dpp v27, v39 row_ror:12 row_mask:0xf bank_mask:0x4\n\t"
    "v_mov_b32_dpp v28, v39 row_ror:12 row_mask:0xf bank_mask:0x8\n\t"
    "v_mov_b32_dpp v29, v39 row_ror:8 row_mask:0xf bank_mask:0x8\n\t"
    "v_mov_b32_dpp v30, v39 row_ror:4 row_mask:0xf bank_mask:0x8\n\t"
    "v_mov_b32_dpp v31, v39 quad_perm:[0,1,2,3] row_mask:0xf bank_mask:0x8\n\t"
    "v_mfma_f32_16x16x32_bf16 v[44:47], a[88:91], v[4:7], 0\n\t"
    "v_mfma_f32_16x16x32_bf16 v[48:51], a[92:95], v[4:7], 0\n\t"
    "v_mfma_f32_16x16x32_bf16 v[52:55], a[96:99], v[4:7], 0\n\t"
    "s_waitcnt vmcnt(35)\n\t"
    "s_add_u32 s32, s34, 0x30000\n\t"
    "s_and_b32 s32, s32, 0x7ffff\n\t"
    "s_add_u32 s32, s32, s56\n\t"
    "s_add_u32 s57, s32, 0\n\t"
    "s_xor_b32 s57, s57, s80\n\t"
    "buffer_load_dwordx4 a[24:27], v0, s[40:43], s57 offen offset:0 nt\n\t"
    "v_add_f32_dpp v40, v45, v44 row_ror:12 row_mask:0xf bank_mask:0xf\n\t"
    "v_add_f32_dpp v42, v47, v46 row_ror:12 row_mask:0xf bank_mask:0xf\n\t"
    "v_mfma_f32_16x16x32_bf16 v[44:47], a[100:103], v[4:7], 0\n\t"
    "v_add_f32_dpp v41, v50, v49 row_ror:12 row_mask:0xf bank_mask:0xf\n\t"
    "v_add_f32_dpp v43, v48, v51 row_ror:12 row_mask:0xf bank_mask:0xf\n\t"
    "v_add_f32_dpp v35, v42, v40 row_ror:8 row_mask:0xf bank_mask:0x1\n\t"
    "v_mfma_f32_16x16x32_bf16 v[48:51], a[104:107], v[4:7], 0\n\t"
    "v_add_f32_dpp v40, v55, v54 row_ror:12 row_mask:0xf bank_mask:0xf\n\t"
    "v_add_f32_dpp v42, v53, v52 row_ror:12 row_mask:0xf bank_mask:0xf\n\t"
    "v_add_f32_dpp v35, v43, v41 row_ror:8 row_mask:0xf bank_mask:0x2\n\t"
    "v_mfma_f32_16x16x32_bf16 v[52:55], a[108:111], v[4:7], 0\n\t"
    "v_add_f32_dpp v41, v44, v47 row_ror:12 row_mask:0xf bank_mask:0xf\n\t"
    "v_add_f32_dpp v43, v46, v45 row_ror:12 row_mask:0xf bank_mask:0xf\n\t"
    "v_add_f32_dpp v35, v42, v40 row_ror:8 row_mask:0xf bank_mask:0x4\n\t"
    "v_mfma_f32_16x16x32_bf16 v[44:47], a[112:115], v[4:7], 0\n\t"
    "v_add_f32_dpp v40, v49, v48 row_ror:12 row_mask:0xf bank_mask:0xf\n\t"
    "v_add_f32_dpp v42, v51, v50 row_ror:12 row_mask:0xf bank_mask:0xf\n\t"
    "v_add_f32_dpp v35, v43, v41 row_ror:8 row_mask:0xf bank_mask:0x8\n\t"
    "v_mfma_f32_16x16x32_bf16 v[48:51], a[116:119], v[4:7], 0\n\t"
    "v_add_f32_dpp v41, v54, v53 row_ror:12 row_mask:0xf bank_mask:0xf\n\t"
    "v_add_f32_dpp v43, v52, v55 row_ror:12 row_mask:0xf bank_mask:0xf\n\t"
    "v_add_f32_dpp v36, v42, v40 row_ror:8 row_mask:0xf bank_mask:0x1\n\t"
    "s_nop 0\n\t"
    "v_add_f32_dpp v40, v47, v46 row_ror:12 row_mask:0xf bank_mask:0xf\n\t"
    "v_add_f32_dpp v42, v45, v44 row_ror:12 row_mask:0xf bank_mask:0xf\n\t"
    "v_add_f32_dpp v36, v43, v41 row_ror:8 row_mask:0xf bank_mask:0x2\n\t"
    "s_nop 0\n\t"
    "v_add_f32_dpp v41, v48, v51 row_ror:12 row_mask:0xf bank_mask:0xf\n\t"
    "v_add_f32_dpp v43, v50, v49 row_ror:12 row_mask:0xf bank_mask:0xf\n\t"
    "v_add_f32_dpp v36, v42, v40 row_ror:8 row_mask:0xf bank_mask:0x4\n\t"
    "s_nop 0\n\t"
    "v_add_f32_dpp v36, v43, v41 row_ror:8 row_mask:0xf bank_mask:0x8\n\t"
    "s_waitcnt vmcnt(35)\n\t"
    "s_add_u32 s57, s32, 0x1000\n\t"
    "s_xor_b32 s57, s57, s80\n\t"
    "buffer_load_dwordx4 a[28:31], v0, s[40:43], s57 offen offset:0 nt\n\t"
    "s_waitcnt vmcnt(35)\n\t"
    "s_add_u32 s57, s32, 0x2000\n\t"
    "s_xor_b32 s57, s57, s80\n\t"
    "buffer_load_dwordx4 a[32:35], v0, s[40:43], s57 offen offset:0 nt\n\t"
    "s_waitcnt vmcnt(35)\n\t"
    "s_add_u32 s57, s32, 0x3000\n\t"
    "s_xor_b32 s57, s57, s80\n\t"
    "buffer_load_dwordx4 a[36:39], v0, s[40:43], s57 offen offset:0 nt\n\t"
    "v_perm_b32 v44, v72, v56, s66\n\t"
    "v_perm_b32 v45, v76, v60, s66\n\t"
    "v_perm_b32 v46, v80, v64, s66\n\t"
    "v_perm_b32 v47, v84, v68, s66\n\t"
    "v_perm_b32 v48, v72, v56, s67\n\t"
    "v_perm_b32 v49, v76, v60, s67\n\t"
    "v_perm_b32 v50, v80, v64, s67\n\t"
    "v_perm_b32 v51, v84, v68, s67\n\t"
    "v_max_f32 v37, v35, v36\n\t"
    "v_mfma_f32_16x16x32_bf16 v[8:11], v[16:19], v[44:47], v[8:11]\n\t"
    "v_perm_b32 v52, v73, v57, s66\n\t"
    "v_perm_b32 v53, v77, v61, s66\n\t"
    "v_perm_b32 v54, v81, v65, s66\n\t"
    "v_perm_b32 v55, v85, v69, s66\n\t"
    "v_max_f32_dpp v37, v37, v37 row_ror:4 row_mask:0xf bank_mask:0xf\n\t"
    "v_mfma_f32_16x16x32_bf16 v[8:11], v[20:23], v[48:51], v[8:11]\n\t"
    "v_perm_b32 v44, v73, v57, s67\n\t"
    "v_perm_b32 v45, v77, v61, s67\n\t"
    "v_perm_b32 v46, v81, v65, s67\n\t"
    "v_perm_b32 v47, v85, v69, s67\n\t"
    "v_max_f32_dpp v37, v37, v37 row_ror:8 row_mask:0xf bank_mask:0xf\n\t"
    "v_mfma_f32_16x16x32_bf16 v[8:11], v[24:27], v[52:55], v[8:11]\n\t"
    "v_perm_b32 v48, v74, v58, s66\n\t"
    "v_perm_b32 v49, v78, v62, s66\n\t"
    "v_perm_b32 v50, v82, v66, s66\n\t"
    "v_perm_b32 v51, v86, v70, s66\n\t"
    "v_mov_b32 v38, v37\n\t"
    "v_mfma_f32_16x16x32_bf16 v[8:11], v[28:31], v[44:47], v[8:11]\n\t"
    "v_perm_b32 v52, v74, v58, s67\n\t"
    "v_perm_b32 v53, v78, v62, s67\n\t"
    "v_perm_b32 v54, v82, v66, s67\n\t"
    "v_perm_b32 v55, v86, v70, s67\n\t"
    "v_permlane16_swap_b32 v37, v38\n\t"
    "v_mfma_f32_16x16x32_bf16 v[12:15], v[16:19], v[48:51], v[12:15]\n\t"
    "v_perm_b32 v44, v75, v59, s66\n\t"
    "v_perm_b32 v45, v79, v63, s66\n\t"
    "v_perm_b32 v46, v83, v67, s66\n\t"
    "v_perm_b32 v47, v87, v71, s66\n\t"
    "v_max_f32 v37, v37, v38\n\t"
    "v_mfma_f32_16x16x32_bf16 v[12:15], v[20:23], v[52:55], v[12:15]\n\t"
    "v_perm_b32 v48, v75, v59, s67\n\t"
    "v_perm_b32 v49, v79, v63, s67\n\t"
    "v_perm_b32 v50, v83, v67, s67\n\t"
    "v_perm_b32 v51, v87, v71, s67\n\t"
    "v_mov_b32 v38, v37\n\t"
    "v_mfma_f32_16x16x32_bf16 v[12:15], v[24:27], v[44:47], v[12:15]\n\t"
    "s_nop 0\n\t"
    "v_permlane32_swap_b32 v37, v38\n\t"
    "v_mfma_f32_16x16x32_bf16 v[12:15], v[28:31], v[48:51], v[12:15]\n\t"
    "v_max_f32 v37, v37, v38\n\t"
    "v_cmp_gt_f32 vcc, v37, v32\n\t"
    "s_cbranch_vccz .Lr33_%=\n\t"
    "v_max_f32 v37, v37, v32\n\t"
    "v_sub_f32 v38, v32, v37\n\t"
    "v_mul_f32 v38, s64, v38\n\t"
    "v_exp_f32 v38, v38\n\t"
    "v_mov_b32 v32, v37\n\t"
    "v_mul_f32 v34, s65, v37\n\t"
    "v_mul_f32 v33, v33, v38\n\t"
    "v_mul_f32_dpp v8, v38, v8 quad_perm:[0,0,0,0] row_mask:0xf bank_mask:0xf\n\t"
    "v_mul_f32_dpp v9, v38, v9 quad_perm:[1,1,1,1] row_mask:0xf bank_mask:0xf\n\t"
    "v_mul_f32_dpp v10, v38, v10 quad_perm:[2,2,2,2] row_mask:0xf bank_mask:0xf\n\t"
    "v_mul_f32_dpp v11, v38, v11 quad_perm:[3,3,3,3] row_mask:0xf bank_mask:0xf\n\t"
    "v_mul_f32_dpp v12, v38, v12 quad_perm:[0,0,0,0] row_mask:0xf bank_mask:0xf\n\t"
    "v_mul_f32_dpp v13, v38, v13 quad_perm:[1,1,1,1] row_mask:0xf bank_mask:0xf\n\t"
    "v_mul_f32_dpp v14, v38, v14 quad_perm:[2,2,2,2] row_mask:0xf bank_mask:0xf\n\t"
    "v_mul_f32_dpp v15, v38, v15 quad_perm:[3,3,3,3] row_mask:0xf bank_mask:0xf\n\t"
    ".Lr33_%=:\n\t"
    "v_fma_f32 v35, v35, s64, v34\n\t"
    "v_fma_f32 v36, v36, s64, v34\n\t"
    "v_exp_f32 v35, v35\n\t"
    "v_exp_f32 v36, v36\n\t"
    "s_nop 0\n\t"
    "v_cvt_pk_bf16_f32 v39, v35, v36\n\t"
    "v_dot2_f32_bf16 v33, v39, s68, v33\n\t"
    "s_waitcnt vmcnt(35)\n\t"
    "s_add_u32 s57, s32, 0x4000\n\t"
    "s_xor_b32 s57, s57, s80\n\t"
    "buffer_load_dwordx4 a[40:43], v0, s[40:43], s57 offen offset:0 nt\n\t"
    "v_mov_b32_dpp v16, v39 quad_perm:[0,1,2,3] row_mask:0xf bank_mask:0x1\n\t"
    "v_mov_b32_dpp v17, v39 row_ror:12 row_mask:0xf bank_mask:0x1\n\t"
    "v_mov_b32_dpp v18, v39 row_ror:8 row_mask:0xf bank_mask:0x1\n\t"
    "v_mov_b32_dpp v19, v39 row_ror:4 row_mask:0xf bank_mask:0x1\n\t"
    "v_mov_b32_dpp v20, v39 row_ror:4 row_mask:0xf bank_mask:0x2\n\t"
    "v_mov_b32_dpp v21, v39 quad_perm:[0,1,2,3] row_mask:0xf bank_mask:0x2\n\t"
    "v_mov_b32_dpp v22, v39 row_ror:12 row_mask:0xf bank_mask:0x2\n\t"
    "v_mov_b32_dpp v23, v39 row_ror:8 row_mask:0xf bank_mask:0x2\n\t"
    "v_mov_b32_dpp v24, v39 row_ror:8 row_mask:0xf bank_mask:0x4\n\t"
    "v_mov_b32_dpp v25, v39 row_ror:4 row_mask:0xf bank_mask:0x4\n\t"
    "v_mov_b32_dpp v26, v39 quad_perm:[0,1,2,3] row_mask:0xf bank_mask:0x4\n\t"
    "v_mov_b32_dpp v27, v39 row_ror:12 row_mask:0xf bank_mask:0x4\n\t"
    "v_mov_b32_dpp v28, v39 row_ror:12 row_mask:0xf bank_mask:0x8\n\t"
    "v_mov_b32_dpp v29, v39 row_ror:8 row_mask:0xf bank_mask:0x8\n\t"
    "v_mov_b32_dpp v30, v39 row_ror:4 row_mask:0xf bank_mask:0x8\n\t"
    "v_mov_b32_dpp v31, v39 quad_perm:[0,1,2,3] row_mask:0xf bank_mask:0x8\n\t"
    "v_mfma_f32_16x16x32_bf16 v[44:47], a[120:123], v[4:7], 0\n\t"
    "s_waitcnt vmcnt(35)\n\t"
    "s_add_u32 s57, s32, 0x5000\n\t"
    "s_xor_b32 s57, s57, s80\n\t"
    "buffer_load_dwordx4 a[44:47], v0, s[40:43], s57 offen offset:0 nt\n\t"
    "v_mfma_f32_16x16x32_bf16 v[48:51], a[124:127], v[4:7], 0\n\t"
    "s_waitcnt vmcnt(35)\n\t"
    "s_add_u32 s57, s32, 0x6000\n\t"
    "s_xor_b32 s57, s57, s80\n\t"
    "buffer_load_dwordx4 a[48:51], v0, s[40:43], s57 offen offset:0 nt\n\t"
    "v_mfma_f32_16x16x32_bf16 v[52:55], a[128:131], v[4:7], 0\n\t"
    "v_add_f32_dpp v40, v45, v44 row_ror:12 row_mask:0xf bank_mask:0xf\n\t"
    "v_add_f32_dpp v42, v47, v46 row_ror:12 row_mask:0xf bank_mask:0xf\n\t"
    "s_waitcnt vmcnt(35)\n\t"
    "s_add_u32 s57, s32, 0x7000\n\t"
    "s_xor_b32 s57, s57, s80\n\t"
    "buffer_load_dwordx4 a[52:55], v0, s[40:43], s57 offen offset:0 nt\n\t"
    "s_add_u32 s33, s34, 0x60000\n\t"
    "s_and_b32 s33, s33, 0x7ffff\n\t"
    "s_add_u32 s33, s33, s56\n\t"
    "s_add_u32 s57, s33, 0\n\t"
    "s_xor_b32 s57, s57, s80\n\t"
    "buffer_load_dwordx4 v[160:163], v1, s[44:47], s57 offen offset:0 nt\n\t"
    "s_add_u32 s57, s33, 0x1000\n\t"
    "s_xor_b32 s57, s57, s80\n\t"
    "buffer_load_dwordx4 v[164:167], v1, s[44:47], s57 offen offset:0 nt\n\t"
    "s_add_u32 s57, s33, 0x2000\n\t"
    "s_xor_b32 s57, s57, s80\n\t"
    "buffer_load_dwordx4 v[168:171], v1, s[44:47], s57 offen offset:0 nt\n\t"
    "s_add_u32 s57, s33, 0x3000\n\t"
    "s_xor_b32 s57, s57, s80\n\t"
    "buffer_load_dwordx4 v[172:175], v1, s[44:47], s57 offen offset:0 nt\n\t"
    "s_add_u32 s57, s33, 0x4000\n\t"
    "s_xor_b32 s57, s57, s80\n\t"
    "buffer_load_dwordx4 v[220:223], v1, s[44:47], s57 offen offset:0 nt\n\t"
    "s_add_u32 s57, s33, 0x5000\n\t"
    "s_xor_b32 s57, s57, s80\n\t"
    "buffer_load_dwordx4 v[216:219], v1, s[44:47], s57 offen offset:0 nt\n\t"
    "s_add_u32 s57, s33, 0x6000\n\t"
    "s_xor_b32 s57, s57, s80\n\t"
    "buffer_load_dwordx4 v[212:215], v1, s[44:47], s57 offen offset:0 nt\n\t"
    "s_add_u32 s57, s33, 0x7000\n\t"
    "s_xor_b32 s57, s57, s80\n\t"
    "buffer_load_dwordx4 v[208:211], v1, s[44:47], s57 offen offset:0 nt\n\t"
    "v_mfma_f32_16x16x32_bf16 v[44:47], a[132:135], v[4:7], 0\n\t"
    "v_add_f32_dpp v41, v50, v49 row_ror:12 row_mask:0xf bank_mask:0xf\n\t"
    "v_add_f32_dpp v43, v48, v51 row_ror:12 row_mask:0xf bank_mask:0xf\n\t"
    "v_add_f32_dpp v35, v42, v40 row_ror:8 row_mask:0xf bank_mask:0x1\n\t"
    "s_waitcnt vmcnt(43)\n\t"
    "s_add_u32 s32, s34, 0x38000\n\t"
    "s_and_b32 s32, s32, 0x7ffff\n\t"
    "s_add_u32 s32, s32, s56\n\t"
    "s_add_u32 s57, s32, 0\n\t"
    "s_xor_b32 s57, s57, s80\n\t"
    "buffer_load_dwordx4 a[56:59], v0, s[40:43], s57 offen offset:0 nt\n\t"
    "v_mfma_f32_16x16x32_bf16 v[48:51], a[136:139], v[4:7], 0\n\t"
    "v_add_f32_dpp v40, v55, v54 row_ror:12 row_mask:0xf bank_mask:0xf\n\t"
    "v_add_f32_dpp v42, v53, v52 row_ror:12 row_mask:0xf bank_mask:0xf\n\t"
    "v_add_f32_dpp v35, v43, v41 row_ror:8 row_mask:0xf bank_mask:0x2\n\t"
    "s_waitcnt vmcnt(43)\n\t"
    "s_add_u32 s57, s32, 0x1000\n\t"
    "s_xor_b32 s57, s57, s80\n\t"
    "buffer_load_dwordx4 a[60:63], v0, s[40:43], s57 offen offset:0 nt\n\t"
    "v_mfma_f32_16x16x32_bf16 v[52:55], a[140:143], v[4:7], 0\n\t"
    "v_add_f32_dpp v41, v44, v47 row_ror:12 row_mask:0xf bank_mask:0xf\n\t"
    "v_add_f32_dpp v43, v46, v45 row_ror:12 row_mask:0xf bank_mask:0xf\n\t"
    "v_add_f32_dpp v35, v42, v40 row_ror:8 row_mask:0xf bank_mask:0x4\n\t"
    "s_waitcnt vmcnt(43)\n\t"
    "s_add_u32 s57, s32, 0x2000\n\t"
    "s_xor_b32 s57, s57, s80\n\t"
    "buffer_load_dwordx4 a[184:187], v0, s[40:43], s57 offen offset:0 nt\n\t"
    "v_mfma_f32_16x16x32_bf16 v[44:47], a[144:147], v[4:7], 0\n\t"
    "v_add_f32_dpp v40, v49, v48 row_ror:12 row_mask:0xf bank_mask:0xf\n\t"
    "v_add_f32_dpp v42, v51, v50 row_ror:12 row_mask:0xf bank_mask:0xf\n\t"
    "v_add_f32_dpp v35, v43, v41 row_ror:8 row_mask:0xf bank_mask:0x8\n\t"
    "s_waitcnt vmcnt(43)\n\t"
    "s_add_u32 s57, s32, 0x3000\n\t"
    "s_xor_b32 s57, s57, s80\n\t"
    "buffer_load_dwordx4 a[188:191], v0, s[40:43], s57 offen offset:0 nt\n\t"
    "v_mfma_f32_16x16x32_bf16 v[48:51], a[148:151], v[4:7], 0\n\t"
    "v_add_f32_dpp v41, v54, v53 row_ror:12 row_mask:0xf bank_mask:0xf\n\t"
    "v_add_f32_dpp v43, v52, v55 row_ror:12 row_mask:0xf bank_mask:0xf\n\t"
    "v_add_f32_dpp v36, v42, v40 row_ror:8 row_mask:0xf bank_mask:0x1\n\t"
    "v_add_f32_dpp v40, v47, v46 row_ror:12 row_mask:0xf bank_mask:0xf\n\t"
    "v_add_f32_dpp v42, v45, v44 row_ror:12 row_mask:0xf bank_mask:0xf\n\t"
    "v_add_f32_dpp v36, v43, v41 row_ror:8 row_mask:0xf bank_mask:0x2\n\t"
    "s_nop 1\n\t"
    "v_add_f32_dpp v41, v48, v51 row_ror:12 row_mask:0xf bank_mask:0xf\n\t"
    "v_add_f32_dpp v43, v50, v49 row_ror:12 row_mask:0xf bank_mask:0xf\n\t"
    "v_add_f32_dpp v36, v42, v40 row_ror:8 row_mask:0xf bank_mask:0x4\n\t"
    "s_nop 0\n\t"
    "v_add_f32_dpp v36, v43, v41 row_ror:8 row_mask:0xf bank_mask:0x8\n\t"
    "s_waitcnt vmcnt(43)\n\t"
    "s_waitcnt vmcnt(42)\n\t"
    "s_waitcnt vmcnt(41)\n\t"
    "s_waitcnt vmcnt(40)\n\t"
    "s_waitcnt vmcnt(39)\n\t"
    "s_waitcnt vmcnt(38)\n\t"
    "s_waitcnt vmcnt(37)\n\t"
    "s_waitcnt vmcnt(36)\n\t"
    "v_perm_b32 v44, v104, v88, s66\n\t"
    "v_perm_b32 v45, v108, v92, s66\n\t"
    "v_perm_b32 v46, v112, v96, s66\n\t"
    "v_perm_b32 v47, v116, v100, s66\n\t"
    "v_perm_b32 v48, v104, v88, s67\n\t"
    "v_perm_b32 v49, v108, v92, s67\n\t"
    "v_perm_b32 v50, v112, v96, s67\n\t"
    "v_perm_b32 v51, v116, v100, s67\n\t"
    "v_max_f32 v37, v35, v36\n\t"
    "v_mfma_f32_16x16x32_bf16 v[8:11], v[16:19], v[44:47], v[8:11]\n\t"
    "v_perm_b32 v52, v105, v89, s66\n\t"
    "v_perm_b32 v53, v109, v93, s66\n\t"
    "v_perm_b32 v54, v113, v97, s66\n\t"
    "v_perm_b32 v55, v117, v101, s66\n\t"
    "v_max_f32_dpp v37, v37, v37 row_ror:4 row_mask:0xf bank_mask:0xf\n\t"
    "v_mfma_f32_16x16x32_bf16 v[8:11], v[20:23], v[48:51], v[8:11]\n\t"
    "v_perm_b32 v44, v105, v89, s67\n\t"
    "v_perm_b32 v45, v109, v93, s67\n\t"
    "v_perm_b32 v46, v113, v97, s67\n\t"
    "v_perm_b32 v47, v117, v101, s67\n\t"
    "v_max_f32_dpp v37, v37, v37 row_ror:8 row_mask:0xf bank_mask:0xf\n\t"
    "v_mfma_f32_16x16x32_bf16 v[8:11], v[24:27], v[52:55], v[8:11]\n\t"
    "v_perm_b32 v48, v106, v90, s66\n\t"
    "v_perm_b32 v49, v110, v94, s66\n\t"
    "v_perm_b32 v50, v114, v98, s66\n\t"
    "v_perm_b32 v51, v118, v102, s66\n\t"
    "v_mov_b32 v38, v37\n\t"
    "v_mfma_f32_16x16x32_bf16 v[8:11], v[28:31], v[44:47], v[8:11]\n\t"
    "v_perm_b32 v52, v106, v90, s67\n\t"
    "v_perm_b32 v53, v110, v94, s67\n\t"
    "v_perm_b32 v54, v114, v98, s67\n\t"
    "v_perm_b32 v55, v118, v102, s67\n\t"
    "v_permlane16_swap_b32 v37, v38\n\t"
    "v_mfma_f32_16x16x32_bf16 v[12:15], v[16:19], v[48:51], v[12:15]\n\t"
    "v_perm_b32 v44, v107, v91, s66\n\t"
    "v_perm_b32 v45, v111, v95, s66\n\t"
    "v_perm_b32 v46, v115, v99, s66\n\t"
    "v_perm_b32 v47, v119, v103, s66\n\t"
    "v_max_f32 v37, v37, v38\n\t"
    "v_mfma_f32_16x16x32_bf16 v[12:15], v[20:23], v[52:55], v[12:15]\n\t"
    "s_waitcnt vmcnt(27)\n\t"
    "s_add_u32 s57, s32, 0x4000\n\t"
    "s_xor_b32 s57, s57, s80\n\t"
    "buffer_load_dwordx4 a[192:195], v0, s[40:43], s57 offen offset:0 nt\n\t"
    "v_perm_b32 v48, v107, v91, s67\n\t"
    "v_perm_b32 v49, v111, v95, s67\n\t"
    "v_perm_b32 v50, v115, v99, s67\n\t"
    "v_perm_b32 v51, v119, v103, s67\n\t"
    "v_mov_b32 v38, v37\n\t"
    "v_mfma_f32_16x16x32_bf16 v[12:15], v[24:27], v[44:47], v[12:15]\n\t"
    "s_nop 0\n\t"
    "v_permlane32_swap_b32 v37, v38\n\t"
    "v_mfma_f32_16x16x32_bf16 v[12:15], v[28:31], v[48:51], v[12:15]\n\t"
    "v_max_f32 v37, v37, v38\n\t"
    "v_cmp_gt_f32 vcc, v37, v32\n\t"
    "s_cbranch_vccz .Lr34_%=\n\t"
    "v_max_f32 v37, v37, v32\n\t"
    "v_sub_f32 v38, v32, v37\n\t"
    "v_mul_f32 v38, s64, v38\n\t"
    "v_exp_f32 v38, v38\n\t"
    "v_mov_b32 v32, v37\n\t"
    "v_mul_f32 v34, s65, v37\n\t"
    "v_mul_f32 v33, v33, v38\n\t"
    "v_mul_f32_dpp v8, v38, v8 quad_perm:[0,0,0,0] row_mask:0xf bank_mask:0xf\n\t"
    "v_mul_f32_dpp v9, v38, v9 quad_perm:[1,1,1,1] row_mask:0xf bank_mask:0xf\n\t"
    "v_mul_f32_dpp v10, v38, v10 quad_perm:[2,2,2,2] row_mask:0xf bank_mask:0xf\n\t"
    "v_mul_f32_dpp v11, v38, v11 quad_perm:[3,3,3,3] row_mask:0xf bank_mask:0xf\n\t"
    "v_mul_f32_dpp v12, v38, v12 quad_perm:[0,0,0,0] row_mask:0xf bank_mask:0xf\n\t"
    "v_mul_f32_dpp v13, v38, v13 quad_perm:[1,1,1,1] row_mask:0xf bank_mask:0xf\n\t"
    "v_mul_f32_dpp v14, v38, v14 quad_perm:[2,2,2,2] row_mask:0xf bank_mask:0xf\n\t"
    "v_mul_f32_dpp v15, v38, v15 quad_perm:[3,3,3,3] row_mask:0xf bank_mask:0xf\n\t"
    ".Lr34_%=:\n\t"
    "v_fma_f32 v35, v35, s64, v34\n\t"
    "v_fma_f32 v36, v36, s64, v34\n\t"
    "v_exp_f32 v35, v35\n\t"
    "v_exp_f32 v36, v36\n\t"
    "s_nop 0\n\t"
    "v_cvt_pk_bf16_f32 v39, v35, v36\n\t"
    "v_dot2_f32_bf16 v33, v39, s68, v33\n\t"
    "s_nop 0\n\t"
    "v_mov_b32_dpp v16, v39 quad_perm:[0,1,2,3] row_mask:0xf bank_mask:0x1\n\t"
    "v_mov_b32_dpp v17, v39 row_ror:12 row_mask:0xf bank_mask:0x1\n\t"
    "v_mov_b32_dpp v18, v39 row_ror:8 row_mask:0xf bank_mask:0x1\n\t"
    "v_mov_b32_dpp v19, v39 row_ror:4 row_mask:0xf bank_mask:0x1\n\t"
    "v_mov_b32_dpp v20, v39 row_ror:4 row_mask:0xf bank_mask:0x2\n\t"
    "v_mov_b32_dpp v21, v39 quad_perm:[0,1,2,3] row_mask:0xf bank_mask:0x2\n\t"
    "v_mov_b32_dpp v22, v39 row_ror:12 row_mask:0xf bank_mask:0x2\n\t"
    "v_mov_b32_dpp v23, v39 row_ror:8 row_mask:0xf bank_mask:0x2\n\t"
    "v_mov_b32_dpp v24, v39 row_ror:8 row_mask:0xf bank_mask:0x4\n\t"
    "v_mov_b32_dpp v25, v39 row_ror:4 row_mask:0xf bank_mask:0x4\n\t"
    "v_mov_b32_dpp v26, v39 quad_perm:[0,1,2,3] row_mask:0xf bank_mask:0x4\n\t"
    "v_mov_b32_dpp v27, v39 row_ror:12 row_mask:0xf bank_mask:0x4\n\t"
    "v_mov_b32_dpp v28, v39 row_ror:12 row_mask:0xf bank_mask:0x8\n\t"
    "v_mov_b32_dpp v29, v39 row_ror:8 row_mask:0xf bank_mask:0x8\n\t"
    "v_mov_b32_dpp v30, v39 row_ror:4 row_mask:0xf bank_mask:0x8\n\t"
    "v_mov_b32_dpp v31, v39 quad_perm:[0,1,2,3] row_mask:0xf bank_mask:0x8\n\t"
    "v_mfma_f32_16x16x32_bf16 v[44:47], a[152:155], v[4:7], 0\n\t"
    "v_mfma_f32_16x16x32_bf16 v[48:51], a[156:159], v[4:7], 0\n\t"
    "v_mfma_f32_16x16x32_bf16 v[52:55], a[160:163], v[4:7], 0\n\t"
    "s_nop 5\n\t"
    "v_add_f32_dpp v40, v45, v44 row_ror:12 row_mask:0xf bank_mask:0xf\n\t"
    "v_add_f32_dpp v42, v47, v46 row_ror:12 row_mask:0xf bank_mask:0xf\n\t"
    "v_mfma_f32_16x16x32_bf16 v[44:47], a[164:167], v[4:7], 0\n\t"
    "v_add_f32_dpp v41, v50, v49 row_ror:12 row_mask:0xf bank_mask:0xf\n\t"
    "v_add_f32_dpp v43, v48, v51 row_ror:12 row_mask:0xf bank_mask:0xf\n\t"
    "v_add_f32_dpp v35, v42, v40 row_ror:8 row_mask:0xf bank_mask:0x1\n\t"
    "v_mfma_f32_16x16x32_bf16 v[48:51], a[168:171], v[4:7], 0\n\t"
    "s_waitcnt vmcnt(27)\n\t"
    "s_add_u32 s57, s32, 0x5000\n\t"
    "s_xor_b32 s57, s57, s80\n\t"
    "buffer_load_dwordx4 a[196:199], v0, s[40:43], s57 offen offset:0 nt\n\t"
    "v_add_f32_dpp v40, v55, v54 row_ror:12 row_mask:0xf bank_mask:0xf\n\t"
    "v_add_f32_dpp v42, v53, v52 row_ror:12 row_mask:0xf bank_mask:0xf\n\t"
    "v_add_f32_dpp v35, v43, v41 row_ror:8 row_mask:0xf bank_mask:0x2\n\t"
    "v_mfma_f32_16x16x32_bf16 v[52:55], a[172:175], v[4:7], 0\n\t"
    "v_add_f32_dpp v41, v44, v47 row_ror:12 row_mask:0xf bank_mask:0xf\n\t"
    "v_add_f32_dpp v43, v46, v45 row_ror:12 row_mask:0xf bank_mask:0xf\n\t"
    "v_add_f32_dpp v35, v42, v40 row_ror:8 row_mask:0xf bank_mask:0x4\n\t"
    "v_mfma_f32_16x16x32_bf16 v[44:47], a[176:179], v[4:7], 0\n\t"
    "v_add_f32_dpp v40, v49, v48 row_ror:12 row_mask:0xf bank_mask:0xf\n\t"
    "v_add_f32_dpp v42, v51, v50 row_ror:12 row_mask:0xf bank_mask:0xf\n\t"
    "v_add_f32_dpp v35, v43, v41 row_ror:8 row_mask:0xf bank_mask:0x8\n\t"
    "v_mfma_f32_16x16x32_bf16 v[48:51], a[180:183], v[4:7], 0\n\t"
    "v_add_f32_dpp v41, v54, v53 row_ror:12 row_mask:0xf bank_mask:0xf\n\t"
    "v_add_f32_dpp v43, v52, v55 row_ror:12 row_mask:0xf bank_mask:0xf\n\t"
    "v_add_f32_dpp v36, v42, v40 row_ror:8 row_mask:0xf bank_mask:0x1\n\t"
    "s_nop 0\n\t"
    "v_add_f32_dpp v40, v47, v46 row_ror:12 row_mask:0xf bank_mask:0xf\n\t"
    "v_add_f32_dpp v42, v45, v44 row_ror:12 row_mask:0xf bank_mask:0xf\n\t"
    "v_add_f32_dpp v36, v43, v41 row_ror:8 row_mask:0xf bank_mask:0x2\n\t"
    "s_nop 0\n\t"
    "v_add_f32_dpp v41, v48, v51 row_ror:12 row_mask:0xf bank_mask:0xf\n\t"
    "v_add_f32_dpp v43, v50, v49 row_ror:12 row_mask:0xf bank_mask:0xf\n\t"
    "v_add_f32_dpp v36, v42, v40 row_ror:8 row_mask:0xf bank_mask:0x4\n\t"
    "s_nop 0\n\t"
    "v_add_f32_dpp v36, v43, v41 row_ror:8 row_mask:0xf bank_mask:0x8\n\t"
    "s_waitcnt vmcnt(27)\n\t"
    "s_add_u32 s57, s32, 0x6000\n\t"
    "s_xor_b32 s57, s57, s80\n\t"
    "buffer_load_dwordx4 a[200:203], v0, s[40:43], s57 offen offset:0 nt\n\t"
    "s_waitcnt vmcnt(27)\n\t"
    "s_add_u32 s57, s32, 0x7000\n\t"
    "s_xor_b32 s57, s57, s80\n\t"
    "buffer_load_dwordx4 a[204:207], v0, s[40:43], s57 offen offset:0 nt\n\t"
    "s_waitcnt vmcnt(27)\n\t"
    "s_add_u32 s33, s34, 0x30000\n\t"
    "s_and_b32 s33, s33, 0x7ffff\n\t"
    "s_add_u32 s33, s33, s56\n\t"
    "s_add_u32 s57, s33, 0\n\t"
    "s_xor_b32 s57, s57, s80\n\t"
    "buffer_load_dwordx4 v[252:255], v1, s[44:47], s57 offen offset:0 nt\n\t"
    "s_waitcnt vmcnt(27)\n\t"
    "s_add_u32 s57, s33, 0x1000\n\t"
    "s_xor_b32 s57, s57, s80\n\t"
    "buffer_load_dwordx4 v[248:251], v1, s[44:47], s57 offen offset:0 nt\n\t"
    "s_waitcnt vmcnt(27)\n\t"
    "s_add_u32 s57, s33, 0x2000\n\t"
    "s_xor_b32 s57, s57, s80\n\t"
    "buffer_load_dwordx4 v[244:247], v1, s[44:47], s57 offen offset:0 nt\n\t"
    "s_waitcnt vmcnt(27)\n\t"
    "s_add_u32 s57, s33, 0x3000\n\t"
    "s_xor_b32 s57, s57, s80\n\t"
    "buffer_load_dwordx4 v[240:243], v1, s[44:47], s57 offen offset:0 nt\n\t"
    "v_perm_b32 v44, v144, v128, s66\n\t"
    "v_perm_b32 v45, v148, v132, s66\n\t"
    "v_perm_b32 v46, v152, v136, s66\n\t"
    "v_perm_b32 v47, v156, v140, s66\n\t"
    "v_perm_b32 v48, v144, v128, s67\n\t"
    "v_perm_b32 v49, v148, v132, s67\n\t"
    "v_perm_b32 v50, v152, v136, s67\n\t"
    "v_perm_b32 v51, v156, v140, s67\n\t"
    "v_max_f32 v37, v35, v36\n\t"
    "v_mfma_f32_16x16x32_bf16 v[8:11], v[16:19], v[44:47], v[8:11]\n\t"
    "v_perm_b32 v52, v145, v129, s66\n\t"
    "v_perm_b32 v53, v149, v133, s66\n\t"
    "v_perm_b32 v54, v153, v137, s66\n\t"
    "v_perm_b32 v55, v157, v141, s66\n\t"
    "v_max_f32_dpp v37, v37, v37 row_ror:4 row_mask:0xf bank_mask:0xf\n\t"
    "v_mfma_f32_16x16x32_bf16 v[8:11], v[20:23], v[48:51], v[8:11]\n\t"
    "v_perm_b32 v44, v145, v129, s67\n\t"
    "v_perm_b32 v45, v149, v133, s67\n\t"
    "v_perm_b32 v46, v153, v137, s67\n\t"
    "v_perm_b32 v47, v157, v141, s67\n\t"
    "v_max_f32_dpp v37, v37, v37 row_ror:8 row_mask:0xf bank_mask:0xf\n\t"
    "v_mfma_f32_16x16x32_bf16 v[8:11], v[24:27], v[52:55], v[8:11]\n\t"
    "v_perm_b32 v48, v146, v130, s66\n\t"
    "v_perm_b32 v49, v150, v134, s66\n\t"
    "v_perm_b32 v50, v154, v138, s66\n\t"
    "v_perm_b32 v51, v158, v142, s66\n\t"
    "v_mov_b32 v38, v37\n\t"
    "v_mfma_f32_16x16x32_bf16 v[8:11], v[28:31], v[44:47], v[8:11]\n\t"
    "v_perm_b32 v52, v146, v130, s67\n\t"
    "v_perm_b32 v53, v150, v134, s67\n\t"
    "v_perm_b32 v54, v154, v138, s67\n\t"
    "v_perm_b32 v55, v158, v142, s67\n\t"
    "v_permlane16_swap_b32 v37, v38\n\t"
    "v_mfma_f32_16x16x32_bf16 v[12:15], v[16:19], v[48:51], v[12:15]\n\t"
    "v_perm_b32 v44, v147, v131, s66\n\t"
    "v_perm_b32 v45, v151, v135, s66\n\t"
    "v_perm_b32 v46, v155, v139, s66\n\t"
    "v_perm_b32 v47, v159, v143, s66\n\t"
    "v_max_f32 v37, v37, v38\n\t"
    "v_mfma_f32_16x16x32_bf16 v[12:15], v[20:23], v[52:55], v[12:15]\n\t"
    "v_perm_b32 v48, v147, v131, s67\n\t"
    "v_perm_b32 v49, v151, v135, s67\n\t"
    "v_perm_b32 v50, v155, v139, s67\n\t"
    "v_perm_b32 v51, v159, v143, s67\n\t"
    "v_mov_b32 v38, v37\n\t"
    "v_mfma_f32_16x16x32_bf16 v[12:15], v[24:27], v[44:47], v[12:15]\n\t"
    "s_nop 0\n\t"
    "v_permlane32_swap_b32 v37, v38\n\t"
    "v_mfma_f32_16x16x32_bf16 v[12:15], v[28:31], v[48:51], v[12:15]\n\t"
    "v_max_f32 v37, v37, v38\n\t"
    "v_cmp_gt_f32 vcc, v37, v32\n\t"
    "s_cbranch_vccz .Lr35_%=\n\t"
    "v_max_f32 v37, v37, v32\n\t"
    "v_sub_f32 v38, v32, v37\n\t"
    "v_mul_f32 v38, s64, v38\n\t"
    "v_exp_f32 v38, v38\n\t"
    "v_mov_b32 v32, v37\n\t"
    "v_mul_f32 v34, s65, v37\n\t"
    "v_mul_f32 v33, v33, v38\n\t"
    "v_mul_f32_dpp v8, v38, v8 quad_perm:[0,0,0,0] row_mask:0xf bank_mask:0xf\n\t"
    "v_mul_f32_dpp v9, v38, v9 quad_perm:[1,1,1,1] row_mask:0xf bank_mask:0xf\n\t"
    "v_mul_f32_dpp v10, v38, v10 quad_perm:[2,2,2,2] row_mask:0xf bank_mask:0xf\n\t"
    "v_mul_f32_dpp v11, v38, v11 quad_perm:[3,3,3,3] row_mask:0xf bank_mask:0xf\n\t"
    "v_mul_f32_dpp v12, v38, v12 quad_perm:[0,0,0,0] row_mask:0xf bank_mask:0xf\n\t"
    "v_mul_f32_dpp v13, v38, v13 quad_perm:[1,1,1,1] row_mask:0xf bank_mask:0xf\n\t"
    "v_mul_f32_dpp v14, v38, v14 quad_perm:[2,2,2,2] row_mask:0xf bank_mask:0xf\n\t"
    "v_mul_f32_dpp v15, v38, v15 quad_perm:[3,3,3,3] row_mask:0xf bank_mask:0xf\n\t"
    ".Lr35_%=:\n\t"
    "v_fma_f32 v35, v35, s64, v34\n\t"
    "v_fma_f32 v36, v36, s64, v34\n\t"
    "v_exp_f32 v35, v35\n\t"
    "v_exp_f32 v36, v36\n\t"
    "s_nop 0\n\t"
    "v_cvt_pk_bf16_f32 v39, v35, v36\n\t"
    "v_dot2_f32_bf16 v33, v39, s68, v33\n\t"
    "s_waitcnt vmcnt(27)\n\t"
    "s_add_u32 s57, s33, 0x4000\n\t"
    "s_xor_b32 s57, s57, s80\n\t"
    "buffer_load_dwordx4 v[236:239], v1, s[44:47], s57 offen offset:0 nt\n\t"
    "v_mov_b32_dpp v16, v39 quad_perm:[0,1,2,3] row_mask:0xf bank_mask:0x1\n\t"
    "v_mov_b32_dpp v17, v39 row_ror:12 row_mask:0xf bank_mask:0x1\n\t"
    "v_mov_b32_dpp v18, v39 row_ror:8 row_mask:0xf bank_mask:0x1\n\t"
    "v_mov_b32_dpp v19, v39 row_ror:4 row_mask:0xf bank_mask:0x1\n\t"
    "v_mov_b32_dpp v20, v39 row_ror:4 row_mask:0xf bank_mask:0x2\n\t"
    "v_mov_b32_dpp v21, v39 quad_perm:[0,1,2,3] row_mask:0xf bank_mask:0x2\n\t"
    "v_mov_b32_dpp v22, v39 row_ror:12 row_mask:0xf bank_mask:0x2\n\t"
    "v_mov_b32_dpp v23, v39 row_ror:8 row_mask:0xf bank_mask:0x2\n\t"
    "v_mov_b32_dpp v24, v39 row_ror:8 row_mask:0xf bank_mask:0x4\n\t"
    "v_mov_b32_dpp v25, v39 row_ror:4 row_mask:0xf bank_mask:0x4\n\t"
    "v_mov_b32_dpp v26, v39 quad_perm:[0,1,2,3] row_mask:0xf bank_mask:0x4\n\t"
    "v_mov_b32_dpp v27, v39 row_ror:12 row_mask:0xf bank_mask:0x4\n\t"
    "v_mov_b32_dpp v28, v39 row_ror:12 row_mask:0xf bank_mask:0x8\n\t"
    "v_mov_b32_dpp v29, v39 row_ror:8 row_mask:0xf bank_mask:0x8\n\t"
    "v_mov_b32_dpp v30, v39 row_ror:4 row_mask:0xf bank_mask:0x8\n\t"
    "v_mov_b32_dpp v31, v39 quad_perm:[0,1,2,3] row_mask:0xf bank_mask:0x8\n\t"
    "v_mfma_f32_16x16x32_bf16 v[44:47], a[24:27], v[4:7], 0\n\t"
    "s_waitcnt vmcnt(27)\n\t"
    "s_add_u32 s57, s33, 0x5000\n\t"
    "s_xor_b32 s57, s57, s80\n\t"
    "buffer_load_dwordx4 v[232:235], v1, s[44:47], s57 offen offset:0 nt\n\t"
    "v_mfma_f32_16x16x32_bf16 v[48:51], a[28:31], v[4:7], 0\n\t"
    "s_waitcnt vmcnt(27)\n\t"
    "s_add_u32 s57, s33, 0x6000\n\t"
    "s_xor_b32 s57, s57, s80\n\t"
    "buffer_load_dwordx4 v[228:231], v1, s[44:47], s57 offen offset:0 nt\n\t"
    "v_mfma_f32_16x16x32_bf16 v[52:55], a[32:35], v[4:7], 0\n\t"
    "v_add_f32_dpp v40, v45, v44 row_ror:12 row_mask:0xf bank_mask:0xf\n\t"
    "v_add_f32_dpp v42, v47, v46 row_ror:12 row_mask:0xf bank_mask:0xf\n\t"
    "s_waitcnt vmcnt(27)\n\t"
    "s_add_u32 s57, s33, 0x7000\n\t"
    "s_xor_b32 s57, s57, s80\n\t"
    "buffer_load_dwordx4 v[224:227], v1, s[44:47], s57 offen offset:0 nt\n\t"
    "v_mfma_f32_16x16x32_bf16 v[44:47], a[36:39], v[4:7], 0\n\t"
    "v_add_f32_dpp v41, v50, v49 row_ror:12 row_mask:0xf bank_mask:0xf\n\t"
    "v_add_f32_dpp v43, v48, v51 row_ror:12 row_mask:0xf bank_mask:0xf\n\t"
    "v_add_f32_dpp v35, v42, v40 row_ror:8 row_mask:0xf bank_mask:0x1\n\t"
    "s_waitcnt vmcnt(27)\n\t"
    "s_add_u32 s33, s34, 0x38000\n\t"
    "s_and_b32 s33, s33, 0x7ffff\n\t"
    "s_add_u32 s33, s33, s56\n\t"
    "s_add_u32 s57, s33, 0\n\t"
    "s_xor_b32 s57, s57, s80\n\t"
    "buffer_load_dwordx4 v[120:123], v1, s[44:47], s57 offen offset:0 nt\n\t"
    "v_mfma_f32_16x16x32_bf16 v[48:51], a[40:43], v[4:7], 0\n\t"
    "v_add_f32_dpp v40, v55, v54 row_ror:12 row_mask:0xf bank_mask:0xf\n\t"
    "v_add_f32_dpp v42, v53, v52 row_ror:12 row_mask:0xf bank_mask:0xf\n\t"
    "v_add_f32_dpp v35, v43, v41 row_ror:8 row_mask:0xf bank_mask:0x2\n\t"
    "s_waitcnt vmcnt(27)\n\t"
    "s_add_u32 s57, s33, 0x1000\n\t"
    "s_xor_b32 s57, s57, s80\n\t"
    "buffer_load_dwordx4 v[124:127], v1, s[44:47], s57 offen offset:0 nt\n\t"
    "v_mfma_f32_16x16x32_bf16 v[52:55], a[44:47], v[4:7], 0\n\t"
    "v_add_f32_dpp v41, v44, v47 row_ror:12 row_mask:0xf bank_mask:0xf\n\t"
    "v_add_f32_dpp v43, v46, v45 row_ror:12 row_mask:0xf bank_mask:0xf\n\t"
    "v_add_f32_dpp v35, v42, v40 row_ror:8 row_mask:0xf bank_mask:0x4\n\t"
    "s_waitcnt vmcnt(27)\n\t"
    "s_add_u32 s57, s33, 0x2000\n\t"
    "s_xor_b32 s57, s57, s80\n\t"
    "buffer_load_dwordx4 v[176:179], v1, s[44:47], s57 offen offset:0 nt\n\t"
    "v_mfma_f32_16x16x32_bf16 v[44:47], a[48:51], v[4:7], 0\n\t"
    "v_add_f32_dpp v40, v49, v48 row_ror:12 row_mask:0xf bank_mask:0xf\n\t"
    "v_add_f32_dpp v42, v51, v50 row_ror:12 row_mask:0xf bank_mask:0xf\n\t"
    "v_add_f32_dpp v35, v43, v41 row_ror:8 row_mask:0xf bank_mask:0x8\n\t"
    "s_waitcnt vmcnt(27)\n\t"
    "s_add_u32 s57, s33, 0x3000\n\t"
    "s_xor_b32 s57, s57, s80\n\t"
    "buffer_load_dwordx4 v[180:183], v1, s[44:47], s57 offen offset:0 nt\n\t"
    "v_mfma_f32_16x16x32_bf16 v[48:51], a[52:55], v[4:7], 0\n\t"
    "v_add_f32_dpp v41, v54, v53 row_ror:12 row_mask:0xf bank_mask:0xf\n\t"
    "v_add_f32_dpp v43, v52, v55 row_ror:12 row_mask:0xf bank_mask:0xf\n\t"
    "v_add_f32_dpp v36, v42, v40 row_ror:8 row_mask:0xf bank_mask:0x1\n\t"
    "v_add_f32_dpp v40, v47, v46 row_ror:12 row_mask:0xf bank_mask:0xf\n\t"
    "v_add_f32_dpp v42, v45, v44 row_ror:12 row_mask:0xf bank_mask:0xf\n\t"
    "v_add_f32_dpp v36, v43, v41 row_ror:8 row_mask:0xf bank_mask:0x2\n\t"
    "s_nop 1\n\t"
    "v_add_f32_dpp v41, v48, v51 row_ror:12 row_mask:0xf bank_mask:0xf\n\t"
    "v_add_f32_dpp v43, v50, v49 row_ror:12 row_mask:0xf bank_mask:0xf\n\t"
    "v_add_f32_dpp v36, v42, v40 row_ror:8 row_mask:0xf bank_mask:0x4\n\t"
    "s_nop 0\n\t"
    "v_add_f32_dpp v36, v43, v41 row_ror:8 row_mask:0xf bank_mask:0x8\n\t"
    "s_waitcnt vmcnt(27)\n\t"
    "s_waitcnt vmcnt(26)\n\t"
    "s_waitcnt vmcnt(25)\n\t"
    "s_waitcnt vmcnt(24)\n\t"
    "s_waitcnt vmcnt(23)\n\t"
    "s_waitcnt vmcnt(22)\n\t"
    "s_waitcnt vmcnt(21)\n\t"
    "s_waitcnt vmcnt(20)\n\t"
    "v_perm_b32 v44, v220, v160, s66\n\t"
    "v_perm_b32 v45, v216, v164, s66\n\t"
    "v_perm_b32 v46, v212, v168, s66\n\t"
    "v_perm_b32 v47, v208, v172, s66\n\t"
    "v_perm_b32 v48, v220, v160, s67\n\t"
    "v_perm_b32 v49, v216, v164, s67\n\t"
    "v_perm_b32 v50, v212, v168, s67\n\t"
    "v_perm_b32 v51, v208, v172, s67\n\t"
    "v_max_f32 v37, v35, v36\n\t"
    "v_mfma_f32_16x16x32_bf16 v[8:11], v[16:19], v[44:47], v[8:11]\n\t"
    "v_perm_b32 v52, v221, v161, s66\n\t"
    "v_perm_b32 v53, v217, v165, s66\n\t"
    "v_perm_b32 v54, v213, v169, s66\n\t"
    "v_perm_b32 v55, v209, v173, s66\n\t"
    "v_max_f32_dpp v37, v37, v37 row_ror:4 row_mask:0xf bank_mask:0xf\n\t"
    "v_mfma_f32_16x16x32_bf16 v[8:11], v[20:23], v[48:51], v[8:11]\n\t"
    "v_perm_b32 v44, v221, v161, s67\n\t"
    "v_perm_b32 v45, v217, v165, s67\n\t"
    "v_perm_b32 v46, v213, v169, s67\n\t"
    "v_perm_b32 v47, v209, v173, s67\n\t"
    "v_max_f32_dpp v37, v37, v37 row_ror:8 row_mask:0xf bank_mask:0xf\n\t"
    "v_mfma_f32_16x16x32_bf16 v[8:11], v[24:27], v[52:55], v[8:11]\n\t"
    "v_perm_b32 v48, v222, v162, s66\n\t"
    "v_perm_b32 v49, v218, v166, s66\n\t"
    "v_perm_b32 v50, v214, v170, s66\n\t"
    "v_perm_b32 v51, v210, v174, s66\n\t"
    "v_mov_b32 v38, v37\n\t"
    "v_mfma_f32_16x16x32_bf16 v[8:11], v[28:31], v[44:47], v[8:11]\n\t"
    "v_perm_b32 v52, v222, v162, s67\n\t"
    "v_perm_b32 v53, v218, v166, s67\n\t"
    "v_perm_b32 v54, v214, v170, s67\n\t"
    "v_perm_b32 v55, v210, v174, s67\n\t"
    "v_permlane16_swap_b32 v37, v38\n\t"
    "v_mfma_f32_16x16x32_bf16 v[12:15], v[16:19], v[48:51], v[12:15]\n\t"
    "v_perm_b32 v44, v223, v163, s66\n\t"
    "v_perm_b32 v45, v219, v167, s66\n\t"
    "v_perm_b32 v46, v215, v171, s66\n\t"
    "v_perm_b32 v47, v211, v175, s66\n\t"
    "v_max_f32 v37, v37, v38\n\t"
    "v_mfma_f32_16x16x32_bf16 v[12:15], v[20:23], v[52:55], v[12:15]\n\t"
    "s_waitcnt vmcnt(19)\n\t"
    "s_add_u32 s57, s33, 0x4000\n\t"
    "s_xor_b32 s57, s57, s80\n\t"
    "buffer_load_dwordx4 v[184:187], v1, s[44:47], s57 offen offset:0 nt\n\t"
    "v_perm_b32 v48, v223, v163, s67\n\t"
    "v_perm_b32 v49, v219, v167, s67\n\t"
    "v_perm_b32 v50, v215, v171, s67\n\t"
    "v_perm_b32 v51, v211, v175, s67\n\t"
    "v_mov_b32 v38, v37\n\t"
    "v_mfma_f32_16x16x32_bf16 v[12:15], v[24:27], v[44:47], v[12:15]\n\t"
    "s_nop 0\n\t"
    "v_permlane32_swap_b32 v37, v38\n\t"
    "v_mfma_f32_16x16x32_bf16 v[12:15], v[28:31], v[48:51], v[12:15]\n\t"
    "v_max_f32 v37, v37, v38\n\t"
    "v_cmp_gt_f32 vcc, v37, v32\n\t"
    "s_cbranch_vccz .Lr36_%=\n\t"
    "v_max_f32 v37, v37, v32\n\t"
    "v_sub_f32 v38, v32, v37\n\t"
    "v_mul_f32 v38, s64, v38\n\t"
    "v_exp_f32 v38, v38\n\t"
    "v_mov_b32 v32, v37\n\t"
    "v_mul_f32 v34, s65, v37\n\t"
    "v_mul_f32 v33, v33, v38\n\t"
    "v_mul_f32_dpp v8, v38, v8 quad_perm:[0,0,0,0] row_mask:0xf bank_mask:0xf\n\t"
    "v_mul_f32_dpp v9, v38, v9 quad_perm:[1,1,1,1] row_mask:0xf bank_mask:0xf\n\t"
    "v_mul_f32_dpp v10, v38, v10 quad_perm:[2,2,2,2] row_mask:0xf bank_mask:0xf\n\t"
    "v_mul_f32_dpp v11, v38, v11 quad_perm:[3,3,3,3] row_mask:0xf bank_mask:0xf\n\t"
    "v_mul_f32_dpp v12, v38, v12 quad_perm:[0,0,0,0] row_mask:0xf bank_mask:0xf\n\t"
    "v_mul_f32_dpp v13, v38, v13 quad_perm:[1,1,1,1] row_mask:0xf bank_mask:0xf\n\t"
    "v_mul_f32_dpp v14, v38, v14 quad_perm:[2,2,2,2] row_mask:0xf bank_mask:0xf\n\t"
    "v_mul_f32_dpp v15, v38, v15 quad_perm:[3,3,3,3] row_mask:0xf bank_mask:0xf\n\t"
    ".Lr36_%=:\n\t"
    "v_fma_f32 v35, v35, s64, v34\n\t"
    "v_fma_f32 v36, v36, s64, v34\n\t"
    "v_exp_f32 v35, v35\n\t"
    "v_exp_f32 v36, v36\n\t"
    "s_nop 0\n\t"
    "v_cvt_pk_bf16_f32 v39, v35, v36\n\t"
    "v_dot2_f32_bf16 v33, v39, s68, v33\n\t"
    "s_nop 0\n\t"
    "v_mov_b32_dpp v16, v39 quad_perm:[0,1,2,3] row_mask:0xf bank_mask:0x1\n\t"
    "v_mov_b32_dpp v17, v39 row_ror:12 row_mask:0xf bank_mask:0x1\n\t"
    "v_mov_b32_dpp v18, v39 row_ror:8 row_mask:0xf bank_mask:0x1\n\t"
    "v_mov_b32_dpp v19, v39 row_ror:4 row_mask:0xf bank_mask:0x1\n\t"
    "v_mov_b32_dpp v20, v39 row_ror:4 row_mask:0xf bank_mask:0x2\n\t"
    "v_mov_b32_dpp v21, v39 quad_perm:[0,1,2,3] row_mask:0xf bank_mask:0x2\n\t"
    "v_mov_b32_dpp v22, v39 row_ror:12 row_mask:0xf bank_mask:0x2\n\t"
    "v_mov_b32_dpp v23, v39 row_ror:8 row_mask:0xf bank_mask:0x2\n\t"
    "v_mov_b32_dpp v24, v39 row_ror:8 row_mask:0xf bank_mask:0x4\n\t"
    "v_mov_b32_dpp v25, v39 row_ror:4 row_mask:0xf bank_mask:0x4\n\t"
    "v_mov_b32_dpp v26, v39 quad_perm:[0,1,2,3] row_mask:0xf bank_mask:0x4\n\t"
    "v_mov_b32_dpp v27, v39 row_ror:12 row_mask:0xf bank_mask:0x4\n\t"
    "v_mov_b32_dpp v28, v39 row_ror:12 row_mask:0xf bank_mask:0x8\n\t"
    "v_mov_b32_dpp v29, v39 row_ror:8 row_mask:0xf bank_mask:0x8\n\t"
    "v_mov_b32_dpp v30, v39 row_ror:4 row_mask:0xf bank_mask:0x8\n\t"
    "v_mov_b32_dpp v31, v39 quad_perm:[0,1,2,3] row_mask:0xf bank_mask:0x8\n\t"
    "v_mfma_f32_16x16x32_bf16 v[44:47], a[56:59], v[4:7], 0\n\t"
    "s_waitcnt vmcnt(19)\n\t"
    "s_add_u32 s57, s33, 0x5000\n\t"
    "s_xor_b32 s57, s57, s80\n\t"
    "buffer_load_dwordx4 v[188:191], v1, s[44:47], s57 offen offset:0 nt\n\t"
    "v_mfma_f32_16x16x32_bf16 v[48:51], a[60:63], v[4:7], 0\n\t"
    "s_waitcnt vmcnt(19)\n\t"
    "s_add_u32 s57, s33, 0x6000\n\t"
    "s_xor_b32 s57, s57, s80\n\t"
    "buffer_load_dwordx4 v[192:195], v1, s[44:47], s57 offen offset:0 nt\n\t"
    "v_mfma_f32_16x16x32_bf16 v[52:55], a[184:187], v[4:7], 0\n\t"
    "v_add_f32_dpp v40, v45, v44 row_ror:12 row_mask:0xf bank_mask:0xf\n\t"
    "v_add_f32_dpp v42, v47, v46 row_ror:12 row_mask:0xf bank_mask:0xf\n\t"
    "s_waitcnt vmcnt(19)\n\t"
    "s_add_u32 s57, s33, 0x7000\n\t"
    "s_xor_b32 s57, s57, s80\n\t"
    "buffer_load_dwordx4 v[196:199], v1, s[44:47], s57 offen offset:0 nt\n\t"
    "v_mfma_f32_16x16x32_bf16 v[44:47], a[188:191], v[4:7], 0\n\t"
    "v_add_f32_dpp v41, v50, v49 row_ror:12 row_mask:0xf bank_mask:0xf\n\t"
    "v_add_f32_dpp v43, v48, v51 row_ror:12 row_mask:0xf bank_mask:0xf\n\t"
    "v_add_f32_dpp v35, v42, v40 row_ror:8 row_mask:0xf bank_mask:0x1\n\t"
    "s_waitcnt vmcnt(19)\n\t"
    "s_add_u32 s33, s34, 0x10000\n\t"
    "s_and_b32 s33, s33, 0x7ffff\n\t"
    "s_add_u32 s33, s33, s56\n\t"
    "s_add_u32 s57, s33, 0\n\t"
    "s_xor_b32 s57, s57, s80\n\t"
    "buffer_load_dwordx4 v[200:203], v1, s[44:47], s57 offen offset:0 nt\n\t"
    "v_mfma_f32_16x16x32_bf16 v[48:51], a[192:195], v[4:7], 0\n\t"
    "v_add_f32_dpp v40, v55, v54 row_ror:12 row_mask:0xf bank_mask:0xf\n\t"
    "v_add_f32_dpp v42, v53, v52 row_ror:12 row_mask:0xf bank_mask:0xf\n\t"
    "v_add_f32_dpp v35, v43, v41 row_ror:8 row_mask:0xf bank_mask:0x2\n\t"
    "s_waitcnt vmcnt(19)\n\t"
    "s_add_u32 s57, s33, 0x1000\n\t"
    "s_xor_b32 s57, s57, s80\n\t"
    "buffer_load_dwordx4 v[204:207], v1, s[44:47], s57 offen offset:0 nt\n\t"
    "v_mfma_f32_16x16x32_bf16 v[52:55], a[196:199], v[4:7], 0\n\t"
    "v_add_f32_dpp v41, v44, v47 row_ror:12 row_mask:0xf bank_mask:0xf\n\t"
    "v_add_f32_dpp v43, v46, v45 row_ror:12 row_mask:0xf bank_mask:0xf\n\t"
    "v_add_f32_dpp v35, v42, v40 row_ror:8 row_mask:0xf bank_mask:0x4\n\t"
    "s_waitcnt vmcnt(19)\n\t"
    "s_add_u32 s57, s33, 0x2000\n\t"
    "s_xor_b32 s57, s57, s80\n\t"
    "buffer_load_dwordx4 v[56:59], v1, s[44:47], s57 offen offset:0 nt\n\t"
    "v_mfma_f32_16x16x32_bf16 v[44:47], a[200:203], v[4:7], 0\n\t"
    "v_add_f32_dpp v40, v49, v48 row_ror:12 row_mask:0xf bank_mask:0xf\n\t"
    "v_add_f32_dpp v42, v51, v50 row_ror:12 row_mask:0xf bank_mask:0xf\n\t"
    "v_add_f32_dpp v35, v43, v41 row_ror:8 row_mask:0xf bank_mask:0x8\n\t"
    "s_waitcnt vmcnt(19)\n\t"
    "s_add_u32 s57, s33, 0x3000\n\t"
    "s_xor_b32 s57, s57, s80\n\t"
    "buffer_load_dwordx4 v[60:63], v1, s[44:47], s57 offen offset:0 nt\n\t"
    "v_mfma_f32_16x16x32_bf16 v[48:51], a[204:207], v[4:7], 0\n\t"
    "v_add_f32_dpp v41, v54, v53 row_ror:12 row_mask:0xf bank_mask:0xf\n\t"
    "v_add_f32_dpp v43, v52, v55 row_ror:12 row_mask:0xf bank_mask:0xf\n\t"
    "v_add_f32_dpp v36, v42, v40 row_ror:8 row_mask:0xf bank_mask:0x1\n\t"
    "v_add_f32_dpp v40, v47, v46 row_ror:12 row_mask:0xf bank_mask:0xf\n\t"
    "v_add_f32_dpp v42, v45, v44 row_ror:12 row_mask:0xf bank_mask:0xf\n\t"
    "v_add_f32_dpp v36, v43, v41 row_ror:8 row_mask:0xf bank_mask:0x2\n\t"
    "s_nop 1\n\t"
    "v_add_f32_dpp v41, v48, v51 row_ror:12 row_mask:0xf bank_mask:0xf\n\t"
    "v_add_f32_dpp v43, v50, v49 row_ror:12 row_mask:0xf bank_mask:0xf\n\t"
    "v_add_f32_dpp v36, v42, v40 row_ror:8 row_mask:0xf bank_mask:0x4\n\t"
    "s_nop 0\n\t"
    "v_add_f32_dpp v36, v43, v41 row_ror:8 row_mask:0xf bank_mask:0x8\n\t"
    "s_waitcnt vmcnt(19)\n\t"
    "s_add_u32 s57, s33, 0x4000\n\t"
    "s_xor_b32 s57, s57, s80\n\t"
    "buffer_load_dwordx4 v[64:67], v1, s[44:47], s57 offen offset:0 nt\n\t"
    "s_waitcnt vmcnt(19)\n\t"
    "s_add_u32 s57, s33, 0x5000\n\t"
    "s_xor_b32 s57, s57, s80\n\t"
    "buffer_load_dwordx4 v[68:71], v1, s[44:47], s57 offen offset:0 nt\n\t"
    "s_waitcnt vmcnt(19)\n\t"
    "s_add_u32 s57, s33, 0x6000\n\t"
    "s_xor_b32 s57, s57, s80\n\t"
    "buffer_load_dwordx4 v[72:75], v1, s[44:47], s57 offen offset:0 nt\n\t"
    "s_waitcnt vmcnt(19)\n\t"
    "s_add_u32 s57, s33, 0x7000\n\t"
    "s_xor_b32 s57, s57, s80\n\t"
    "buffer_load_dwordx4 v[76:79], v1, s[44:47], s57 offen offset:0 nt\n\t"
    "s_waitcnt vmcnt(19)\n\t"
    "s_waitcnt vmcnt(18)\n\t"
    "s_waitcnt vmcnt(17)\n\t"
    "s_waitcnt vmcnt(16)\n\t"
    "v_perm_b32 v44, v236, v252, s66\n\t"
    "v_perm_b32 v45, v232, v248, s66\n\t"
    "v_perm_b32 v46, v228, v244, s66\n\t"
    "v_perm_b32 v47, v224, v240, s66\n\t"
    "v_perm_b32 v48, v236, v252, s67\n\t"
    "v_perm_b32 v49, v232, v248, s67\n\t"
    "v_perm_b32 v50, v228, v244, s67\n\t"
    "v_perm_b32 v51, v224, v240, s67\n\t"
    "v_max_f32 v37, v35, v36\n\t"
    "v_mfma_f32_16x16x32_bf16 v[8:11], v[16:19], v[44:47], v[8:11]\n\t"
    "v_perm_b32 v52, v237, v253, s66\n\t"
    "v_perm_b32 v53, v233, v249, s66\n\t"
    "v_perm_b32 v54, v229, v245, s66\n\t"
    "v_perm_b32 v55, v225, v241, s66\n\t"
    "v_max_f32_dpp v37, v37, v37 row_ror:4 row_mask:0xf bank_mask:0xf\n\t"
    "v_mfma_f32_16x16x32_bf16 v[8:11], v[20:23], v[48:51], v[8:11]\n\t"
    "v_perm_b32 v44, v237, v253, s67\n\t"
    "v_perm_b32 v45, v233, v249, s67\n\t"
    "v_perm_b32 v46, v229, v245, s67\n\t"
    "v_perm_b32 v47, v225, v241, s67\n\t"
    "v_max_f32_dpp v37, v37, v37 row_ror:8 row_mask:0xf bank_mask:0xf\n\t"
    "v_mfma_f32_16x16x32_bf16 v[8:11], v[24:27], v[52:55], v[8:11]\n\t"
    "v_perm_b32 v48, v238, v254, s66\n\t"
    "v_perm_b32 v49, v234, v250, s66\n\t"
    "v_perm_b32 v50, v230, v246, s66\n\t"
    "v_perm_b32 v51, v226, v242, s66\n\t"
    "v_mov_b32 v38, v37\n\t"
    "v_mfma_f32_16x16x32_bf16 v[8:11], v[28:31], v[44:47], v[8:11]\n\t"
    "v_perm_b32 v52, v238, v254, s67\n\t"
    "v_perm_b32 v53, v234, v250, s67\n\t"
    "v_perm_b32 v54, v230, v246, s67\n\t"
    "v_perm_b32 v55, v226, v242, s67\n\t"
    "v_permlane16_swap_b32 v37, v38\n\t"
    "v_mfma_f32_16x16x32_bf16 v[12:15], v[16:19], v[48:51], v[12:15]\n\t"
    "v_perm_b32 v44, v239, v255, s66\n\t"
    "v_perm_b32 v45, v235, v251, s66\n\t"
    "v_perm_b32 v46, v231, v247, s66\n\t"
    "v_perm_b32 v47, v227, v243, s66\n\t"
    "v_max_f32 v37, v37, v38\n\t"
    "v_mfma_f32_16x16x32_bf16 v[12:15], v[20:23], v[52:55], v[12:15]\n\t"
    "v_perm_b32 v48, v239, v255, s67\n\t"
    "v_perm_b32 v49, v235, v251, s67\n\t"
    "v_perm_b32 v50, v231, v247, s67\n\t"
    "v_perm_b32 v51, v227, v243, s67\n\t"
    "v_mov_b32 v38, v37\n\t"
    "v_mfma_f32_16x16x32_bf16 v[12:15], v[24:27], v[44:47], v[12:15]\n\t"
    "s_nop 0\n\t"
    "v_permlane32_swap_b32 v37, v38\n\t"
    "v_mfma_f32_16x16x32_bf16 v[12:15], v[28:31], v[48:51], v[12:15]\n\t"
    "v_max_f32 v37, v37, v38\n\t"
    "v_cmp_gt_f32 vcc, v37, v32\n\t"
    "s_cbranch_vccz .Lr37_%=\n\t"
    "v_max_f32 v37, v37, v32\n\t"
    "v_sub_f32 v38, v32, v37\n\t"
    "v_mul_f32 v38, s64, v38\n\t"
    "v_exp_f32 v38, v38\n\t"
    "v_mov_b32 v32, v37\n\t"
    "v_mul_f32 v34, s65, v37\n\t"
    "v_mul_f32 v33, v33, v38\n\t"
    "v_mul_f32_dpp v8, v38, v8 quad_perm:[0,0,0,0] row_mask:0xf bank_mask:0xf\n\t"
    "v_mul_f32_dpp v9, v38, v9 quad_perm:[1,1,1,1] row_mask:0xf bank_mask:0xf\n\t"
    "v_mul_f32_dpp v10, v38, v10 quad_perm:[2,2,2,2] row_mask:0xf bank_mask:0xf\n\t"
    "v_mul_f32_dpp v11, v38, v11 quad_perm:[3,3,3,3] row_mask:0xf bank_mask:0xf\n\t"
    "v_mul_f32_dpp v12, v38, v12 quad_perm:[0,0,0,0] row_mask:0xf bank_mask:0xf\n\t"
    "v_mul_f32_dpp v13, v38, v13 quad_perm:[1,1,1,1] row_mask:0xf bank_mask:0xf\n\t"
    "v_mul_f32_dpp v14, v38, v14 quad_perm:[2,2,2,2] row_mask:0xf bank_mask:0xf\n\t"
    "v_mul_f32_dpp v15, v38, v15 quad_perm:[3,3,3,3] row_mask:0xf bank_mask:0xf\n\t"
    ".Lr37_%=:\n\t"
    "v_fma_f32 v35, v35, s64, v34\n\t"
    "v_fma_f32 v36, v36, s64, v34\n\t"
    "v_exp_f32 v35, v35\n\t"
    "v_exp_f32 v36, v36\n\t"
    "s_nop 0\n\t"
    "v_cvt_pk_bf16_f32 v39, v35, v36\n\t"
    "v_dot2_f32_bf16 v33, v39, s68, v33\n\t"
    "s_nop 0\n\t"
    "v_mov_b32_dpp v16, v39 quad_perm:[0,1,2,3] row_mask:0xf bank_mask:0x1\n\t"
    "v_mov_b32_dpp v17, v39 row_ror:12 row_mask:0xf bank_mask:0x1\n\t"
    "v_mov_b32_dpp v18, v39 row_ror:8 row_mask:0xf bank_mask:0x1\n\t"
    "v_mov_b32_dpp v19, v39 row_ror:4 row_mask:0xf bank_mask:0x1\n\t"
    "v_mov_b32_dpp v20, v39 row_ror:4 row_mask:0xf bank_mask:0x2\n\t"
    "v_mov_b32_dpp v21, v39 quad_perm:[0,1,2,3] row_mask:0xf bank_mask:0x2\n\t"
    "v_mov_b32_dpp v22, v39 row_ror:12 row_mask:0xf bank_mask:0x2\n\t"
    "v_mov_b32_dpp v23, v39 row_ror:8 row_mask:0xf bank_mask:0x2\n\t"
    "v_mov_b32_dpp v24, v39 row_ror:8 row_mask:0xf bank_mask:0x4\n\t"
    "v_mov_b32_dpp v25, v39 row_ror:4 row_mask:0xf bank_mask:0x4\n\t"
    "v_mov_b32_dpp v26, v39 quad_perm:[0,1,2,3] row_mask:0xf bank_mask:0x4\n\t"
    "v_mov_b32_dpp v27, v39 row_ror:12 row_mask:0xf bank_mask:0x4\n\t"
    "v_mov_b32_dpp v28, v39 row_ror:12 row_mask:0xf bank_mask:0x8\n\t"
    "v_mov_b32_dpp v29, v39 row_ror:8 row_mask:0xf bank_mask:0x8\n\t"
    "v_mov_b32_dpp v30, v39 row_ror:4 row_mask:0xf bank_mask:0x8\n\t"
    "v_mov_b32_dpp v31, v39 quad_perm:[0,1,2,3] row_mask:0xf bank_mask:0x8\n\t"
    "ds_read_b128 a[208:211], v2 offset:8192\n\t"
    "ds_read_b128 a[212:215], v2 offset:9216\n\t"
    "ds_read_b128 a[0:3], v2 offset:10240\n\t"
    "ds_read_b128 a[4:7], v2 offset:11264\n\t"
    "ds_read_b128 a[8:11], v2 offset:12288\n\t"
    "ds_read_b128 a[12:15], v2 offset:13312\n\t"
    "ds_read_b128 a[16:19], v2 offset:14336\n\t"
    "ds_read_b128 a[20:23], v2 offset:15360\n\t"
    "s_waitcnt lgkmcnt(7)\n\t"
    "v_mfma_f32_16x16x32_bf16 v[44:47], a[208:211], v[4:7], 0\n\t"
    "s_waitcnt lgkmcnt(6)\n\t"
    "v_mfma_f32_16x16x32_bf16 v[48:51], a[212:215], v[4:7], 0\n\t"
    "s_waitcnt lgkmcnt(5)\n\t"
    "v_mfma_f32_16x16x32_bf16 v[52:55], a[0:3], v[4:7], 0\n\t"
    "s_nop 3\n\t"
    "v_add_f32_dpp v40, v45, v44 row_ror:12 row_mask:0xf bank_mask:0xf\n\t"
    "v_add_f32_dpp v42, v47, v46 row_ror:12 row_mask:0xf bank_mask:0xf\n\t"
    "s_waitcnt lgkmcnt(4)\n\t"
    "v_mfma_f32_16x16x32_bf16 v[44:47], a[4:7], v[4:7], 0\n\t"
    "v_add_f32_dpp v41, v50, v49 row_ror:12 row_mask:0xf bank_mask:0xf\n\t"
    "v_add_f32_dpp v43, v48, v51 row_ror:12 row_mask:0xf bank_mask:0xf\n\t"
    "v_add_f32_dpp v35, v42, v40 row_ror:8 row_mask:0xf bank_mask:0x1\n\t"
    "s_waitcnt lgkmcnt(3)\n\t"
    "v_mfma_f32_16x16x32_bf16 v[48:51], a[8:11], v[4:7], 0\n\t"
    "v_add_f32_dpp v40, v55, v54 row_ror:12 row_mask:0xf bank_mask:0xf\n\t"
    "v_add_f32_dpp v42, v53, v52 row_ror:12 row_mask:0xf bank_mask:0xf\n\t"
    "v_add_f32_dpp v35, v43, v41 row_ror:8 row_mask:0xf bank_mask:0x2\n\t"
    "s_waitcnt lgkmcnt(2)\n\t"
    "v_mfma_f32_16x16x32_bf16 v[52:55], a[12:15], v[4:7], 0\n\t"
    "v_add_f32_dpp v41, v44, v47 row_ror:12 row_mask:0xf bank_mask:0xf\n\t"
    "v_add_f32_dpp v43, v46, v45 row_ror:12 row_mask:0xf bank_mask:0xf\n\t"
    "v_add_f32_dpp v35, v42, v40 row_ror:8 row_mask:0xf bank_mask:0x4\n\t"
    "s_waitcnt lgkmcnt(1)\n\t"
    "v_mfma_f32_16x16x32_bf16 v[44:47], a[16:19], v[4:7], 0\n\t"
    "v_add_f32_dpp v40, v49, v48 row_ror:12 row_mask:0xf bank_mask:0xf\n\t"
    "v_add_f32_dpp v42, v51, v50 row_ror:12 row_mask:0xf bank_mask:0xf\n\t"
    "v_add_f32_dpp v35, v43, v41 row_ror:8 row_mask:0xf bank_mask:0x8\n\t"
    "s_waitcnt lgkmcnt(0)\n\t"
    "v_mfma_f32_16x16x32_bf16 v[48:51], a[20:23], v[4:7], 0\n\t"
    "v_add_f32_dpp v41, v54, v53 row_ror:12 row_mask:0xf bank_mask:0xf\n\t"
    "v_add_f32_dpp v43, v52, v55 row_ror:12 row_mask:0xf bank_mask:0xf\n\t"
    "v_add_f32_dpp v36, v42, v40 row_ror:8 row_mask:0xf bank_mask:0x1\n\t"
    "v_add_f32_dpp v40, v47, v46 row_ror:12 row_mask:0xf bank_mask:0xf\n\t"
    "v_add_f32_dpp v42, v45, v44 row_ror:12 row_mask:0xf bank_mask:0xf\n\t"
    "v_add_f32_dpp v36, v43, v41 row_ror:8 row_mask:0xf bank_mask:0x2\n\t"
    "s_nop 1\n\t"
    "v_add_f32_dpp v41, v48, v51 row_ror:12 row_mask:0xf bank_mask:0xf\n\t"
    "v_add_f32_dpp v43, v50, v49 row_ror:12 row_mask:0xf bank_mask:0xf\n\t"
    "v_add_f32_dpp v36, v42, v40 row_ror:8 row_mask:0xf bank_mask:0x4\n\t"
    "s_nop 0\n\t"
    "v_add_f32_dpp v36, v43, v41 row_ror:8 row_mask:0xf bank_mask:0x8\n\t"
    "s_waitcnt vmcnt(15)\n\t"
    "s_waitcnt vmcnt(14)\n\t"
    "s_waitcnt vmcnt(13)\n\t"
    "s_waitcnt vmcnt(12)\n\t"
    "s_waitcnt vmcnt(11)\n\t"
    "s_waitcnt vmcnt(10)\n\t"
    "s_waitcnt vmcnt(9)\n\t"
    "s_waitcnt vmcnt(8)\n\t"
    "v_perm_b32 v44, v184, v120, s66\n\t"
    "v_perm_b32 v45, v188, v124, s66\n\t"
    "v_perm_b32 v46, v192, v176, s66\n\t"
    "v_perm_b32 v47, v196, v180, s66\n\t"
    "v_perm_b32 v48, v184, v120, s67\n\t"
    "v_perm_b32 v49, v188, v124, s67\n\t"
    "v_perm_b32 v50, v192, v176, s67\n\t"
    "v_perm_b32 v51, v196, v180, s67\n\t"
    "v_max_f32 v37, v35, v36\n\t"
    "v_mfma_f32_16x16x32_bf16 v[8:11], v[16:19], v[44:47], v[8:11]\n\t"
    "v_perm_b32 v52, v185, v121, s66\n\t"
    "v_perm_b32 v53, v189, v125, s66\n\t"
    "v_perm_b32 v54, v193, v177, s66\n\t"
    "v_perm_b32 v55, v197, v181, s66\n\t"
    "v_max_f32_dpp v37, v37, v37 row_ror:4 row_mask:0xf bank_mask:0xf\n\t"
    "v_mfma_f32_16x16x32_bf16 v[8:11], v[20:23], v[48:51], v[8:11]\n\t"
    "v_perm_b32 v44, v185, v121, s67\n\t"
    "v_perm_b32 v45, v189, v125, s67\n\t"
    "v_perm_b32 v46, v193, v177, s67\n\t"
    "v_perm_b32 v47, v197, v181, s67\n\t"
    "v_max_f32_dpp v37, v37, v37 row_ror:8 row_mask:0xf bank_mask:0xf\n\t"
    "v_mfma_f32_16x16x32_bf16 v[8:11], v[24:27], v[52:55], v[8:11]\n\t"
    "v_perm_b32 v48, v186, v122, s66\n\t"
    "v_perm_b32 v49, v190, v126, s66\n\t"
    "v_perm_b32 v50, v194, v178, s66\n\t"
    "v_perm_b32 v51, v198, v182, s66\n\t"
    "v_mov_b32 v38, v37\n\t"
    "v_mfma_f32_16x16x32_bf16 v[8:11], v[28:31], v[44:47], v[8:11]\n\t"
    "v_perm_b32 v52, v186, v122, s67\n\t"
    "v_perm_b32 v53, v190, v126, s67\n\t"
    "v_perm_b32 v54, v194, v178, s67\n\t"
    "v_perm_b32 v55, v198, v182, s67\n\t"
    "v_permlane16_swap_b32 v37, v38\n\t"
    "v_mfma_f32_16x16x32_bf16 v[12:15], v[16:19], v[48:51], v[12:15]\n\t"
    "v_perm_b32 v44, v187, v123, s66\n\t"
    "v_perm_b32 v45, v191, v127, s66\n\t"
    "v_perm_b32 v46, v195, v179, s66\n\t"
    "v_perm_b32 v47, v199, v183, s66\n\t"
    "v_max_f32 v37, v37, v38\n\t"
    "v_mfma_f32_16x16x32_bf16 v[12:15], v[20:23], v[52:55], v[12:15]\n\t"
    "v_perm_b32 v48, v187, v123, s67\n\t"
    "v_perm_b32 v49, v191, v127, s67\n\t"
    "v_perm_b32 v50, v195, v179, s67\n\t"
    "v_perm_b32 v51, v199, v183, s67\n\t"
    "v_mov_b32 v38, v37\n\t"
    "v_mfma_f32_16x16x32_bf16 v[12:15], v[24:27], v[44:47], v[12:15]\n\t"
    "s_nop 0\n\t"
    "v_permlane32_swap_b32 v37, v38\n\t"
    "v_mfma_f32_16x16x32_bf16 v[12:15], v[28:31], v[48:51], v[12:15]\n\t"
    "v_max_f32 v37, v37, v38\n\t"
    "v_cmp_gt_f32 vcc, v37, v32\n\t"
    "s_cbranch_vccz .Lr38_%=\n\t"
    "v_max_f32 v37, v37, v32\n\t"
    "v_sub_f32 v38, v32, v37\n\t"
    "v_mul_f32 v38, s64, v38\n\t"
    "v_exp_f32 v38, v38\n\t"
    "v_mov_b32 v32, v37\n\t"
    "v_mul_f32 v34, s65, v37\n\t"
    "v_mul_f32 v33, v33, v38\n\t"
    "v_mul_f32_dpp v8, v38, v8 quad_perm:[0,0,0,0] row_mask:0xf bank_mask:0xf\n\t"
    "v_mul_f32_dpp v9, v38, v9 quad_perm:[1,1,1,1] row_mask:0xf bank_mask:0xf\n\t"
    "v_mul_f32_dpp v10, v38, v10 quad_perm:[2,2,2,2] row_mask:0xf bank_mask:0xf\n\t"
    "v_mul_f32_dpp v11, v38, v11 quad_perm:[3,3,3,3] row_mask:0xf bank_mask:0xf\n\t"
    "v_mul_f32_dpp v12, v38, v12 quad_perm:[0,0,0,0] row_mask:0xf bank_mask:0xf\n\t"
    "v_mul_f32_dpp v13, v38, v13 quad_perm:[1,1,1,1] row_mask:0xf bank_mask:0xf\n\t"
    "v_mul_f32_dpp v14, v38, v14 quad_perm:[2,2,2,2] row_mask:0xf bank_mask:0xf\n\t"
    "v_mul_f32_dpp v15, v38, v15 quad_perm:[3,3,3,3] row_mask:0xf bank_mask:0xf\n\t"
    ".Lr38_%=:\n\t"
    "v_fma_f32 v35, v35, s64, v34\n\t"
    "v_fma_f32 v36, v36, s64, v34\n\t"
    "v_exp_f32 v35, v35\n\t"
    "v_exp_f32 v36, v36\n\t"
    "s_nop 0\n\t"
    "v_cvt_pk_bf16_f32 v39, v35, v36\n\t"
    "v_dot2_f32_bf16 v33, v39, s68, v33\n\t"
    "s_nop 0\n\t"
    "v_mov_b32_dpp v16, v39 quad_perm:[0,1,2,3] row_mask:0xf bank_mask:0x1\n\t"
    "v_mov_b32_dpp v17, v39 row_ror:12 row_mask:0xf bank_mask:0x1\n\t"
    "v_mov_b32_dpp v18, v39 row_ror:8 row_mask:0xf bank_mask:0x1\n\t"
    "v_mov_b32_dpp v19, v39 row_ror:4 row_mask:0xf bank_mask:0x1\n\t"
    "v_mov_b32_dpp v20, v39 row_ror:4 row_mask:0xf bank_mask:0x2\n\t"
    "v_mov_b32_dpp v21, v39 quad_perm:[0,1,2,3] row_mask:0xf bank_mask:0x2\n\t"
    "v_mov_b32_dpp v22, v39 row_ror:12 row_mask:0xf bank_mask:0x2\n\t"
    "v_mov_b32_dpp v23, v39 row_ror:8 row_mask:0xf bank_mask:0x2\n\t"
    "v_mov_b32_dpp v24, v39 row_ror:8 row_mask:0xf bank_mask:0x4\n\t"
    "v_mov_b32_dpp v25, v39 row_ror:4 row_mask:0xf bank_mask:0x4\n\t"
    "v_mov_b32_dpp v26, v39 quad_perm:[0,1,2,3] row_mask:0xf bank_mask:0x4\n\t"
    "v_mov_b32_dpp v27, v39 row_ror:12 row_mask:0xf bank_mask:0x4\n\t"
    "v_mov_b32_dpp v28, v39 row_ror:12 row_mask:0xf bank_mask:0x8\n\t"
    "v_mov_b32_dpp v29, v39 row_ror:8 row_mask:0xf bank_mask:0x8\n\t"
    "v_mov_b32_dpp v30, v39 row_ror:4 row_mask:0xf bank_mask:0x8\n\t"
    "v_mov_b32_dpp v31, v39 quad_perm:[0,1,2,3] row_mask:0xf bank_mask:0x8\n\t"
    "v_mbcnt_lo_u32_b32 v3, -1, 0\n\t"
    "v_mbcnt_hi_u32_b32 v3, -1, v3\n\t"
    "v_add_f32_dpp v33, v33, v33 row_ror:4 row_mask:0xf bank_mask:0xf\n\t"
    "v_add_u32 v37, s60, v3\n\t"
    "v_and_b32 v37, 3, v37\n\t"
    "v_add_f32_dpp v33, v33, v33 row_ror:8 row_mask:0xf bank_mask:0xf\n\t"
    "v_and_b32 v39, 3, v3\n\t"
    "v_lshlrev_b32 v39, 3, v39\n\t"
    "v_lshl_add_u32 v37, v37, 5, v39\n\t"
    "v_mov_b32 v38, v33\n\t"
    "s_nop 1\n\t"
    "v_permlane16_swap_b32 v33, v38\n\t"
    "v_add_f32 v33, v33, v38\n\t"
    "s_add_u32 s72, s60, 1\n\t"
    "s_and_b32 s72, s72, 3\n\t"
    "s_mul_i32 s72, s72, 0x600\n\t"
    "s_add_u32 s72, s72, 0xc000\n\t"
    "s_add_u32 s73, s60, 2\n\t"
    "s_and_b32 s73, s73, 3\n\t"
    "s_mul_i32 s73, s73, 0x600\n\t"
    "s_add_u32 s73, s73, 0xc200\n\t"
    "s_add_u32 s74, s60, 3\n\t"
    "s_and_b32 s74, s74, 3\n\t"
    "s_mul_i32 s74, s74, 0x600\n\t"
    "s_add_u32 s74, s74, 0xc400\n\t"
    "v_mov_b32 v38, v33\n\t"
    "s_nop 1\n\t"
    "v_permlane32_swap_b32 v33, v38\n\t"
    "v_add_f32 v33, v33, v38\n\t"
    "ds_write_b64 v37, v[32:33] offset:55296\n\t"
    "s_mul_i32 s75, s60, 0x600\n\t"
    "s_add_u32 s75, s75, 0xc000\n\t"
    "s_lshl_b32 s76, s60, 5\n\t"
    "s_add_u32 s76, s76, 0xd800\n\t"
    "v_mov_b32 v37, s76\n\t"
    "s_lshr_b32 s77, s59, 5\n\t"
    "s_lshl_b32 s77, s77, 2\n\t"
    "s_add_u32 s77, s77, s60\n\t"
    "s_lshl_b32 s77, s77, 13\n\t"
    "s_bfe_u32 s78, s59, 0x10004\n\t"
    "s_lshl_b32 s78, s78, 12\n\t"
    "s_add_u32 s77, s77, s78\n\t"
    "s_and_b32 s78, s59, 15\n\t"
    "s_lshl_b32 s78, s78, 6\n\t"
    "s_add_u32 s77, s77, s78\n\t"
    "s_add_u32 s77, s77, s62\n\t"
    "s_add_u32 s77, s77, 0x10000\n\t"
    "v_lshrrev_b32 v40, 4, v3\n\t"
    "v_lshlrev_b32 v40, 10, v40\n\t"
    "v_and_b32 v39, 15, v3\n\t"
    "v_lshl_add_u32 v40, v39, 2, v40\n\t"
    "s_lshl_b32 s78, s59, 7\n\t"
    "s_lshl_b32 s79, s60, 3\n\t"
    "s_add_u32 s78, s78, s79\n\t"
    "s_add_u32 s78, s78, s62\n\t"
    "s_add_u32 s78, s78, 0x2000\n\t"
    "v_cmp_eq_u32 vcc, 0, v3\n\t"
    "v_mov_b32 v41, 0x7fffff00\n\t"
    "s_nop 0\n\t"
    "v_cndmask_b32 v41, v41, v3, vcc\n\t"
    "s_waitcnt vmcnt(7)\n\t"
    "s_waitcnt vmcnt(6)\n\t"
    "s_waitcnt vmcnt(5)\n\t"
    "s_waitcnt vmcnt(4)\n\t"
    "s_waitcnt vmcnt(3)\n\t"
    "s_waitcnt vmcnt(2)\n\t"
    "s_waitcnt vmcnt(1)\n\t"
    "s_waitcnt vmcnt(0)\n\t"
    "v_perm_b32 v44, v64, v200, s66\n\t"
    "v_perm_b32 v45, v68, v204, s66\n\t"
    "v_perm_b32 v46, v72, v56, s66\n\t"
    "v_perm_b32 v47, v76, v60, s66\n\t"
    "v_perm_b32 v48, v64, v200, s67\n\t"
    "v_perm_b32 v49, v68, v204, s67\n\t"
    "v_perm_b32 v50, v72, v56, s67\n\t"
    "v_perm_b32 v51, v76, v60, s67\n\t"
    "v_mfma_f32_16x16x32_bf16 v[8:11], v[16:19], v[44:47], v[8:11]\n\t"
    "v_perm_b32 v52, v65, v201, s66\n\t"
    "v_perm_b32 v53, v69, v205, s66\n\t"
    "v_perm_b32 v54, v73, v57, s66\n\t"
    "v_perm_b32 v55, v77, v61, s66\n\t"
    "v_mfma_f32_16x16x32_bf16 v[8:11], v[20:23], v[48:51], v[8:11]\n\t"
    "v_perm_b32 v44, v65, v201, s67\n\t"
    "v_perm_b32 v45, v69, v205, s67\n\t"
    "v_perm_b32 v46, v73, v57, s67\n\t"
    "v_perm_b32 v47, v77, v61, s67\n\t"
    "v_mfma_f32_16x16x32_bf16 v[8:11], v[24:27], v[52:55], v[8:11]\n\t"
    "v_perm_b32 v48, v66, v202, s66\n\t"
    "v_perm_b32 v49, v70, v206, s66\n\t"
    "v_perm_b32 v50, v74, v58, s66\n\t"
    "v_perm_b32 v51, v78, v62, s66\n\t"
    "v_mfma_f32_16x16x32_bf16 v[8:11], v[28:31], v[44:47], v[8:11]\n\t"
    "v_perm_b32 v52, v66, v202, s67\n\t"
    "v_perm_b32 v53, v70, v206, s67\n\t"
    "v_perm_b32 v54, v74, v58, s67\n\t"
    "v_perm_b32 v55, v78, v62, s67\n\t"
    "v_mfma_f32_16x16x32_bf16 v[12:15], v[16:19], v[48:51], v[12:15]\n\t"
    "v_perm_b32 v44, v67, v203, s66\n\t"
    "v_perm_b32 v45, v71, v207, s66\n\t"
    "v_perm_b32 v46, v75, v59, s66\n\t"
    "v_perm_b32 v47, v79, v63, s66\n\t"
    "v_mfma_f32_16x16x32_bf16 v[12:15], v[20:23], v[52:55], v[12:15]\n\t"
    "v_perm_b32 v48, v67, v203, s67\n\t"
    "v_perm_b32 v49, v71, v207, s67\n\t"
    "v_perm_b32 v50, v75, v59, s67\n\t"
    "v_perm_b32 v51, v79, v63, s67\n\t"
    "v_mfma_f32_16x16x32_bf16 v[12:15], v[24:27], v[44:47], v[12:15]\n\t"
    "s_nop 0\n\t"
    "v_mfma_f32_16x16x32_bf16 v[12:15], v[28:31], v[48:51], v[12:15]\n\t"
    "s_nop 7\n\t"
    ".Lchain_%=:\n\t"
    "s_mov_b32 m0, s72\n\t"
    "s_nop 0\n\t"
    "ds_write_addtid_b32 v9\n\t"
    "ds_write_addtid_b32 v13 offset:256\n\t"
    "s_mov_b32 m0, s73\n\t"
    "s_nop 0\n\t"
    "ds_write_addtid_b32 v10\n\t"
    "ds_write_addtid_b32 v14 offset:256\n\t"
    "s_mov_b32 m0, s74\n\t"
    "s_nop 0\n\t"
    "ds_write_addtid_b32 v11\n\t"
    "ds_write_addtid_b32 v15 offset:256\n\t"
    "s_mov_b32 m0, s75\n\t"
    "s_waitcnt lgkmcnt(0)\n\t"
    "s_barrier\n\t"
    "ds_read_b128 v[44:47], v37\n\t"
    "ds_read_b128 v[48:51], v37 offset:16\n\t"
    "ds_read_addtid_b32 v16 offset:0\n\t"
    "ds_read_addtid_b32 v17 offset:256\n\t"
    "ds_read_addtid_b32 v18 offset:512\n\t"
    "ds_read_addtid_b32 v19 offset:768\n\t"
    "ds_read_addtid_b32 v20 offset:1024\n\t"
    "ds_read_addtid_b32 v21 offset:1280\n\t"
    "s_lshl_b32 s72, s60, 10\n\t"
    "v_lshlrev_b32 v0, 4, v3\n\t"
    "v_add_u32 v2, s72, v0\n\t"
    "v_bfe_u32 v4, v3, 2, 1\n\t"
    "v_lshlrev_b32 v1, 11, v4\n\t"
    "v_lshrrev_b32 v5, 4, v3\n\t"
    "v_lshl_add_u32 v1, v5, 9, v1\n\t"
    "v_and_b32 v4, 3, v3\n\t"
    "v_lshl_add_u32 v1, v4, 7, v1\n\t"
    "v_and_b32 v4, 15, v3\n\t"
    "v_lshlrev_b32 v0, 6, v4\n\t"
    "v_lshl_add_u32 v0, v5, 4, v0\n\t"
    "s_waitcnt lgkmcnt(6)\n\t"
    "v_max3_f32 v36, v44, v46, v48\n\t"
    "v_max_f32 v36, v36, v50\n\t"
    "v_mul_f32 v39, s65, v36\n\t"
    "v_fma_f32 v52, v44, s64, v39\n\t"
    "v_fma_f32 v53, v46, s64, v39\n\t"
    "v_fma_f32 v54, v48, s64, v39\n\t"
    "v_fma_f32 v55, v50, s64, v39\n\t"
    "v_exp_f32 v52, v52\n\t"
    "v_exp_f32 v53, v53\n\t"
    "v_exp_f32 v54, v54\n\t"
    "v_exp_f32 v55, v55\n\t"
    "v_mul_f32 v8, v52, v8\n\t"
    "v_mul_f32 v12, v52, v12\n\t"
    "s_waitcnt lgkmcnt(0)\n\t"
    "v_fmac_f32 v8, v53, v16\n\t"
    "v_fmac_f32 v12, v53, v17\n\t"
    "v_fmac_f32 v8, v54, v18\n\t"
    "v_fmac_f32 v12, v54, v19\n\t"
    "v_fmac_f32 v8, v55, v20\n\t"
    "v_fmac_f32 v12, v55, v21\n\t"
    "v_cvt_pk_bf16_f32 v8, v8, v12\n\t"
    "v_min_u32 v8, -2, v8\n\t"
    "buffer_store_dword v8, v40, s[48:51], s77 offen sc1\n\t"
    "v_mul_f32 v37, v52, v45\n\t"
    "v_fmac_f32 v37, v53, v47\n\t"
    "v_fmac_f32 v37, v54, v49\n\t"
    "v_fmac_f32 v37, v55, v51\n\t"
    "v_min_u32 v36, -2, v36\n\t"
    "v_min_u32 v37, -2, v37\n\t"
    "buffer_store_dwordx2 v[36:37], v41, s[48:51], s78 offen sc1\n\t"
    "s_nop 1\n\t"
    "ds_read_b128 v[36:39], v2 offset:16384\n\t"
    "ds_read_b128 v[40:43], v2 offset:20480\n\t"
    "ds_read_b128 v[44:47], v2 offset:24576\n\t"
    "ds_read_b128 v[48:51], v2 offset:28672\n\t"
    "s_lshr_b32 s72, s61, 5\n\t"
    "s_lshl_b32 s73, s72, 2\n\t"
    "s_add_u32 s73, s73, s60\n\t"
    "s_lshl_b32 s73, s73, 13\n\t"
    "s_add_u32 s73, s73, s62\n\t"
    "s_add_u32 s73, s73, 0x10000\n\t"
    "s_add_u32 s74, s73, 0x1000\n\t"
    "s_lshl_b32 s75, s72, 12\n\t"
    "s_lshl_b32 s76, s60, 3\n\t"
    "s_add_u32 s75, s75, s76\n\t"
    "s_add_u32 s75, s75, s62\n\t"
    "s_add_u32 s75, s75, 0x2000\n\t"
    "s_sleep 14\n\t"
    "s_mov_b32 s58, 0x100000\n\t"
    ".Lp39_%=:\n\t"
    "buffer_load_dwordx4 v[4:7], v0, s[48:51], s73 offen sc1\n\t"
    "buffer_load_dwordx4 v[8:11], v0, s[48:51], s73 offen offset:1024 sc1\n\t"
    "buffer_load_dwordx4 v[12:15], v0, s[48:51], s73 offen offset:2048 sc1\n\t"
    "buffer_load_dwordx4 v[16:19], v0, s[48:51], s73 offen offset:3072 sc1\n\t"
    "buffer_load_dwordx4 v[20:23], v0, s[48:51], s74 offen sc1\n\t"
    "buffer_load_dwordx4 v[24:27], v0, s[48:51], s74 offen offset:1024 sc1\n\t"
    "buffer_load_dwordx4 v[28:31], v0, s[48:51], s74 offen offset:2048 sc1\n\t"
    "buffer_load_dwordx4 v[32:35], v0, s[48:51], s74 offen offset:3072 sc1\n\t"
    "buffer_load_dwordx2 v[52:53], v1, s[48:51], s75 offen sc1\n\t"
    "s_waitcnt vmcnt(0)\n\t"
    ".Lp40_%=:\n\t"
    "v_max3_u32 v54, v4, v5, v6\n\t"
    "v_max3_u32 v54, v54, v7, v8\n\t"
    "v_max3_u32 v54, v54, v9, v10\n\t"
    "v_max3_u32 v54, v54, v11, v12\n\t"
    "v_max3_u32 v54, v54, v13, v14\n\t"
    "v_max3_u32 v54, v54, v15, v16\n\t"
    "v_max3_u32 v54, v54, v17, v18\n\t"
    "v_max3_u32 v54, v54, v19, v20\n\t"
    "v_max3_u32 v54, v54, v21, v22\n\t"
    "v_max3_u32 v54, v54, v23, v24\n\t"
    "v_max3_u32 v54, v54, v25, v26\n\t"
    "v_max3_u32 v54, v54, v27, v28\n\t"
    "v_max3_u32 v54, v54, v29, v30\n\t"
    "v_max3_u32 v54, v54, v31, v32\n\t"
    "v_max3_u32 v54, v54, v33, v34\n\t"
    "v_max3_u32 v54, v54, v35, v52\n\t"
    "v_max_u32 v54, v54, v53\n\t"
    "v_cmp_eq_u32 vcc, -1, v54\n\t"
    "s_cbranch_vccz .Lp41_%=\n\t"
    "s_mov_b64 exec, vcc\n\t"
    "s_sub_u32 s58, s58, 1\n\t"
    "s_cbranch_scc1 .Lp41_%=\n\t"
    "s_sleep 3\n\t"
    "s_branch .Lp39_%=\n\t"
    ".Lp41_%=:\n\t"
    "s_mov_b64 exec, -1\n\t"
    "s_waitcnt lgkmcnt(0)\n\t"
    "v_mov_b32 v0, v52\n\t"
    "v_mov_b32 v1, v53\n\t"
    "v_mfma_f32_16x16x32_bf16 v[52:55], v[4:7], v[36:39], 0\n\t"
    "v_mfma_f32_16x16x32_bf16 v[52:55], v[8:11], v[40:43], v[52:55]\n\t"
    "v_max_f32_dpp v8, v0, v0 quad_perm:[1,0,3,2] row_mask:0xf bank_mask:0xf\n\t"
    "v_mfma_f32_16x16x32_bf16 v[52:55], v[12:15], v[44:47], v[52:55]\n\t"
    "s_nop 0\n\t"
    "v_max_f32_dpp v8, v8, v8 quad_perm:[2,3,0,1] row_mask:0xf bank_mask:0xf\n\t"
    "v_mfma_f32_16x16x32_bf16 v[52:55], v[16:19], v[48:51], v[52:55]\n\t"
    "s_nop 0\n\t"
    "v_max_f32_dpp v8, v8, v8 row_half_mirror row_mask:0xf bank_mask:0xf\n\t"
    "v_mfma_f32_16x16x32_bf16 v[4:7], v[20:23], v[36:39], 0\n\t"
    "v_mov_b32 v9, v8\n\t"
    "v_mfma_f32_16x16x32_bf16 v[4:7], v[24:27], v[40:43], v[4:7]\n\t"
    "s_nop 0\n\t"
    "v_permlane16_swap_b32 v8, v9\n\t"
    "v_max_f32 v8, v8, v9\n\t"
    "v_mfma_f32_16x16x32_bf16 v[4:7], v[28:31], v[44:47], v[4:7]\n\t"
    "v_mov_b32 v9, v8\n\t"
    "v_mfma_f32_16x16x32_bf16 v[4:7], v[32:35], v[48:51], v[4:7]\n\t"
    "s_nop 0\n\t"
    "v_permlane32_swap_b32 v8, v9\n\t"
    "v_max_f32 v8, v8, v9\n\t"
    "v_sub_f32 v0, v0, v8\n\t"
    "v_mul_f32 v0, s64, v0\n\t"
    "v_exp_f32 v10, v0\n\t"
    "ds_read_b128 v[36:39], v2 offset:32768\n\t"
    "ds_read_b128 v[40:43], v2 offset:36864\n\t"
    "ds_read_b128 v[44:47], v2 offset:40960\n\t"
    "ds_read_b128 v[48:51], v2 offset:45056\n\t"
    "v_mul_f32 v11, v10, v1\n\t"
    "v_mul_f32_dpp v12, v10, v52 row_newbcast:0 row_mask:0xf bank_mask:0xf\n\t"
    "v_mul_f32_dpp v13, v10, v4 row_newbcast:4 row_mask:0xf bank_mask:0xf\n\t"
    "v_add_f32_dpp v11, v11, v11 quad_perm:[1,0,3,2] row_mask:0xf bank_mask:0xf\n\t"
    "v_fmac_f32_dpp v12, v10, v53 row_newbcast:1 row_mask:0xf bank_mask:0xf\n\t"
    "v_fmac_f32_dpp v13, v10, v5 row_newbcast:5 row_mask:0xf bank_mask:0xf\n\t"
    "v_add_f32_dpp v11, v11, v11 quad_perm:[2,3,0,1] row_mask:0xf bank_mask:0xf\n\t"
    "v_fmac_f32_dpp v12, v10, v54 row_newbcast:2 row_mask:0xf bank_mask:0xf\n\t"
    "v_fmac_f32_dpp v13, v10, v6 row_newbcast:6 row_mask:0xf bank_mask:0xf\n\t"
    "v_add_f32_dpp v11, v11, v11 row_half_mirror row_mask:0xf bank_mask:0xf\n\t"
    "v_fmac_f32_dpp v12, v10, v55 row_newbcast:3 row_mask:0xf bank_mask:0xf\n\t"
    "v_fmac_f32_dpp v13, v10, v7 row_newbcast:7 row_mask:0xf bank_mask:0xf\n\t"
    "v_add_f32 v12, v12, v13\n\t"
    "v_and_b32 v14, 15, v3\n\t"
    "v_lshlrev_b32 v14, 4, v14\n\t"
    "v_permlane32_swap_b32 v12, v11\n\t"
    "v_add_f32 v12, v12, v11\n\t"
    "v_mov_b32 v13, v12\n\t"
    "s_lshl_b32 s73, s60, 2\n\t"
    "s_add_u32 s73, s73, 0xd880\n\t"
    "v_add_u32 v15, s73, v14\n\t"
    "v_permlane16_swap_b32 v12, v13\n\t"
    "v_add_f32 v12, v12, v13\n\t"
    "v_mov_b32 v11, v12\n\t"
    "s_cmp_eq_u32 s60, 0\n\t"
    "s_cselect_b32 s82, 0xffff, 0\n\t"
    "s_mov_b32 s83, 0\n\t"
    "v_permlane32_swap_b32 v12, v11\n\t"
    "v_rcp_f32 v11, v11\n\t"
    "v_lshlrev_b32 v16, 2, v3\n\t"
    "v_mul_f32 v12, v12, v11\n\t"
    "ds_write_b32 v15, v12\n\t"
    "v_mov_b32 v17, 0x7fffff00\n\t"
    "v_cndmask_b32 v16, v17, v16, s[82:83]\n\t"
    "s_add_u32 s73, s61, 8\n\t"
    "s_and_b32 s73, s73, 31\n\t"
    "s_lshl_b32 s73, s73, 6\n\t"
    "s_lshr_b32 s74, s61, 5\n\t"
    "s_lshl_b32 s74, s74, 11\n\t"
    "s_add_u32 s73, s73, s74\n\t"
    "s_add_u32 s73, s73, s62\n\t"
    "s_add_u32 s73, s73, 0xa000\n\t"
    "s_waitcnt lgkmcnt(0)\n\t"
    "s_barrier\n\t"
    "ds_read_b128 v[20:23], v14 offset:55424\n\t"
    "s_waitcnt lgkmcnt(0)\n\t"
    "v_add_f32 v20, v20, v21\n\t"
    "v_add_f32 v22, v22, v23\n\t"
    "v_add_f32 v20, v20, v22\n\t"
    "v_min_u32 v20, -2, v20\n\t"
    "buffer_store_dword v20, v16, s[48:51], s73 offen sc1\n\t"
    "v_lshrrev_b32 v1, 1, v2\n\t"
    "v_lshrrev_b32 v53, 2, v2\n\t"
    "v_lshrrev_b32 v54, 10, v2\n\t"
    "v_lshlrev_b32 v54, 4, v54\n\t"
    "v_bfe_u32 v55, v2, 6, 2\n\t"
    "v_lshl_add_u32 v54, v55, 2, v54\n\t"
    "v_bfe_u32 v55, v2, 8, 2\n\t"
    "v_add_u32 v54, v54, v55\n\t"
    "v_mul_u32_u24 v54, 16, v54\n\t"
    "v_and_b32 v55, 12, v53\n\t"
    "v_add_u32 v54, v54, v55\n\t"
    "v_add_u32 v54, 0xf000, v54\n\t"
    "s_mov_b64 s[52:53], %[x]\n\t"
    "s_and_b32 s53, s53, 0xffff\n\t"
    "s_mov_b32 s54, 0x400\n\t"
    "s_mov_b32 s55, 0x20000\n\t"
    "s_cmp_eq_u32 s63, 0\n\t"
    "s_cselect_b64 s[52:53], s[52:53], s[48:49]\n\t"
    "s_cselect_b32 s54, s54, s50\n\t"
    "s_cselect_b64 s[82:83], -1, 0\n\t"
    "v_cndmask_b32 v54, v54, v53, s[82:83]\n\t"
    "buffer_load_dword v0, v54, s[52:55], 0 offen sc1\n\t"
    "v_and_b32 v29, 15, v3\n\t"
    "v_lshlrev_b32 v29, 4, v29\n\t"
    "v_lshrrev_b32 v54, 5, v3\n\t"
    "v_lshlrev_b32 v54, 4, v54\n\t"
    "v_add_u32 v54, 0x100, v54\n\t"
    "v_cmp_gt_u32 vcc, 16, v3\n\t"
    "s_lshl_b32 s79, s60, 2\n\t"
    "s_add_u32 s79, s79, 0xd980\n\t"
    "v_cndmask_b32 v28, v54, v29, vcc\n\t"
    "v_add_u32 v28, s79, v28\n\t"
    "v_add_u32 v29, 0xd980, v29\n\t"
    "v_mov_b32 v30, 0xd980\n\t"
    "ds_read_b128 v[24:27], v29 offset:960\n\t"
    "s_waitcnt lgkmcnt(0)\n\t"
    "v_add_f32 v24, v24, v25\n\t"
    "v_add_f32 v26, v26, v27\n\t"
    "v_add_f32 v52, v24, v26\n\t"
    "s_add_u32 s73, s62, 0xa000\n\t"
    "s_add_u32 s74, s73, 0x1000\n\t"
    "s_add_u32 s75, s73, 0x2000\n\t"
    "s_add_u32 s76, s73, 0x3000\n\t"
    "v_add_u32 v33, 0xc000, v53\n\t"
    "s_lshl_b32 s77, s60, 8\n\t"
    "s_add_u32 s77, s77, 0xc000\n\t"
    "v_and_b32 v55, 3, v3\n\t"
    "v_lshlrev_b32 v34, 6, v55\n\t"
    "v_lshrrev_b32 v55, 4, v3\n\t"
    "v_lshl_add_u32 v34, v55, 4, v34\n\t"
    "v_add_u32 v34, s77, v34\n\t"
    "s_cmp_eq_u32 s60, 0\n\t"
    "s_cselect_b32 s82, 0x55, 0\n\t"
    "s_mov_b32 s83, 0\n\t"
    "v_lshlrev_b32 v35, 1, v3\n\t"
    "v_mov_b32 v55, 0x7fffff00\n\t"
    "v_cndmask_b32 v35, v55, v35, s[82:83]\n\t"
    "s_lshr_b32 s77, s61, 6\n\t"
    "s_lshl_b32 s77, s77, 6\n\t"
    "s_bfe_u32 s78, s61, 0x20003\n\t"
    "s_lshl_b32 s78, s78, 4\n\t"
    "s_add_u32 s77, s77, s78\n\t"
    "s_and_b32 s78, s61, 7\n\t"
    "s_lshl_b32 s78, s78, 1\n\t"
    "s_add_u32 s77, s77, s78\n\t"
    "s_bfe_u32 s78, s61, 0x10005\n\t"
    "s_add_u32 s77, s77, s78\n\t"
    "s_mul_i32 s77, s77, 16\n\t"
    "s_add_u32 s77, s77, s62\n\t"
    "s_add_u32 s77, s77, 0xe000\n\t"
    "s_cmp_lt_u32 s61, 0xe0\n\t"
    "s_cselect_b32 s77, s77, 0x7fffff00\n\t"
    "s_sleep 12\n\t"
    "s_mov_b32 s58, 0x100000\n\t"
    ".Lp42_%=:\n\t"
    "buffer_load_dwordx2 v[4:5], v1, s[48:51], s73 offen sc1\n\t"
    "buffer_load_dwordx2 v[6:7], v1, s[48:51], s73 offen offset:2048 sc1\n\t"
    "buffer_load_dwordx2 v[8:9], v1, s[48:51], s74 offen sc1\n\t"
    "buffer_load_dwordx2 v[10:11], v1, s[48:51], s74 offen offset:2048 sc1\n\t"
    "buffer_load_dwordx2 v[12:13], v1, s[48:51], s75 offen sc1\n\t"
    "buffer_load_dwordx2 v[14:15], v1, s[48:51], s75 offen offset:2048 sc1\n\t"
    "buffer_load_dwordx2 v[16:17], v1, s[48:51], s76 offen sc1\n\t"
    "buffer_load_dwordx2 v[18:19], v1, s[48:51], s76 offen offset:2048 sc1\n\t"
    "s_waitcnt vmcnt(0)\n\t"
    ".Lp43_%=:\n\t"
    "v_max3_u32 v53, v4, v5, v6\n\t"
    "v_max3_u32 v53, v53, v7, v8\n\t"
    "v_max3_u32 v53, v53, v9, v10\n\t"
    "v_max3_u32 v53, v53, v11, v12\n\t"
    "v_max3_u32 v53, v53, v13, v14\n\t"
    "v_max3_u32 v53, v53, v15, v16\n\t"
    "v_max3_u32 v53, v53, v17, v18\n\t"
    "v_max_u32 v53, v53, v19\n\t"
    "v_cmp_eq_u32 vcc, -1, v53\n\t"
    "s_cbranch_vccz .Lp44_%=\n\t"
    "s_mov_b64 exec, vcc\n\t"
    "s_sub_u32 s58, s58, 1\n\t"
    "s_cbranch_scc1 .Lp44_%=\n\t"
    "s_sleep 3\n\t"
    "s_branch .Lp42_%=\n\t"
    ".Lp44_%=:\n\t"
    "s_mov_b64 exec, -1\n\t"
    "v_add_f32 v4, v4, v12\n\t"
    "v_add_f32 v6, v6, v14\n\t"
    "v_add_f32 v8, v8, v16\n\t"
    "v_add_f32 v10, v10, v18\n\t"
    "v_add_f32 v5, v5, v13\n\t"
    "v_add_f32 v7, v7, v15\n\t"
    "v_add_f32 v9, v9, v17\n\t"
    "v_add_f32 v11, v11, v19\n\t"
    "v_lshlrev_b32 v54, 16, v0\n\t"
    "v_and_b32 v55, 0xffff0000, v0\n\t"
    "v_add_f32 v4, v4, v8\n\t"
    "v_add_f32 v6, v6, v10\n\t"
    "v_add_f32 v5, v5, v9\n\t"
    "v_add_f32 v7, v7, v11\n\t"
    "v_add_f32 v4, v4, v6\n\t"
    "v_add_f32 v5, v5, v7\n\t"
    "v_cvt_pk_bf16_f32 v53, v4, v5\n\t"
    "v_lshlrev_b32 v32, 16, v53\n\t"
    "v_and_b32 v53, 0xffff0000, v53\n\t"
    "v_add_f32 v54, v54, v32\n\t"
    "v_add_f32 v55, v55, v53\n\t"
    "v_cvt_pk_bf16_f32 v53, v54, v55\n\t"
    "ds_write_b32 v33, v53\n\t"
    "ds_read_b128 v[20:23], v34\n\t"
    "s_waitcnt lgkmcnt(0)\n\t"
    "v_mfma_f32_16x16x32_bf16 v[4:7], v[20:23], v[36:39], 0\n\t"
    "v_dot2_f32_bf16 v55, v20, s68, 0\n\t"
    "v_dot2_f32_bf16 v32, v20, v20, 0\n\t"
    "v_mfma_f32_16x16x32_bf16 v[8:11], v[20:23], v[40:43], 0\n\t"
    "v_dot2_f32_bf16 v55, v21, s68, v55\n\t"
    "v_dot2_f32_bf16 v32, v21, v21, v32\n\t"
    "v_mfma_f32_16x16x32_bf16 v[12:15], v[20:23], v[44:47], 0\n\t"
    "v_dot2_f32_bf16 v55, v22, s68, v55\n\t"
    "v_dot2_f32_bf16 v32, v22, v22, v32\n\t"
    "v_mfma_f32_16x16x32_bf16 v[16:19], v[20:23], v[48:51], 0\n\t"
    "v_dot2_f32_bf16 v55, v23, s68, v55\n\t"
    "v_dot2_f32_bf16 v32, v23, v23, v32\n\t"
    "s_nop 2\n\t"
    "v_permlane32_swap_b32 v55, v32\n\t"
    "v_add_f32 v55, v55, v32\n\t"
    "v_mov_b32 v32, v55\n\t"
    "v_add_f32 v53, v4, v9\n\t"
    "s_nop 0\n\t"
    "v_permlane16_swap_b32 v55, v32\n\t"
    "v_add_f32 v55, v55, v32\n\t"
    "v_add_f32 v54, v14, v19\n\t"
    "s_nop 0\n\t"
    "v_add_f32_dpp v55, v55, v55 quad_perm:[1,0,3,2] row_mask:0xf bank_mask:0xf\n\t"
    "v_add_f32 v53, v53, v54\n\t"
    "s_nop 0\n\t"
    "v_add_f32_dpp v55, v55, v55 quad_perm:[2,3,0,1] row_mask:0xf bank_mask:0xf\n\t"
    "v_cndmask_b32 v53, v55, v53, s[36:37]\n\t"
    "ds_write_b32 v28, v53\n\t"
    "s_waitcnt lgkmcnt(0)\n\t"
    "s_barrier\n\t"
    "ds_read_b128 v[8:11], v30 offset:256\n\t"
    "ds_read_b128 v[12:15], v30 offset:272\n\t"
    "ds_read_b128 v[4:7], v29\n\t"
    "s_waitcnt lgkmcnt(1)\n\t"
    "v_add_f32 v8, v8, v9\n\t"
    "v_add_f32 v10, v10, v11\n\t"
    "v_add_f32 v12, v12, v13\n\t"
    "v_add_f32 v14, v14, v15\n\t"
    "v_add_f32 v55, v8, v10\n\t"
    "v_add_f32 v32, v12, v14\n\t"
    "v_mul_f32 v55, 0x3b000000, v55\n\t"
    "v_mul_f32 v32, 0x3b000000, v32\n\t"
    "v_fma_f32 v32, -v55, v55, v32\n\t"
    "v_max_f32 v32, 0, v32\n\t"
    "v_add_f32 v32, 0x3727c5ac, v32\n\t"
    "v_rsq_f32 v32, v32\n\t"
    "s_waitcnt lgkmcnt(0)\n\t"
    "v_add_f32 v4, v4, v5\n\t"
    "v_add_f32 v6, v6, v7\n\t"
    "v_add_f32 v53, v4, v6\n\t"
    "v_fma_f32 v53, -v55, v52, v53\n\t"
    "v_mul_f32 v53, v53, v32\n\t"
    "v_cvt_pk_bf16_f32 v53, v53, v53\n\t"
    "v_lshlrev_b32 v53, 16, v53\n\t"
    "v_mul_f32 v54, 0xbfb8aa3b, v53\n\t"
    "v_exp_f32 v54, v54\n\t"
    "s_nop 0\n\t"
    "v_add_f32 v54, 1.0, v54\n\t"
    "v_rcp_f32 v54, v54\n\t"
    "s_nop 0\n\t"
    "v_mul_f32 v54, v53, v54\n\t"
    "v_cvt_pk_bf16_f32 v54, v54, v54\n\t"
    "v_lshlrev_b32 v54, 16, v54\n\t"
    "v_mul_f32_dpp v54, v53, v54 row_ror:8 row_mask:0xf bank_mask:0xf\n\t"
    "s_nop 1\n\t"
    "v_mov_b32_dpp v55, v54 quad_perm:[1,0,3,2] row_mask:0xf bank_mask:0xf\n\t"
    "v_cvt_pk_bf16_f32 v54, v54, v55\n\t"
    "v_min_u32 v54, -2, v54\n\t"
    "buffer_store_dword v54, v35, s[48:51], s77 offen sc1\n\t"
    "s_cmp_eq_u32 s71, 0\n\t"
    "s_nop 0\n\t"
    "s_cbranch_scc1 .Lnox_%=\n\t"
    "s_add_u32 s79, s69, 0\n\t"
    "buffer_load_dwordx4 v[4:7], v2, s[48:51], s79 offen sc1\n\t"
    "s_add_u32 s79, s69, 0x1000\n\t"
    "buffer_load_dwordx4 v[8:11], v2, s[48:51], s79 offen sc1\n\t"
    "s_add_u32 s79, s69, 0x2000\n\t"
    "buffer_load_dwordx4 v[12:15], v2, s[48:51], s79 offen sc1\n\t"
    "s_add_u32 s79, s69, 0x3000\n\t"
    "buffer_load_dwordx4 v[16:19], v2, s[48:51], s79 offen sc1\n\t"
    "s_add_u32 s79, s69, 0x4000\n\t"
    "buffer_load_dwordx4 v[20:23], v2, s[48:51], s79 offen sc1\n\t"
    "s_add_u32 s79, s69, 0x5000\n\t"
    "buffer_load_dwordx4 v[52:55], v2, s[48:51], s79 offen sc1\n\t"
    "s_add_u32 s79, s69, 0x6000\n\t"
    "buffer_load_dwordx4 v[24:27], v2, s[48:51], s79 offen sc1\n\t"
    "s_lshl_b32 s73, s70, 2\n\t"
    "s_add_u32 s73, s73, s60\n\t"
    "s_lshl_b32 s73, s73, 2\n\t"
    "s_add_u32 s73, s73, 0xc000\n\t"
    "v_mov_b32 v33, s73\n\t"
    "ds_read_b32 v32, v33\n\t"
    "v_mul_u32_u24 v0, 16, v3\n\t"
    "v_and_b32 v33, 1, v3\n\t"
    "v_cmp_eq_u32 s[82:83], 1, v33\n\t"
    "v_mov_b32 v33, 0x7fffff00\n\t"
    "s_nop 0\n\t"
    "v_cndmask_b32 v1, v0, v33, s[82:83]\n\t"
    "s_add_u32 s73, s62, 0xe000\n\t"
    "s_add_u32 s74, s73, 0x800\n\t"
    "s_mov_b32 s75, 0xc0c0303\n\t"
    "s_mov_b32 s76, 0xc0c03030\n\t"
    "s_lshl_b32 s77, s60, 2\n\t"
    "s_add_u32 s77, s77, 0xdc00\n\t"
    "v_mov_b32 v30, s77\n\t"
    "v_mov_b32 v31, 0xdc00\n\t"
    "v_cmp_eq_u32 vcc, 0, v2\n\t"
    "v_mov_b32 v35, 0x7fffff00\n\t"
    "s_nop 0\n\t"
    "v_cndmask_b32 v35, v35, v2, vcc\n\t"
    "s_and_b32 s77, s70, 48\n\t"
    "s_and_b32 s78, s70, 3\n\t"
    "s_lshl_b32 s78, s78, 2\n\t"
    "s_add_u32 s77, s77, s78\n\t"
    "s_bfe_u32 s78, s70, 0x20002\n\t"
    "s_add_u32 s77, s77, s78\n\t"
    "s_mul_i32 s77, s77, 16\n\t"
    "s_add_u32 s77, s77, 0xf000\n\t"
    "s_sleep 14\n\t"
    "s_mov_b32 s58, 0x100000\n\t"
    ".Lp45_%=:\n\t"
    "buffer_load_dwordx4 v[36:39], v0, s[48:51], s73 offen sc1\n\t"
    "buffer_load_dwordx4 v[40:43], v0, s[48:51], s73 offen offset:1024 sc1\n\t"
    "buffer_load_dwordx4 v[44:47], v0, s[48:51], s74 offen sc1\n\t"
    "buffer_load_dwordx4 v[48:51], v1, s[48:51], s74 offen offset:1024 sc1\n\t"
    "s_waitcnt vmcnt(0)\n\t"
    ".Lp46_%=:\n\t"
    "v_max3_u32 v33, v36, v37, v38\n\t"
    "v_max3_u32 v33, v33, v39, v40\n\t"
    "v_max3_u32 v33, v33, v41, v42\n\t"
    "v_max3_u32 v33, v33, v43, v44\n\t"
    "v_max3_u32 v33, v33, v45, v46\n\t"
    "v_max3_u32 v33, v33, v47, v48\n\t"
    "v_max3_u32 v33, v33, v49, v50\n\t"
    "v_max_u32 v33, v33, v51\n\t"
    "v_cmp_eq_u32 vcc, -1, v33\n\t"
    "s_cbranch_vccz .Lp47_%=\n\t"
    "s_mov_b64 exec, vcc\n\t"
    "s_sub_u32 s58, s58, 1\n\t"
    "s_cbranch_scc1 .Lp47_%=\n\t"
    "s_sleep 3\n\t"
    "s_branch .Lp45_%=\n\t"
    ".Lp47_%=:\n\t"
    "s_mov_b64 exec, -1\n\t"
    "s_mov_b32 s52, s75\n\t"
    "s_mov_b32 s53, s76\n\t"
    "v_mov_b32 v34, v3\n\t"
    "v_mfma_f32_16x16x32_bf16 v[0:3], v[36:39], v[4:7], 0\n\t"
    "v_mfma_f32_16x16x32_bf16 v[0:3], v[40:43], v[12:15], v[0:3]\n\t"
    "v_mfma_f32_16x16x32_bf16 v[4:7], v[36:39], v[8:11], 0\n\t"
    "v_mfma_f32_16x16x32_bf16 v[0:3], v[44:47], v[20:23], v[0:3]\n\t"
    "v_mfma_f32_16x16x32_bf16 v[4:7], v[40:43], v[16:19], v[4:7]\n\t"
    "v_mfma_f32_16x16x32_bf16 v[0:3], v[48:51], v[24:27], v[0:3]\n\t"
    "v_mfma_f32_16x16x32_bf16 v[4:7], v[44:47], v[52:55], v[4:7]\n\t"
    "s_waitcnt lgkmcnt(0)\n\t"
    "v_lshlrev_b32 v10, 16, v32\n\t"
    "v_and_b32 v11, 0xffff0000, v32\n\t"
    "s_nop 4\n\t"
    "v_add_f32 v8, v0, v5\n\t"
    "v_add_f32 v9, v2, v7\n\t"
    "v_cndmask_b32 v8, v8, v9, s[82:83]\n\t"
    "v_cndmask_b32 v8, 0, v8, s[52:53]\n\t"
    "s_nop 1\n\t"
    "v_add_f32_dpp v8, v8, v8 quad_perm:[1,0,3,2] row_mask:0xf bank_mask:0xf\n\t"
    "s_nop 1\n\t"
    "v_add_f32_dpp v8, v8, v8 quad_perm:[2,3,0,1] row_mask:0xf bank_mask:0xf\n\t"
    "s_nop 1\n\t"
    "v_add_f32_dpp v8, v8, v8 row_half_mirror row_mask:0xf bank_mask:0xf\n\t"
    "v_mov_b32 v9, v8\n\t"
    "s_nop 1\n\t"
    "v_permlane16_swap_b32 v8, v9\n\t"
    "v_add_f32 v8, v8, v9\n\t"
    "v_mov_b32 v9, v8\n\t"
    "s_nop 1\n\t"
    "v_permlane32_swap_b32 v8, v9\n\t"
    "v_add_f32 v8, v8, v9\n\t"
    "s_nop 1\n\t"
    "v_mov_b32_dpp v9, v8 row_newbcast:8 row_mask:0xf bank_mask:0xf\n\t"
    "v_mov_b32_dpp v12, v8 row_newbcast:0 row_mask:0xf bank_mask:0xf\n\t"
    "v_cvt_pk_bf16_f32 v8, v12, v9\n\t"
    "v_lshlrev_b32 v9, 16, v8\n\t"
    "v_and_b32 v8, 0xffff0000, v8\n\t"
    "v_add_f32 v10, v10, v9\n\t"
    "v_add_f32 v11, v11, v8\n\t"
    "v_cvt_pk_bf16_f32 v8, v10, v11\n\t"
    "v_mov_b32 v3, v34\n\t"
    "s_lshl_b32 s72, s60, 10\n\t"
    "v_lshl_add_u32 v2, v3, 4, s72\n\t"
    "s_cmp_eq_u32 s63, 0\n\t"
    "s_cbranch_scc0 .Lfinal_%=\n\t"
    "v_min_u32 v8, -2, v8\n\t"
    "ds_write_b32 v30, v8\n\t"
    "s_waitcnt lgkmcnt(0)\n\t"
    "s_barrier\n\t"
    "ds_read_b128 v[12:15], v31\n\t"
    "s_waitcnt lgkmcnt(0)\n\t"
    "buffer_store_dwordx4 v[12:15], v35, s[48:51], s77 offen sc1\n\t"
    "s_nop 1\n\t"
    ".Lnox_%=:\n\t"
    "s_cmp_eq_u32 s63, 0\n\t"
    "s_cbranch_scc0 .Lend_%=\n\t"
    "ds_read_b128 v[36:39], v2\n\t"
    "ds_read_b128 v[40:43], v2 offset:4096\n\t"
    "ds_read_b128 v[44:47], v2 offset:8192\n\t"
    "ds_read_b128 v[48:51], v2 offset:12288\n\t"
    "v_lshrrev_b32 v0, 4, v3\n\t"
    "v_lshlrev_b32 v0, 2, v0\n\t"
    "v_and_b32 v54, 3, v3\n\t"
    "v_add_u32 v0, v0, v54\n\t"
    "v_mul_u32_u24 v0, 16, v0\n\t"
    "s_mul_i32 s73, s60, 0x100\n\t"
    "s_add_u32 s73, s73, 0xf000\n\t"
    "v_and_b32 v34, 15, v3\n\t"
    "v_lshlrev_b32 v34, 4, v34\n\t"
    "v_lshrrev_b32 v54, 5, v3\n\t"
    "v_lshlrev_b32 v54, 4, v54\n\t"
    "v_add_u32 v54, 0x100, v54\n\t"
    "v_cmp_gt_u32 vcc, 16, v3\n\t"
    "s_lshl_b32 s79, s60, 2\n\t"
    "s_add_u32 s79, s79, 0xdac0\n\t"
    "v_cndmask_b32 v33, v54, v34, vcc\n\t"
    "v_add_u32 v33, s79, v33\n\t"
    "v_add_u32 v34, 0xdac0, v34\n\t"
    "v_mov_b32 v1, 0xdac0\n\t"
    "s_cmp_eq_u32 s60, 0\n\t"
    "s_cselect_b32 s82, 0x5555, 0\n\t"
    "s_mov_b32 s83, 0\n\t"
    "v_lshrrev_b32 v35, 3, v3\n\t"
    "v_lshlrev_b32 v35, 6, v35\n\t"
    "v_and_b32 v54, 6, v3\n\t"
    "v_lshl_add_u32 v35, v54, 1, v35\n\t"
    "v_mov_b32 v54, 0x7fffff00\n\t"
    "v_cndmask_b32 v35, v54, v35, s[82:83]\n\t"
    "s_lshr_b32 s77, s61, 5\n\t"
    "s_lshl_b32 s77, s77, 10\n\t"
    "s_and_b32 s78, s61, 7\n\t"
    "s_lshl_b32 s78, s78, 7\n\t"
    "s_add_u32 s77, s77, s78\n\t"
    "s_bfe_u32 s78, s61, 0x20003\n\t"
    "s_lshl_b32 s78, s78, 4\n\t"
    "s_add_u32 s77, s77, s78\n\t"
    "s_add_u32 s77, s77, 0x50000\n\t"
    "ds_read_b128 v[16:19], v34 offset:384\n\t"
    "s_waitcnt lgkmcnt(0)\n\t"
    "v_add_f32 v16, v16, v17\n\t"
    "v_add_f32 v18, v18, v19\n\t"
    "v_add_f32 v52, v16, v18\n\t"
    "v_bfe_u32 v54, v3, 2, 2\n\t"
    "v_lshlrev_b32 v12, 8, v54\n\t"
    "v_lshrrev_b32 v54, 4, v3\n\t"
    "v_lshl_add_u32 v12, v54, 6, v12\n\t"
    "v_and_b32 v54, 3, v3\n\t"
    "v_lshl_add_u32 v12, v54, 4, v12\n\t"
    "s_add_u32 s32, s34, 0x18000\n\t"
    "s_and_b32 s32, s32, 0x7ffff\n\t"
    "s_add_u32 s32, s32, s56\n\t"
    "s_add_u32 s32, s32, 0x4000\n\t"
    "s_lshl_b32 s33, s60, 10\n\t"
    "v_mov_b32 v8, s38\n\t"
    "v_mov_b32 v9, s39\n\t"
    "v_not_b32 v10, v8\n\t"
    "v_not_b32 v11, v9\n\t"
    "v_cmp_eq_u32 vcc, 0, v2\n\t"
    "v_mov_b32 v55, 0x7fffff00\n\t"
    "s_nop 0\n\t"
    "v_cndmask_b32 v55, v55, v2, vcc\n\t"
    "s_lshl_b32 s74, s59, 4\n\t"
    "s_add_u32 s74, s74, 0xa2000\n\t"
    "buffer_store_dwordx2 v[10:11], v55, s[48:51], s74 offen sc1\n\t"
    "s_cmp_eq_u32 s59, 0\n\t"
    "s_mov_b32 s74, 0xa3000\n\t"
    "s_cmp_eq_u32 s59, 0\n\t"
    "s_cselect_b32 s74, s74, 0x7fffff00\n\t"
    "buffer_store_dwordx2 v[8:9], v55, s[48:51], s74 offen sc1\n\t"
    "s_cmp_eq_u32 s71, 0\n\t"
    "s_cbranch_scc0 .Lq1_%=\n\t"
    "s_sleep 32\n\t"
    ".Lq1_%=:\n\t"
    "s_sleep 14\n\t"
    "s_mov_b32 s58, 0x100000\n\t"
    ".Lp48_%=:\n\t"
    "buffer_load_dwordx4 v[4:7], v0, s[48:51], s73 offen sc1\n\t"
    "s_waitcnt vmcnt(0)\n\t"
    ".Lp49_%=:\n\t"
    "v_max3_u32 v53, v4, v5, v6\n\t"
    "v_max_u32 v53, v53, v7\n\t"
    "v_cmp_eq_u32 vcc, -1, v53\n\t"
    "s_cbranch_vccz .Lp50_%=\n\t"
    "s_mov_b64 exec, vcc\n\t"
    "s_sub_u32 s58, s58, 1\n\t"
    "s_cbranch_scc1 .Lp50_%=\n\t"
    "s_sleep 3\n\t"
    "s_branch .Lp48_%=\n\t"
    ".Lp50_%=:\n\t"
    "s_mov_b64 exec, -1\n\t"
    "v_mfma_f32_16x16x32_bf16 v[16:19], v[4:7], v[36:39], 0\n\t"
    "v_dot2_f32_bf16 v55, v4, s68, 0\n\t"
    "v_dot2_f32_bf16 v32, v4, v4, 0\n\t"
    "v_mfma_f32_16x16x32_bf16 v[20:23], v[4:7], v[40:43], 0\n\t"
    "v_dot2_f32_bf16 v55, v5, s68, v55\n\t"
    "v_dot2_f32_bf16 v32, v5, v5, v32\n\t"
    "v_mfma_f32_16x16x32_bf16 v[24:27], v[4:7], v[44:47], 0\n\t"
    "v_dot2_f32_bf16 v55, v6, s68, v55\n\t"
    "v_dot2_f32_bf16 v32, v6, v6, v32\n\t"
    "v_mfma_f32_16x16x32_bf16 v[28:31], v[4:7], v[48:51], 0\n\t"
    "v_dot2_f32_bf16 v55, v7, s68, v55\n\t"
    "v_dot2_f32_bf16 v32, v7, v7, v32\n\t"
    "s_nop 2\n\t"
    "v_permlane32_swap_b32 v55, v32\n\t"
    "v_add_f32 v55, v55, v32\n\t"
    "v_mov_b32 v32, v55\n\t"
    "v_add_f32 v53, v16, v21\n\t"
    "s_nop 0\n\t"
    "v_permlane16_swap_b32 v55, v32\n\t"
    "v_add_f32 v55, v55, v32\n\t"
    "v_add_f32 v54, v26, v31\n\t"
    "s_nop 0\n\t"
    "v_add_f32_dpp v55, v55, v55 quad_perm:[1,0,3,2] row_mask:0xf bank_mask:0xf\n\t"
    "v_add_f32 v53, v53, v54\n\t"
    "s_nop 0\n\t"
    "v_add_f32_dpp v55, v55, v55 quad_perm:[2,3,0,1] row_mask:0xf bank_mask:0xf\n\t"
    "v_cndmask_b32 v53, v55, v53, s[36:37]\n\t"
    "ds_write_b32 v33, v53\n\t"
    "s_waitcnt lgkmcnt(0)\n\t"
    "s_barrier\n\t"
    "ds_read_b128 v[20:23], v1 offset:256\n\t"
    "ds_read_b128 v[24:27], v1 offset:272\n\t"
    "ds_read_b128 v[16:19], v34\n\t"
    "s_mov_b32 m0, s33\n\t"
    "s_add_u32 s33, s33, 0x1000\n\t"
    "s_xor_b32 s57, s32, s80\n\t"
    "buffer_load_dwordx4 v12, s[40:43], s57 offen nt lds\n\t"
    "s_add_u32 s32, s32, 0x1000\n\t"
    "s_mov_b32 m0, s33\n\t"
    "s_add_u32 s33, s33, 0x1000\n\t"
    "s_xor_b32 s57, s32, s80\n\t"
    "buffer_load_dwordx4 v12, s[40:43], s57 offen nt lds\n\t"
    "s_add_u32 s32, s32, 0x1000\n\t"
    "s_mov_b32 m0, s33\n\t"
    "s_add_u32 s33, s33, 0x1000\n\t"
    "s_xor_b32 s57, s32, s80\n\t"
    "buffer_load_dwordx4 v12, s[40:43], s57 offen nt lds\n\t"
    "s_add_u32 s32, s32, 0x1000\n\t"
    "s_mov_b32 m0, s33\n\t"
    "s_add_u32 s33, s33, 0x1000\n\t"
    "s_xor_b32 s57, s32, s80\n\t"
    "buffer_load_dwordx4 v12, s[40:43], s57 offen nt lds\n\t"
    "s_add_u32 s32, s32, 0x1000\n\t"
    "s_waitcnt lgkmcnt(1)\n\t"
    "v_add_f32 v20, v20, v21\n\t"
    "v_add_f32 v22, v22, v23\n\t"
    "v_add_f32 v24, v24, v25\n\t"
    "v_add_f32 v26, v26, v27\n\t"
    "v_add_f32 v55, v20, v22\n\t"
    "v_add_f32 v32, v24, v26\n\t"
    "v_mul_f32 v55, 0x3b000000, v55\n\t"
    "v_mul_f32 v32, 0x3b000000, v32\n\t"
    "v_fma_f32 v32, -v55, v55, v32\n\t"
    "v_max_f32 v32, 0, v32\n\t"
    "v_add_f32 v32, 0x3727c5ac, v32\n\t"
    "v_rsq_f32 v32, v32\n\t"
    "s_waitcnt lgkmcnt(0)\n\t"
    "v_add_f32 v16, v16, v17\n\t"
    "v_add_f32 v18, v18, v19\n\t"
    "v_add_f32 v53, v16, v18\n\t"
    "v_fma_f32 v53, -v55, v52, v53\n\t"
    "v_mul_f32 v53, v53, v32\n\t"
    "s_nop 1\n\t"
    "v_mov_b32_dpp v54, v53 quad_perm:[1,0,3,2] row_mask:0xf bank_mask:0xf\n\t"
    "v_cvt_pk_bf16_f32 v53, v53, v54\n\t"
    "v_min_u32 v53, -2, v53\n\t"
    "buffer_store_dword v53, v35, s[48:51], s77 offen sc1\n\t"
    "s_mov_b32 s63, 1\n\t"
    "s_mov_b32 s62, 0x50000\n\t"
    "s_branch .Lpass2_%=\n\t"
    ".Lfinal_%=:\n\t"
    "s_mov_b64 s[52:53], %[out]\n\t"
    "s_and_b32 s53, s53, 0xffff\n\t"
    "s_mov_b32 s54, 0x400\n\t"
    "s_mov_b32 s55, 0x20000\n\t"
    "v_cmp_eq_u32 vcc, 0, v3\n\t"
    "v_mov_b32 v35, 0x7fffff00\n\t"
    "s_nop 0\n\t"
    "v_cndmask_b32 v35, v35, v3, vcc\n\t"
    "s_lshl_b32 s73, s70, 2\n\t"
    "s_add_u32 s73, s73, s60\n\t"
    "s_lshl_b32 s73, s73, 2\n\t"
    "buffer_store_dword v8, v35, s[52:55], s73 offen\n\t"
    ".Lend_%=:\n\t"
    : [tid] "+{v0}"(tid)
    : [bid] "s"(blockIdx.x), [x] "s"(x), [kc] "s"(kc), [vc] "s"(vc), [wq] "s"(wq), [wo] "s"(wo), [w1] "s"(w1), [w3] "s"(w3), [w2] "s"(w2), [out] "s"(out), [ws] "s"(ws)
    : "memory", "scc", "vcc", "m0", "s32", "s33", "s34", "s35", "s36", "s37", "s38", "s39", "s40", "s41", "s42", "s43", "s44", "s45", "s46", "s47", "s48", "s49", "s50", "s51", "s52", "s53", "s54", "s55", "s56", "s57", "s58", "s59", "s60", "s61", "s62", "s63", "s64", "s65", "s66", "s67", "s68", "s69", "s70", "s71", "s72", "s73", "s74", "s75", "s76", "s77", "s78", "s79", "s80", "s81", "s82", "s83", "s84", "s85", "s86", "s87", "s88", "s89", "s90", "s91", "s92", "s93", "s94", "s95", "s96", "s97", "s98", "s99", "s100", "s101", "v1", "v2", "v3", "v4", "v5", "v6", "v7", "v8", "v9", "v10", "v11", "v12", "v13", "v14", "v15", "v16", "v17", "v18", "v19", "v20", "v21", "v22", "v23", "v24", "v25", "v26", "v27", "v28", "v29", "v30", "v31", "v32", "v33", "v34", "v35", "v36", "v37", "v38", "v39", "v40", "v41", "v42", "v43", "v44", "v45", "v46", "v47", "v48", "v49", "v50", "v51", "v52", "v53", "v54", "v55", "v56", "v57", "v58", "v59", "v60", "v61", "v62", "v63", "v64", "v65", "v66", "v67", "v68", "v69", "v70", "v71", "v72", "v73", "v74", "v75", "v76", "v77", "v78", "v79", "v80", "v81", "v82", "v83", "v84", "v85", "v86", "v87", "v88", "v89", "v90", "v91", "v92", "v93", "v94", "v95", "v96", "v97", "v98", "v99", "v100", "v101", "v102", "v103", "v104", "v105", "v106", "v107", "v108", "v109", "v110", "v111", "v112", "v113", "v114", "v115", "v116", "v117", "v118", "v119", "v120", "v121", "v122", "v123", "v124", "v125", "v126", "v127", "v128", "v129", "v130", "v131", "v132", "v133", "v134", "v135", "v136", "v137", "v138", "v139", "v140", "v141", "v142", "v143", "v144", "v145", "v146", "v147", "v148", "v149", "v150", "v151", "v152", "v153", "v154", "v155", "v156", "v157", "v158", "v159", "v160", "v161", "v162", "v163", "v164", "v165", "v166", "v167", "v168", "v169", "v170", "v171", "v172", "v173", "v174", "v175", "v176", "v177", "v178", "v179", "v180", "v181", "v182", "v183", "v184", "v185", "v186", "v187", "v188", "v189", "v190", "v191", "v192", "v193", "v194", "v195", "v196", "v197", "v198", "v199", "v200", "v201", "v202", "v203", "v204", "v205", "v206", "v207", "v208", "v209", "v210", "v211", "v212", "v213", "v214", "v215", "v216", "v217", "v218", "v219", "v220", "v221", "v222", "v223", "v224", "v225", "v226", "v227", "v228", "v229", "v230", "v231", "v232", "v233", "v234", "v235", "v236", "v237", "v238", "v239", "v240", "v241", "v242", "v243", "v244", "v245", "v246", "v247", "v248", "v249", "v250", "v251", "v252", "v253", "v254", "v255", "a0", "a1", "a2", "a3", "a4", "a5", "a6", "a7", "a8", "a9", "a10", "a11", "a12", "a13", "a14", "a15", "a16", "a17", "a18", "a19", "a20", "a21", "a22", "a23", "a24", "a25", "a26", "a27", "a28", "a29", "a30", "a31", "a32", "a33", "a34", "a35", "a36", "a37", "a38", "a39", "a40", "a41", "a42", "a43", "a44", "a45", "a46", "a47", "a48", "a49", "a50", "a51", "a52", "a53", "a54", "a55", "a56", "a57", "a58", "a59", "a60", "a61", "a62", "a63", "a64", "a65", "a66", "a67", "a68", "a69", "a70", "a71", "a72", "a73", "a74", "a75", "a76", "a77", "a78", "a79", "a80", "a81", "a82", "a83", "a84", "a85", "a86", "a87", "a88", "a89", "a90", "a91", "a92", "a93", "a94", "a95", "a96", "a97", "a98", "a99", "a100", "a101", "a102", "a103", "a104", "a105", "a106", "a107", "a108", "a109", "a110", "a111", "a112", "a113", "a114", "a115", "a116", "a117", "a118", "a119", "a120", "a121", "a122", "a123", "a124", "a125", "a126", "a127", "a128", "a129", "a130", "a131", "a132", "a133", "a134", "a135", "a136", "a137", "a138", "a139", "a140", "a141", "a142", "a143", "a144", "a145", "a146", "a147", "a148", "a149", "a150", "a151", "a152", "a153", "a154", "a155", "a156", "a157", "a158", "a159", "a160", "a161", "a162", "a163", "a164", "a165", "a166", "a167", "a168", "a169", "a170", "a171", "a172", "a173", "a174", "a175", "a176", "a177", "a178", "a179", "a180", "a181", "a182", "a183", "a184", "a185", "a186", "a187", "a188", "a189", "a190", "a191", "a192", "a193", "a194", "a195", "a196", "a197", "a198", "a199", "a200", "a201", "a202", "a203", "a204", "a205", "a206", "a207", "a208", "a209", "a210", "a211", "a212", "a213", "a214", "a215", "a216", "a217", "a218", "a219", "a220", "a221", "a222", "a223", "a224", "a225", "a226", "a227", "a228", "a229", "a230", "a231", "a232", "a233", "a234", "a235", "a236", "a237", "a238", "a239", "a240", "a241", "a242", "a243", "a244", "a245", "a246", "a247", "a248", "a249", "a250", "a251", "a252", "a253", "a254", "a255");
}

extern "C" void kernel_launch(void* const* d_in, const int* in_sizes, int n_in, void* d_out, int out_size, void* d_ws, size_t ws_size, hipStream_t stream) {
  (void)hipFuncSetAttribute((const void*)mega, hipFuncAttributeMaxDynamicSharedMemorySize, LDS_BYTES);

  mega<<<256, 256, LDS_BYTES, stream>>>(d_in[0], d_in[1], d_in[2], d_in[3], d_in[4], d_in[6], d_in[7], d_in[8], d_out, d_ws);
}
```
